```python
import torch

def _softmax(x, axis=-1):
    return torch.softmax(x, dim=axis)
def _gelu(x):
    return torch.nn.functional.gelu(x, approximate="tanh")
def _silu(x):
    return torch.nn.functional.silu(x)
def _sigmoid(x):
    return torch.sigmoid(x)
def _layernorm(x, axis=-1, eps=1e-5):
    m = x.mean(dim=axis, keepdim=True)
    v = x.var(dim=axis, keepdim=True, unbiased=False)
    return (x - m) / torch.sqrt(v + eps)
def _scatter_add(tgt, idx, src, axis=0):
    return tgt.clone().index_add_(axis, idx, src)
def _segment_sum(src, seg, n):
    out = torch.zeros((n,) + src.shape[1:], dtype=src.dtype)
    return out.index_add_(0, seg, src)
def _pad(x, pw):
    flat = tuple(v for pair in reversed(pw) for v in pair)
    return torch.nn.functional.pad(x, flat)

kernel_name = 'standalone__dense_activation_gelu__prompt'

def setup_inputs():

    _w = (lambda a: (lambda t: t.to(torch.bfloat16) if t.is_floating_point() else t)(torch.as_tensor(a)))
    B, S, D, F = 1, 2048, 1024, 3584
    return {
        "x": _w((lambda *s: torch.randn(*s, dtype=torch.bfloat16) * 0.05)(B, S, D)),
        "w1": _w((lambda *s: torch.randn(*s, dtype=torch.bfloat16) * 3.2)(D, F) * (D ** -0.5)),
        "b1": _w((lambda *s: torch.randn(*s, dtype=torch.bfloat16) * 3.2)(F) * 0.01),
        "w2": _w((lambda *s: torch.randn(*s, dtype=torch.bfloat16) * 0.05)(F, D) * (F ** -0.5)),
        "b2": _w((lambda *s: torch.randn(*s, dtype=torch.bfloat16) * 0.1)(D) * 0.01),
    }

def reference(x, w1, b1, w2, b2):
    h = torch.einsum("bsd,df->bsf", x, w1) + b1
    h = _gelu(h)
    return torch.einsum("bsf,fd->bsd", h, w2) + b2
```

```cpp
#include <hip/hip_runtime.h>
#include <hip/hip_bf16.h>
typedef short s16x4 __attribute__((ext_vector_type(4)));
typedef short s16x8 __attribute__((ext_vector_type(8)));
typedef __bf16 bf16x8 __attribute__((ext_vector_type(8)));
typedef __bf16 bf16x2 __attribute__((ext_vector_type(2)));
typedef float f32x2 __attribute__((ext_vector_type(2)));
typedef float f32x16 __attribute__((ext_vector_type(16)));
typedef unsigned u32x2 __attribute__((ext_vector_type(2)));
#define LAS __attribute__((address_space(3)))
#define DI __device__ __forceinline__
DI unsigned cvtpk(float lo, float hi) { f32x2 v = {lo, hi}; bf16x2 b = __builtin_convertvector(v, bf16x2); return __builtin_bit_cast(unsigned, b); }
DI float bf2f(unsigned short v) { return __uint_as_float(((unsigned)v) << 16); }
DI float gelu_t(float x) {
  const float c0 = -2.0f * 0.7978845608028654f * 1.4426950408889634f, c1 = c0 * 0.044715f;
  float z = x * (c1 * x * x + c0);
  float e = __builtin_amdgcn_exp2f(z);
  return x * __builtin_amdgcn_rcpf(1.0f + e);
}
template <bool GELU>
__global__ __launch_bounds__(256, 2) void gemm128(const unsigned short* __restrict__ A, const unsigned short* __restrict__ B,
                                                  const unsigned short* __restrict__ bias, unsigned short* __restrict__ C, int M, int N, int K) {
  extern __shared__ __attribute__((aligned(16))) char smem[];
  const int nN = N / 128, nM = M / 128, nwg = nN * nM;
  int wg = blockIdx.x; { int q = nwg / 8, x = wg % 8, o = wg / 8; wg = x * q + o; }
  const int GM = 4; int nig = GM * nN, gid = wg / nig, pm = gid * GM + (wg % nig) % GM, pn = (wg % nig) / GM;
  const int tid = threadIdx.x, lane = tid & 63, wid = __builtin_amdgcn_readfirstlane(tid >> 6), wr = wid >> 1, wc = wid & 1;
  const unsigned short* Ab = A + (size_t)pm * 128 * K; const unsigned short* Bb = B + pn * 128;
  int aoff[4], boff[4];
  for (int i = 0; i < 4; ++i) {
    int p = (wid * 4 + i) * 64 + lane;
    int r = p >> 3, s = p & 7; aoff[i] = r * K + ((s ^ ((r >> 1) & 7)) << 3);
    int kr = p >> 4, sl = p & 15; boff[i] = kr * N + ((sl ^ ((kr & 3) << 2)) << 3);
  }
  auto stage = [&](int buf, int kt) {
    for (int i = 0; i < 4; ++i) {
      __builtin_amdgcn_global_load_lds((const void*)(Ab + aoff[i] + kt * 64), (LAS void*)(smem + buf * 32768 + (wid * 4 + i) * 1024), 16, 0, 0);
      __builtin_amdgcn_global_load_lds((const void*)(Bb + boff[i] + (size_t)kt * 64 * N), (LAS void*)(smem + buf * 32768 + 16384 + (wid * 4 + i) * 1024), 16, 0, 0);
    }
  };
  f32x16 acc[2][2] = {};
  const int nt = K / 64;
  const int r = lane & 31, h = lane >> 5, blk = (lane >> 4) & 1, q = (lane & 15) >> 2, p = lane & 3;
  stage(0, 0);
  for (int t = 0; t < nt; ++t) {
    __syncthreads();
    if (t + 1 < nt) stage((t + 1) & 1, t + 1);
    const char* sa = smem + (t & 1) * 32768; const char* sb = sa + 16384;
#pragma unroll
    for (int ks = 0; ks < 4; ++ks) {
      bf16x8 af[2], bfr[2];
#pragma unroll
      for (int m = 0; m < 2; ++m) { int row = wr * 64 + m * 32 + r; af[m] = *(const bf16x8*)(sa + row * 128 + (((2 * ks + h) ^ ((row >> 1) & 7)) << 4)); }
#pragma unroll
      for (int n = 0; n < 2; ++n) {
        s16x4 v[2];
#pragma unroll
        for (int tt = 0; tt < 2; ++tt) { int kr = 16 * ks + 8 * h + 4 * tt + q; int ch = (wc * 64 + n * 32 + 16 * blk) / 8 + (p >> 1);
          v[tt] = __builtin_amdgcn_ds_read_tr16_b64_v4i16((LAS s16x4*)(sb + kr * 256 + ((ch ^ ((kr & 3) << 2)) << 4) + 8 * (p & 1))); }
        bfr[n] = __builtin_bit_cast(bf16x8, __builtin_shufflevector(v[0], v[1], 0, 1, 2, 3, 4, 5, 6, 7));
      }
#pragma unroll
      for (int m = 0; m < 2; ++m)
#pragma unroll
        for (int n = 0; n < 2; ++n) acc[m][n] = __builtin_amdgcn_mfma_f32_32x32x16_bf16(bfr[n], af[m], acc[m][n], 0, 0, 0);
    }
  }
#pragma unroll
  for (int m = 0; m < 2; ++m)
#pragma unroll
    for (int n = 0; n < 2; ++n) {
      int row = pm * 128 + wr * 64 + m * 32 + r;
#pragma unroll
      for (int g = 0; g < 4; ++g) {
        int col = pn * 128 + wc * 64 + n * 32 + 8 * g + 4 * h;
        float v[4];
        s16x4 bv = *(const s16x4*)(bias + col);
#pragma unroll
        for (int j = 0; j < 4; ++j) { v[j] = acc[m][n][4 * g + j] + bf2f((unsigned short)bv[j]); if (GELU) v[j] = gelu_t(v[j]); }
        u32x2 w = {cvtpk(v[0], v[1]), cvtpk(v[2], v[3])};
        *(u32x2*)(C + (size_t)row * N + col) = w;
      }
    }
}
extern "C" void kernel_launch(void* const* d_in, const int* in_sizes, int n_in, void* d_out, int out_size, void* d_ws, size_t ws_size, hipStream_t stream) {
  const unsigned short* x = (const unsigned short*)d_in[0]; const unsigned short* w1 = (const unsigned short*)d_in[1];
  const unsigned short* b1 = (const unsigned short*)d_in[2]; const unsigned short* w2 = (const unsigned short*)d_in[3];
  const unsigned short* b2 = (const unsigned short*)d_in[4];
  unsigned short* hbuf = (unsigned short*)d_ws; unsigned short* out = (unsigned short*)d_out;
  const int S = 2048, D = 1024, F = 3584;
  gemm128<true><<<(S / 128) * (F / 128), 256, 65536, stream>>>(x, w1, b1, hbuf, S, F, D);
  gemm128<false><<<(S / 128) * (D / 128), 256, 65536, stream>>>(hbuf, w2, b2, out, S, D, F);
}
```

```cpp
#include <hip/hip_runtime.h>
#include <hip/hip_bf16.h>
typedef short s16x4 __attribute__((ext_vector_type(4)));
typedef short s16x8 __attribute__((ext_vector_type(8)));
typedef __bf16 bf16x8 __attribute__((ext_vector_type(8)));
typedef __bf16 bf16x2 __attribute__((ext_vector_type(2)));
typedef float f32x2 __attribute__((ext_vector_type(2)));
typedef float f32x16 __attribute__((ext_vector_type(16)));
typedef unsigned u32x2 __attribute__((ext_vector_type(2)));
#define LAS __attribute__((address_space(3)))
#define DI __device__ __forceinline__
DI unsigned cvtpk(float lo, float hi) { f32x2 v = {lo, hi}; bf16x2 b = __builtin_convertvector(v, bf16x2); return __builtin_bit_cast(unsigned, b); }
DI float bf2f(unsigned short v) { return __uint_as_float(((unsigned)v) << 16); }
DI float gelu_t(float x) {
  const float c0 = -2.0f * 0.7978845608028654f * 1.4426950408889634f, c1 = c0 * 0.044715f;
  float z = x * (c1 * x * x + c0);
  float e = __builtin_amdgcn_exp2f(z);
  return x * __builtin_amdgcn_rcpf(1.0f + e);
}
template <bool GELU>
__global__ __launch_bounds__(256, 2) void gemm128(const unsigned short* __restrict__ A, const unsigned short* __restrict__ B,
                                                  const unsigned short* __restrict__ bias, unsigned short* __restrict__ C, int M, int N, int K) {
  extern __shared__ __attribute__((aligned(16))) char smem[];
  const int nN = N / 128, nM = M / 128, nwg = nN * nM;
  int wg = blockIdx.x; { int q = nwg / 8, x = wg % 8, o = wg / 8; wg = x * q + o; }
  const int GM = 4; int nig = GM * nN, gid = wg / nig, pm = gid * GM + (wg % nig) % GM, pn = (wg % nig) / GM;
  const int tid = threadIdx.x, lane = tid & 63, wid = __builtin_amdgcn_readfirstlane(tid >> 6), wr = wid >> 1, wc = wid & 1;
  const unsigned short* Ab = A + (size_t)pm * 128 * K; const unsigned short* Bb = B + pn * 128;
  int aoff[4], boff[4];
  for (int i = 0; i < 4; ++i) {
    int p = (wid * 4 + i) * 64 + lane;
    int r = p >> 3, s = p & 7; aoff[i] = r * K + ((s ^ ((r >> 1) & 7)) << 3);
    int kr = p >> 4, sl = p & 15; boff[i] = kr * N + ((sl ^ ((kr & 3) << 2)) << 3);
  }
  auto stage = [&](int buf, int kt) {
    for (int i = 0; i < 4; ++i) {
      __builtin_amdgcn_global_load_lds((const void*)(Ab + aoff[i] + kt * 64), (LAS void*)(smem + buf * 32768 + (wid * 4 + i) * 1024), 16, 0, 0);
      __builtin_amdgcn_global_load_lds((const void*)(Bb + boff[i] + (size_t)kt * 64 * N), (LAS void*)(smem + buf * 32768 + 16384 + (wid * 4 + i) * 1024), 16, 0, 0);
    }
  };
  f32x16 acc[2][2] = {};
  const int nt = K / 64;
  const int r = lane & 31, h = lane >> 5, blk = (lane >> 4) & 1, q = (lane & 15) >> 2, p = lane & 3;
  stage(0, 0);
  for (int t = 0; t < nt; ++t) {
    __syncthreads();
    if (t + 1 < nt) stage((t + 1) & 1, t + 1);
    const char* sa = smem + (t & 1) * 32768; const char* sb = sa + 16384;
#pragma unroll
    for (int ks = 0; ks < 4; ++ks) {
      bf16x8 af[2], bfr[2];
#pragma unroll
      for (int m = 0; m < 2; ++m) { int row = wr * 64 + m * 32 + r; af[m] = *(const bf16x8*)(sa + row * 128 + (((2 * ks + h) ^ ((row >> 1) & 7)) << 4)); }
#pragma unroll
      for (int n = 0; n < 2; ++n) {
        s16x4 v[2];
#pragma unroll
        for (int tt = 0; tt < 2; ++tt) { int kr = 16 * ks + 8 * h + 4 * tt + q; int ch = (wc * 64 + n * 32 + 16 * blk) / 8 + (p >> 1);
          v[tt] = __builtin_amdgcn_ds_read_tr16_b64_v4i16((LAS s16x4*)(sb + kr * 256 + ((ch ^ ((kr & 3) << 2)) << 4) + 8 * (p & 1))); }
        bfr[n] = __builtin_bit_cast(bf16x8, __builtin_shufflevector(v[0], v[1], 0, 1, 2, 3, 4, 5, 6, 7));
      }
#pragma unroll
      for (int m = 0; m < 2; ++m)
#pragma unroll
        for (int n = 0; n < 2; ++n) acc[m][n] = __builtin_amdgcn_mfma_f32_32x32x16_bf16(bfr[n], af[m], acc[m][n], 0, 0, 0);
    }
  }
#pragma unroll
  for (int m = 0; m < 2; ++m)
#pragma unroll
    for (int n = 0; n < 2; ++n) {
      int row = pm * 128 + wr * 64 + m * 32 + r;
#pragma unroll
      for (int g = 0; g < 4; ++g) {
        int col = pn * 128 + wc * 64 + n * 32 + 8 * g + 4 * h;
        float v[4];
        s16x4 bv = *(const s16x4*)(bias + col);
#pragma unroll
        for (int j = 0; j < 4; ++j) { v[j] = acc[m][n][4 * g + j] + bf2f((unsigned short)bv[j]); if (GELU) v[j] = gelu_t(v[j]); }
        u32x2 w = {cvtpk(v[0], v[1]), cvtpk(v[2], v[3])};
        *(u32x2*)(C + (size_t)row * N + col) = w;
      }
    }
}

__global__ __launch_bounds__(512) void probe(const char* __restrict__ p, int bytes_per_wg) {
  extern __shared__ __attribute__((aligned(16))) char smem[];
  const int lane = threadIdx.x & 63, wid = __builtin_amdgcn_readfirstlane(threadIdx.x >> 6);
  unsigned long long t0 = __builtin_amdgcn_s_memrealtime();
  const char* src = p + (size_t)blockIdx.x * bytes_per_wg + lane * 16;
  for (int o = wid * 1024; o < bytes_per_wg; o += 8192)
    __builtin_amdgcn_global_load_lds((const void*)(src + o), (LAS void*)(smem + o), 16, 0, 0);
  asm volatile("s_waitcnt vmcnt(0)" ::: "memory");
  __syncthreads();
  unsigned long long t1 = __builtin_amdgcn_s_memrealtime();
  unsigned long long dt = t1 - t0; if (dt > 2000) dt = 2000;
  while (__builtin_amdgcn_s_memrealtime() - t1 < dt * 100) __builtin_amdgcn_s_sleep(8);
}
extern "C" void kernel_launch(void* const* d_in, const int* in_sizes, int n_in, void* d_out, int out_size, void* d_ws, size_t ws_size, hipStream_t stream) {
  const unsigned short* x = (const unsigned short*)d_in[0]; const unsigned short* w1 = (const unsigned short*)d_in[1];
  const unsigned short* b1 = (const unsigned short*)d_in[2]; const unsigned short* w2 = (const unsigned short*)d_in[3];
  const unsigned short* b2 = (const unsigned short*)d_in[4];
  unsigned short* hbuf = (unsigned short*)d_ws; unsigned short* out = (unsigned short*)d_out;
  const int S = 2048, D = 1024, F = 3584;
  probe<<<256, 512, 32768, stream>>>((const char*)d_in[0], 16384);
  gemm128<true><<<(S / 128) * (F / 128), 256, 65536, stream>>>(x, w1, b1, hbuf, S, F, D);
  gemm128<false><<<(S / 128) * (D / 128), 256, 65536, stream>>>(hbuf, w2, b2, out, S, D, F);
}
```

```cpp
#include <hip/hip_runtime.h>

__global__ __launch_bounds__(512, 2) void fused_mlp(const void* x, const void* w1, const void* b1, const void* w2, const void* b2, void* out, void* ws, void* pad) {
  __shared__ __attribute__((aligned(1024))) char smem[163840];
  unsigned tid = threadIdx.x;
  asm volatile(
R"ASM(
s_load_dwordx16 s[8:23], %1, 0x0
s_getpc_b64 s[24:25]
.Lcode_pc:
v_lshrrev_b32 v36, 5, v0
s_lshr_b32 s75, %2, 3
s_lshl_b32 s75, s75, 11
v_lshl_add_u32 v36, v36, 7, s75
s_mov_b32 s75, .Lcode_end-.Lcode_pc-4
v_min_u32 v36, s75, v36
s_mov_b32 exec_lo, 1
s_mov_b32 exec_hi, 1
global_load_dword v45, v36, s[24:25]
s_mov_b64 exec, -1
s_mov_b32 s79, %2
v_readfirstlane_b32 s56, v0
v_and_b32 v0, 63, v0
s_and_b32 s76, s79, 3
s_bfe_u32 s78, s79, 0x30003
s_lshr_b32 s77, s79, 6
s_lshr_b32 s56, s56, 6
s_and_b32 s72, s79, 4
s_add_u32 s77, s77, s72
s_cmp_ge_u32 s56, 4
s_cbranch_scc1 .LroleH
s_lshl_b32 s57, s56, 10
s_lshl_b32 s72, s56, 3
v_lshrrev_b32 v1, 3, v0
v_add_u32 v36, s72, v1
s_and_b32 s73, s56, 1
s_lshl_b32 s73, s73, 2
v_and_b32 v37, 7, v0
v_lshrrev_b32 v1, 4, v0
v_add_u32 v1, s73, v1
v_xor_b32 v1, v1, v37
v_lshlrev_b32 v1, 4, v1
v_lshl_add_u32 v33, v36, 11, v1
v_bfe_u32 v1, v0, 4, 1
v_lshl_add_u32 v1, v1, 1, s73
v_xor_b32 v1, v1, v37
v_lshlrev_b32 v1, 4, v1
s_movk_i32 s74, 7168
v_mad_u32_u24 v44, v36, s74, v1
s_lshr_b32 s58, s56, 1
s_mul_i32 s58, s58, 40960
s_lshl_b32 s74, s73, 10
s_add_u32 s58, s58, s74
s_and_b32 s72, s78, 3
s_lshl_b32 s73, s76, 2
s_add_u32 s72, s72, s73
s_mul_i32 s72, s72, 448
s_and_b32 s73, s78, 1
s_lshl_b32 s74, s73, 6
s_add_u32 s70, s72, s74
s_mul_i32 s74, s73, 448
s_sub_u32 s68, 384, s74
s_sub_u32 s69, 256, s68
s_and_b32 s72, s77, 4
s_and_b32 s73, s77, 1
s_add_u32 s73, s73, s76
s_lshl_b32 s73, s73, 1
s_add_u32 s72, s72, s73
s_lshr_b32 s73, s78, 2
s_add_u32 s72, s72, s73
s_and_b32 s73, s78, 1
s_add_u32 s72, s72, s73
s_mov_b32 s63, s72
s_mov_b32 s66, s63
s_mov_b32 s67, s70
s_lshl_b32 s72, s77, 1
s_lshr_b32 s73, s78, 2
s_add_u32 s72, s72, s73
s_lshl_b32 s72, s72, 18
s_waitcnt lgkmcnt(0)
s_add_u32 s36, s8, s72
s_addc_u32 s37, s9, 0
s_and_b32 s37, s37, 0xffff
s_sub_u32 s38, 4194304, s72
s_mov_b32 s39, 0x20000
s_mov_b32 s40, s10
s_and_b32 s41, s11, 0xffff
s_mov_b32 s42, 7340032
s_mov_b32 s43, 0x20000
s_add_u32 s60, s63, 0
s_and_b32 s60, s60, 7
s_lshl_b32 s60, s60, 7
s_add_u32 m0, s57, 81920
s_add_u32 s59, s60, 0
buffer_load_dwordx4 v33, s[36:39], s59 offen lds
s_add_u32 m0, s57, 86016
s_add_u32 s59, s60, 65536
buffer_load_dwordx4 v33, s[36:39], s59 offen lds
s_add_u32 m0, s57, 90112
s_add_u32 s59, s60, 131072
buffer_load_dwordx4 v33, s[36:39], s59 offen lds
s_add_u32 m0, s57, 94208
s_add_u32 s59, s60, 196608
buffer_load_dwordx4 v33, s[36:39], s59 offen lds
s_add_u32 s64, s63, 0
s_and_b32 s64, s64, 7
s_mul_i32 s64, s64, 458752
s_add_u32 s64, s64, s67
s_add_u32 m0, s57, 0
s_add_u32 s59, s64, 0
buffer_load_dwordx4 v44, s[40:43], s59 offen lds
s_add_u32 m0, s57, 4096
s_add_u32 s59, s64, 128
buffer_load_dwordx4 v44, s[40:43], s59 offen lds
s_add_u32 m0, s57, 40960
s_add_u32 s59, s64, 3670016
buffer_load_dwordx4 v44, s[40:43], s59 offen lds
s_add_u32 m0, s57, 45056
s_add_u32 s59, s64, 3670144
buffer_load_dwordx4 v44, s[40:43], s59 offen lds
s_add_u32 s60, s63, 0
s_and_b32 s60, s60, 7
s_lshl_b32 s60, s60, 7
s_add_u32 m0, s57, 98304
s_add_u32 s59, s60, 1024
buffer_load_dwordx4 v33, s[36:39], s59 offen lds
s_add_u32 m0, s57, 102400
s_add_u32 s59, s60, 66560
buffer_load_dwordx4 v33, s[36:39], s59 offen lds
s_add_u32 m0, s57, 106496
s_add_u32 s59, s60, 132096
buffer_load_dwordx4 v33, s[36:39], s59 offen lds
s_add_u32 m0, s57, 110592
s_add_u32 s59, s60, 197632
buffer_load_dwordx4 v33, s[36:39], s59 offen lds
s_lshl_b32 s81, s79, 13
s_load_dwordx2 s[92:93], s[20:21], s81
s_xor_b32 s88, s81, 8192
s_load_dwordx2 s[94:95], s[20:21], s88
s_xor_b32 s89, s81, 16384
s_load_dwordx2 s[96:97], s[20:21], s89
s_xor_b32 s90, s81, 24576
s_load_dwordx2 s[98:99], s[20:21], s90
v_lshlrev_b32 v31, 4, v0
v_bfe_u32 v1, v0, 4, 2
v_lshlrev_b32 v24, 10, v1
v_bfe_u32 v1, v0, 2, 2
v_lshl_add_u32 v24, v1, 7, v24
v_bfe_u32 v1, v0, 3, 2
v_lshl_add_u32 v24, v1, 5, v24
v_bfe_u32 v1, v0, 0, 2
v_lshl_add_u32 v24, v1, 3, v24
v_add_u32 v24, 0, v24
v_xor_b32 v25, 32, v24
v_xor_b32 v26, 64, v24
v_xor_b32 v27, 96, v24
v_bfe_u32 v1, v0, 1, 3
v_lshrrev_b32 v28, 4, v0
v_xor_b32 v1, v1, v28
v_and_b32 v28, 15, v0
v_lshlrev_b32 v28, 7, v28
v_lshl_add_u32 v28, v1, 4, v28
s_lshl_b32 s72, s56, 12
s_add_u32 s73, s72, 81920
v_add_u32 v28, s73, v28
v_xor_b32 v29, 64, v28
s_add_u32 s72, s72, 98304
v_add_u32 v30, s72, v31
s_add_u32 m0, s57, 8192
s_add_u32 s59, s64, 229376
buffer_load_dwordx4 v44, s[40:43], s59 offen lds
s_add_u32 m0, s57, 12288
s_add_u32 s59, s64, 229504
buffer_load_dwordx4 v44, s[40:43], s59 offen lds
s_add_u32 m0, s57, 49152
s_add_u32 s59, s64, 3899392
buffer_load_dwordx4 v44, s[40:43], s59 offen lds
s_add_u32 m0, s57, 53248
s_add_u32 s59, s64, 3899520
buffer_load_dwordx4 v44, s[40:43], s59 offen lds
s_add_u32 s60, s66, 1
s_and_b32 s60, s60, 7
s_lshl_b32 s60, s60, 7
s_add_u32 m0, s57, 114688
s_add_u32 s59, s60, 1024
buffer_load_dwordx4 v33, s[36:39], s59 offen lds
s_add_u32 m0, s57, 118784
s_add_u32 s59, s60, 66560
buffer_load_dwordx4 v33, s[36:39], s59 offen lds
s_add_u32 m0, s57, 122880
s_add_u32 s59, s60, 132096
buffer_load_dwordx4 v33, s[36:39], s59 offen lds
s_add_u32 m0, s57, 126976
s_add_u32 s59, s60, 197632
buffer_load_dwordx4 v33, s[36:39], s59 offen lds
s_mov_b32 s52, s12
s_and_b32 s53, s13, 0xffff
s_mov_b32 s54, 7168
s_mov_b32 s55, 0x20000
s_add_u32 s91, s70, s68
v_lshrrev_b32 v40, 4, v0
v_lshlrev_b32 v40, 3, v40
s_waitcnt lgkmcnt(0)
s_waitcnt vmcnt(8)
s_barrier
ds_read_b128 v[128:131], v28 offset:0
ds_read_b128 v[132:135], v28 offset:2048
)ASM"
R"ASM(
ds_read_b64_tr_b16 v[4:5], v24 offset:0
ds_read_b64_tr_b16 v[6:7], v24 offset:512
s_add_u32 s64, s66, 1
s_and_b32 s64, s64, 7
s_mul_i32 s64, s64, 458752
s_add_u32 s64, s64, s67
s_add_u32 m0, s57, 16384
s_add_u32 s59, s64, 0
buffer_load_dwordx4 v44, s[40:43], s59 offen lds
s_add_u32 m0, s57, 20480
s_add_u32 s59, s64, 128
buffer_load_dwordx4 v44, s[40:43], s59 offen lds
ds_read_b64_tr_b16 v[8:9], v25 offset:0
ds_read_b64_tr_b16 v[10:11], v25 offset:512
s_add_u32 m0, s57, 57344
s_add_u32 s59, s64, 3670016
buffer_load_dwordx4 v44, s[40:43], s59 offen lds
ds_read_b64_tr_b16 v[12:13], v26 offset:0
ds_read_b64_tr_b16 v[14:15], v26 offset:512
ds_read_b128 v[136:139], v29 offset:0
s_add_u32 m0, s57, 61440
s_add_u32 s59, s64, 3670144
buffer_load_dwordx4 v44, s[40:43], s59 offen lds
s_add_u32 s60, s66, 1
s_and_b32 s60, s60, 7
s_lshl_b32 s60, s60, 7
s_add_u32 m0, s57, 131072
s_add_u32 s59, s60, 0
buffer_load_dwordx4 v33, s[36:39], s59 offen lds
ds_read_b64_tr_b16 v[16:17], v27 offset:0
ds_read_b64_tr_b16 v[18:19], v27 offset:512
ds_read_b128 v[140:143], v29 offset:2048
s_add_u32 m0, s57, 135168
s_add_u32 s59, s60, 65536
buffer_load_dwordx4 v33, s[36:39], s59 offen lds
ds_read_b64_tr_b16 v[20:21], v24 offset:4096
s_add_u32 m0, s57, 139264
s_add_u32 s59, s60, 131072
buffer_load_dwordx4 v33, s[36:39], s59 offen lds
s_add_u32 m0, s57, 143360
s_waitcnt lgkmcnt(7)
v_mfma_f32_16x16x32_bf16 v[48:51], v[4:7], v[128:131], 0
ds_read_b64_tr_b16 v[22:23], v24 offset:4608
s_add_u32 s59, s60, 196608
buffer_load_dwordx4 v33, s[36:39], s59 offen lds
s_mul_i32 s72, s76, 1835008
s_lshl_b32 s73, s78, 8
v_mfma_f32_16x16x32_bf16 v[56:59], v[4:7], v[132:135], 0
ds_read_b64_tr_b16 v[4:5], v25 offset:4096
s_add_u32 m0, s57, 24576
s_add_u32 s59, s64, 229376
v_mfma_f32_16x16x32_bf16 v[60:63], v[8:11], v[132:135], 0
ds_read_b64_tr_b16 v[6:7], v25 offset:4608
buffer_load_dwordx4 v44, s[40:43], s59 offen lds
s_add_u32 s72, s72, s73
v_mfma_f32_16x16x32_bf16 v[52:55], v[8:11], v[128:131], 0
ds_read_b64_tr_b16 v[8:9], v26 offset:4096
s_add_u32 m0, s57, 28672
s_add_u32 s59, s64, 229504
buffer_load_dwordx4 v44, s[40:43], s59 offen lds
s_add_u32 m0, s57, 65536
s_waitcnt lgkmcnt(6)
v_mfma_f32_16x16x32_bf16 v[64:67], v[12:15], v[128:131], 0
ds_read_b64_tr_b16 v[10:11], v26 offset:4608
s_add_u32 s59, s64, 3899392
buffer_load_dwordx4 v44, s[40:43], s59 offen lds
s_add_u32 s44, s14, s72
s_addc_u32 s45, s15, 0
s_and_b32 s45, s45, 0xffff
v_mfma_f32_16x16x32_bf16 v[72:75], v[12:15], v[132:135], 0
ds_read_b64_tr_b16 v[12:13], v27 offset:4096
s_add_u32 m0, s57, 69632
s_add_u32 s59, s64, 3899520
v_mfma_f32_16x16x32_bf16 v[76:79], v[16:19], v[132:135], 0
ds_read_b64_tr_b16 v[14:15], v27 offset:4608
buffer_load_dwordx4 v44, s[40:43], s59 offen lds
s_sub_u32 s46, 7340032, s72
v_mfma_f32_16x16x32_bf16 v[68:71], v[16:19], v[128:131], 0
s_waitcnt lgkmcnt(8)
s_waitcnt vmcnt(12)
s_barrier
ds_read_b64_tr_b16 v[16:17], v24 offset:8192
s_add_u32 s60, s63, 2
s_and_b32 s60, s60, 7
s_lshl_b32 s60, s60, 7
s_add_u32 m0, s57, 81920
s_add_u32 s59, s60, 1024
s_waitcnt lgkmcnt(5)
v_mfma_f32_16x16x32_bf16 v[80:83], v[20:23], v[128:131], 0
ds_read_b64_tr_b16 v[18:19], v24 offset:8704
buffer_load_dwordx4 v33, s[36:39], s59 offen lds
s_add_u32 m0, s57, 86016
s_add_u32 s59, s60, 66560
buffer_load_dwordx4 v33, s[36:39], s59 offen lds
s_mov_b32 s47, 0x20000
s_lshl_b32 s72, s56, 4
v_mfma_f32_16x16x32_bf16 v[88:91], v[20:23], v[132:135], 0
ds_read_b64_tr_b16 v[20:21], v25 offset:8192
s_add_u32 m0, s57, 90112
s_add_u32 s59, s60, 132096
v_mfma_f32_16x16x32_bf16 v[92:95], v[4:7], v[132:135], 0
ds_read_b64_tr_b16 v[22:23], v25 offset:8704
buffer_load_dwordx4 v33, s[36:39], s59 offen lds
v_lshrrev_b32 v2, 2, v0
v_mfma_f32_16x16x32_bf16 v[84:87], v[4:7], v[128:131], 0
ds_read_b64_tr_b16 v[4:5], v26 offset:8192
s_add_u32 m0, s57, 94208
s_add_u32 s59, s60, 197632
buffer_load_dwordx4 v33, s[36:39], s59 offen lds
s_add_u32 s64, s63, 2
s_and_b32 s64, s64, 7
s_mul_i32 s64, s64, 458752
s_waitcnt lgkmcnt(5)
v_mfma_f32_16x16x32_bf16 v[96:99], v[8:11], v[128:131], 0
ds_read_b64_tr_b16 v[6:7], v26 offset:8704
s_add_u32 s64, s64, s67
s_add_u32 m0, s57, 32768
s_add_u32 s59, s64, 0
buffer_load_dwordx4 v44, s[40:43], s59 offen lds
v_add_u32 v2, s72, v2
v_lshrrev_b32 v1, 5, v0
v_mfma_f32_16x16x32_bf16 v[104:107], v[8:11], v[132:135], 0
ds_read_b64_tr_b16 v[8:9], v27 offset:8192
s_add_u32 m0, s57, 36864
s_add_u32 s59, s64, 128
v_mfma_f32_16x16x32_bf16 v[108:111], v[12:15], v[132:135], 0
ds_read_b64_tr_b16 v[10:11], v27 offset:8704
buffer_load_dwordx4 v44, s[40:43], s59 offen lds
v_lshlrev_b32 v1, 1, v1
v_mfma_f32_16x16x32_bf16 v[100:103], v[12:15], v[128:131], 0
ds_read_b64_tr_b16 v[12:13], v24 offset:12288
s_add_u32 m0, s57, 73728
s_add_u32 s59, s64, 3670016
buffer_load_dwordx4 v44, s[40:43], s59 offen lds
s_waitcnt lgkmcnt(5)
v_mfma_f32_16x16x32_bf16 v[48:51], v[16:19], v[136:139], v[48:51]
ds_read_b64_tr_b16 v[14:15], v24 offset:12800
s_add_u32 m0, s57, 77824
s_add_u32 s59, s64, 3670144
buffer_load_dwordx4 v44, s[40:43], s59 offen lds
v_and_b32 v45, 3, v0
v_mfma_f32_16x16x32_bf16 v[56:59], v[16:19], v[140:143], v[56:59]
ds_read_b64_tr_b16 v[16:17], v25 offset:12288
s_add_u32 s60, s63, 2
s_and_b32 s60, s60, 7
s_lshl_b32 s60, s60, 7
s_add_u32 m0, s57, 98304
v_mfma_f32_16x16x32_bf16 v[60:63], v[20:23], v[140:143], v[60:63]
ds_read_b64_tr_b16 v[18:19], v25 offset:12800
s_add_u32 s59, s60, 0
buffer_load_dwordx4 v33, s[36:39], s59 offen lds
v_xor_b32 v45, v1, v45
v_lshlrev_b32 v45, 4, v45
v_mfma_f32_16x16x32_bf16 v[52:55], v[20:23], v[136:139], v[52:55]
ds_read_b64_tr_b16 v[20:21], v26 offset:12288
s_add_u32 m0, s57, 102400
s_add_u32 s59, s60, 65536
buffer_load_dwordx4 v33, s[36:39], s59 offen lds
s_waitcnt lgkmcnt(5)
v_mfma_f32_16x16x32_bf16 v[64:67], v[4:7], v[136:139], v[64:67]
ds_read_b64_tr_b16 v[22:23], v26 offset:12800
s_add_u32 m0, s57, 106496
s_add_u32 s59, s60, 131072
buffer_load_dwordx4 v33, s[36:39], s59 offen lds
s_movk_i32 s74, 7168
v_mfma_f32_16x16x32_bf16 v[72:75], v[4:7], v[140:143], v[72:75]
ds_read_b64_tr_b16 v[4:5], v27 offset:12288
s_add_u32 m0, s57, 110592
s_add_u32 s59, s60, 196608
v_mfma_f32_16x16x32_bf16 v[76:79], v[8:11], v[140:143], v[76:79]
ds_read_b64_tr_b16 v[6:7], v27 offset:12800
buffer_load_dwordx4 v33, s[36:39], s59 offen lds
v_mad_u32_u24 v45, v2, s74, v45
s_lshr_b32 s72, s77, 2
v_mfma_f32_16x16x32_bf16 v[68:71], v[8:11], v[136:139], v[68:71]
s_waitcnt vmcnt(16)
s_barrier
ds_read_b128 v[144:147], v28 offset:49152
ds_read_b128 v[148:151], v28 offset:51200
ds_read_b64_tr_b16 v[8:9], v24 offset:16384
s_add_u32 m0, s57, 0
s_add_u32 s59, s64, 229376
s_waitcnt lgkmcnt(7)
v_mfma_f32_16x16x32_bf16 v[80:83], v[12:15], v[136:139], v[80:83]
ds_read_b64_tr_b16 v[10:11], v24 offset:16896
buffer_load_dwordx4 v44, s[40:43], s59 offen lds
s_mul_i32 s72, s72, 448
v_mfma_f32_16x16x32_bf16 v[88:91], v[12:15], v[140:143], v[88:91]
ds_read_b64_tr_b16 v[12:13], v25 offset:16384
s_add_u32 m0, s57, 4096
s_add_u32 s59, s64, 229504
v_mfma_f32_16x16x32_bf16 v[92:95], v[16:19], v[140:143], v[92:95]
ds_read_b64_tr_b16 v[14:15], v25 offset:16896
buffer_load_dwordx4 v44, s[40:43], s59 offen lds
s_and_b32 s73, s77, 3
s_lshl_b32 s73, s73, 3
v_mfma_f32_16x16x32_bf16 v[84:87], v[16:19], v[136:139], v[84:87]
ds_read_b64_tr_b16 v[16:17], v26 offset:16384
ds_read_b128 v[152:155], v29 offset:49152
s_add_u32 m0, s57, 40960
)ASM"
R"ASM(
s_waitcnt lgkmcnt(8)
v_mfma_f32_16x16x32_bf16 v[96:99], v[20:23], v[136:139], v[96:99]
ds_read_b64_tr_b16 v[18:19], v26 offset:16896
s_add_u32 s59, s64, 3899392
buffer_load_dwordx4 v44, s[40:43], s59 offen lds
s_add_u32 s72, s72, s73
v_mfma_f32_16x16x32_bf16 v[104:107], v[20:23], v[140:143], v[104:107]
ds_read_b64_tr_b16 v[20:21], v27 offset:16384
ds_read_b128 v[156:159], v29 offset:51200
s_add_u32 m0, s57, 45056
v_mfma_f32_16x16x32_bf16 v[108:111], v[4:7], v[140:143], v[108:111]
ds_read_b64_tr_b16 v[22:23], v27 offset:16896
s_add_u32 s59, s64, 3899520
buffer_load_dwordx4 v44, s[40:43], s59 offen lds
s_lshl_b32 s72, s72, 11
v_mfma_f32_16x16x32_bf16 v[100:103], v[4:7], v[136:139], v[100:103]
ds_read_b64_tr_b16 v[4:5], v24 offset:20480
s_add_u32 s60, s66, 3
s_and_b32 s60, s60, 7
s_lshl_b32 s60, s60, 7
s_add_u32 m0, s57, 114688
s_waitcnt lgkmcnt(7)
v_mfma_f32_16x16x32_bf16 v[48:51], v[8:11], v[144:147], v[48:51]
ds_read_b64_tr_b16 v[6:7], v24 offset:20992
s_add_u32 s59, s60, 1024
buffer_load_dwordx4 v33, s[36:39], s59 offen lds
v_bfe_u32 v1, v0, 1, 3
v_lshlrev_b32 v43, 11, v1
v_mfma_f32_16x16x32_bf16 v[56:59], v[8:11], v[148:151], v[56:59]
ds_read_b64_tr_b16 v[8:9], v25 offset:20480
s_add_u32 m0, s57, 118784
s_add_u32 s59, s60, 66560
v_mfma_f32_16x16x32_bf16 v[60:63], v[12:15], v[148:151], v[60:63]
ds_read_b64_tr_b16 v[10:11], v25 offset:20992
buffer_load_dwordx4 v33, s[36:39], s59 offen lds
v_bfe_u32 v1, v0, 0, 1
v_mfma_f32_16x16x32_bf16 v[52:55], v[12:15], v[144:147], v[52:55]
ds_read_b64_tr_b16 v[12:13], v26 offset:20480
s_add_u32 m0, s57, 122880
s_add_u32 s59, s60, 132096
s_waitcnt lgkmcnt(5)
v_mfma_f32_16x16x32_bf16 v[64:67], v[16:19], v[144:147], v[64:67]
ds_read_b64_tr_b16 v[14:15], v26 offset:20992
buffer_load_dwordx4 v33, s[36:39], s59 offen lds
v_lshl_add_u32 v43, v1, 7, v43
v_add_u32 v43, s72, v43
v_mfma_f32_16x16x32_bf16 v[72:75], v[16:19], v[148:151], v[72:75]
ds_read_b64_tr_b16 v[16:17], v27 offset:20480
s_add_u32 m0, s57, 126976
s_add_u32 s59, s60, 197632
v_mfma_f32_16x16x32_bf16 v[76:79], v[20:23], v[148:151], v[76:79]
ds_read_b64_tr_b16 v[18:19], v27 offset:20992
buffer_load_dwordx4 v33, s[36:39], s59 offen lds
s_mov_b32 s48, s20
v_mfma_f32_16x16x32_bf16 v[68:71], v[20:23], v[144:147], v[68:71]
s_waitcnt vmcnt(16)
s_barrier
ds_read_b64_tr_b16 v[20:21], v24 offset:24576
s_add_u32 s64, s66, 3
s_and_b32 s64, s64, 7
s_mul_i32 s64, s64, 458752
s_add_u32 s64, s64, s67
s_waitcnt lgkmcnt(5)
v_mfma_f32_16x16x32_bf16 v[80:83], v[4:7], v[144:147], v[80:83]
ds_read_b64_tr_b16 v[22:23], v24 offset:25088
s_add_u32 m0, s57, 8192
s_add_u32 s59, s64, 0
buffer_load_dwordx4 v44, s[40:43], s59 offen lds
s_and_b32 s49, s21, 0xffff
s_mov_b32 s50, 33554432
v_mfma_f32_16x16x32_bf16 v[88:91], v[4:7], v[148:151], v[88:91]
ds_read_b64_tr_b16 v[4:5], v25 offset:24576
s_add_u32 m0, s57, 12288
s_add_u32 s59, s64, 128
v_mfma_f32_16x16x32_bf16 v[92:95], v[8:11], v[148:151], v[92:95]
ds_read_b64_tr_b16 v[6:7], v25 offset:25088
buffer_load_dwordx4 v44, s[40:43], s59 offen lds
s_mov_b32 s51, 0x20000
v_mfma_f32_16x16x32_bf16 v[84:87], v[8:11], v[144:147], v[84:87]
ds_read_b64_tr_b16 v[8:9], v26 offset:24576
s_add_u32 m0, s57, 49152
s_add_u32 s59, s64, 3670016
s_waitcnt lgkmcnt(5)
v_mfma_f32_16x16x32_bf16 v[96:99], v[12:15], v[144:147], v[96:99]
ds_read_b64_tr_b16 v[10:11], v26 offset:25088
buffer_load_dwordx4 v44, s[40:43], s59 offen lds
s_lshl_b32 s80, s77, 2
v_mfma_f32_16x16x32_bf16 v[104:107], v[12:15], v[148:151], v[104:107]
ds_read_b64_tr_b16 v[12:13], v27 offset:24576
s_add_u32 m0, s57, 53248
s_add_u32 s59, s64, 3670144
v_mfma_f32_16x16x32_bf16 v[108:111], v[16:19], v[148:151], v[108:111]
ds_read_b64_tr_b16 v[14:15], v27 offset:25088
buffer_load_dwordx4 v44, s[40:43], s59 offen lds
s_add_u32 s80, s80, s76
s_mul_i32 s80, s80, 458752
v_mfma_f32_16x16x32_bf16 v[100:103], v[16:19], v[144:147], v[100:103]
ds_read_b64_tr_b16 v[16:17], v24 offset:28672
s_add_u32 s60, s66, 3
s_and_b32 s60, s60, 7
s_lshl_b32 s60, s60, 7
s_waitcnt lgkmcnt(5)
v_mfma_f32_16x16x32_bf16 v[48:51], v[20:23], v[152:155], v[48:51]
ds_read_b64_tr_b16 v[18:19], v24 offset:29184
s_add_u32 m0, s57, 131072
s_add_u32 s59, s60, 0
buffer_load_dwordx4 v33, s[36:39], s59 offen lds
s_mul_i32 s72, s78, 57344
v_mfma_f32_16x16x32_bf16 v[56:59], v[20:23], v[156:159], v[56:59]
ds_read_b64_tr_b16 v[20:21], v25 offset:28672
s_add_u32 m0, s57, 135168
s_add_u32 s59, s60, 65536
v_mfma_f32_16x16x32_bf16 v[60:63], v[4:7], v[156:159], v[60:63]
ds_read_b64_tr_b16 v[22:23], v25 offset:29184
buffer_load_dwordx4 v33, s[36:39], s59 offen lds
s_add_u32 s80, s80, s72
s_lshl_b32 s72, s56, 11
v_mfma_f32_16x16x32_bf16 v[52:55], v[4:7], v[152:155], v[52:55]
ds_read_b64_tr_b16 v[4:5], v26 offset:28672
s_add_u32 m0, s57, 139264
s_add_u32 s59, s60, 131072
s_waitcnt lgkmcnt(5)
v_mfma_f32_16x16x32_bf16 v[64:67], v[8:11], v[152:155], v[64:67]
ds_read_b64_tr_b16 v[6:7], v26 offset:29184
buffer_load_dwordx4 v33, s[36:39], s59 offen lds
s_add_u32 s80, s80, s72
v_mfma_f32_16x16x32_bf16 v[72:75], v[8:11], v[156:159], v[72:75]
ds_read_b64_tr_b16 v[8:9], v27 offset:28672
s_add_u32 m0, s57, 143360
s_add_u32 s59, s60, 196608
v_mfma_f32_16x16x32_bf16 v[76:79], v[12:15], v[156:159], v[76:79]
ds_read_b64_tr_b16 v[10:11], v27 offset:29184
buffer_load_dwordx4 v33, s[36:39], s59 offen lds
s_add_u32 s80, s80, 2097152
s_lshl_b32 s72, s56, 12
v_mfma_f32_16x16x32_bf16 v[68:71], v[12:15], v[152:155], v[68:71]
s_waitcnt vmcnt(16)
s_barrier
ds_read_b128 v[160:163], v28 offset:16384
ds_read_b128 v[164:167], v28 offset:18432
ds_read_b64_tr_b16 v[12:13], v24 offset:32768
s_add_u32 m0, s57, 16384
s_add_u32 s59, s64, 229376
s_waitcnt lgkmcnt(7)
v_mfma_f32_16x16x32_bf16 v[80:83], v[16:19], v[152:155], v[80:83]
ds_read_b64_tr_b16 v[14:15], v24 offset:33280
buffer_load_dwordx4 v44, s[40:43], s59 offen lds
s_add_u32 s72, s72, 16777216
v_mfma_f32_16x16x32_bf16 v[88:91], v[16:19], v[156:159], v[88:91]
ds_read_b64_tr_b16 v[16:17], v25 offset:32768
s_add_u32 m0, s57, 20480
s_add_u32 s59, s64, 229504
v_mfma_f32_16x16x32_bf16 v[92:95], v[20:23], v[156:159], v[92:95]
ds_read_b64_tr_b16 v[18:19], v25 offset:33280
buffer_load_dwordx4 v44, s[40:43], s59 offen lds
s_mul_i32 s84, s79, 49152
s_add_u32 s84, s84, s72
v_mfma_f32_16x16x32_bf16 v[84:87], v[20:23], v[152:155], v[84:87]
ds_read_b64_tr_b16 v[20:21], v26 offset:32768
ds_read_b128 v[168:171], v29 offset:16384
s_add_u32 m0, s57, 57344
s_waitcnt lgkmcnt(8)
v_mfma_f32_16x16x32_bf16 v[96:99], v[4:7], v[152:155], v[96:99]
ds_read_b64_tr_b16 v[22:23], v26 offset:33280
s_add_u32 s59, s64, 3899392
buffer_load_dwordx4 v44, s[40:43], s59 offen lds
s_and_b32 s75, s76, 1
v_mfma_f32_16x16x32_bf16 v[104:107], v[4:7], v[156:159], v[104:107]
ds_read_b64_tr_b16 v[4:5], v27 offset:32768
ds_read_b128 v[172:175], v29 offset:18432
s_add_u32 m0, s57, 61440
v_mfma_f32_16x16x32_bf16 v[108:111], v[8:11], v[156:159], v[108:111]
ds_read_b64_tr_b16 v[6:7], v27 offset:33280
s_add_u32 s59, s64, 3899520
buffer_load_dwordx4 v44, s[40:43], s59 offen lds
s_xor_b32 s75, s75, 2
v_mfma_f32_16x16x32_bf16 v[100:103], v[8:11], v[152:155], v[100:103]
ds_read_b64_tr_b16 v[8:9], v24 offset:36864
s_add_u32 s60, s63, 4
s_and_b32 s60, s60, 7
s_lshl_b32 s60, s60, 7
s_add_u32 m0, s57, 81920
s_waitcnt lgkmcnt(7)
v_mfma_f32_16x16x32_bf16 v[48:51], v[12:15], v[160:163], v[48:51]
ds_read_b64_tr_b16 v[10:11], v24 offset:37376
s_add_u32 s59, s60, 1024
buffer_load_dwordx4 v33, s[36:39], s59 offen lds
s_xor_b32 s73, s79, s75
s_mul_i32 s73, s73, 49152
v_mfma_f32_16x16x32_bf16 v[56:59], v[12:15], v[164:167], v[56:59]
ds_read_b64_tr_b16 v[12:13], v25 offset:36864
s_add_u32 m0, s57, 86016
s_add_u32 s59, s60, 66560
v_mfma_f32_16x16x32_bf16 v[60:63], v[16:19], v[164:167], v[60:63]
ds_read_b64_tr_b16 v[14:15], v25 offset:37376
buffer_load_dwordx4 v33, s[36:39], s59 offen lds
s_sub_u32 s75, s75, 1
v_mfma_f32_16x16x32_bf16 v[52:55], v[16:19], v[160:163], v[52:55]
ds_read_b64_tr_b16 v[16:17], v26 offset:36864
s_add_u32 m0, s57, 90112
)ASM"
R"ASM(
s_add_u32 s59, s60, 132096
s_waitcnt lgkmcnt(5)
v_mfma_f32_16x16x32_bf16 v[64:67], v[20:23], v[160:163], v[64:67]
ds_read_b64_tr_b16 v[18:19], v26 offset:37376
buffer_load_dwordx4 v33, s[36:39], s59 offen lds
s_lshl_b32 s75, s75, 14
s_add_u32 s73, s73, s75
v_mfma_f32_16x16x32_bf16 v[72:75], v[20:23], v[164:167], v[72:75]
ds_read_b64_tr_b16 v[20:21], v27 offset:36864
s_add_u32 m0, s57, 94208
s_add_u32 s59, s60, 197632
v_mfma_f32_16x16x32_bf16 v[76:79], v[4:7], v[164:167], v[76:79]
ds_read_b64_tr_b16 v[22:23], v27 offset:37376
buffer_load_dwordx4 v33, s[36:39], s59 offen lds
s_add_u32 s85, s73, s72
v_mfma_f32_16x16x32_bf16 v[68:71], v[4:7], v[160:163], v[68:71]
s_waitcnt vmcnt(16)
s_barrier
ds_read_b64_tr_b16 v[4:5], v24 offset:0
s_add_u32 s64, s63, 4
s_and_b32 s64, s64, 7
s_mul_i32 s64, s64, 458752
s_add_u32 s64, s64, s67
s_waitcnt lgkmcnt(5)
v_mfma_f32_16x16x32_bf16 v[80:83], v[8:11], v[160:163], v[80:83]
ds_read_b64_tr_b16 v[6:7], v24 offset:512
s_add_u32 m0, s57, 24576
s_add_u32 s59, s64, 0
buffer_load_dwordx4 v44, s[40:43], s59 offen lds
s_and_b32 s75, s76, 1
s_xor_b32 s75, s75, 3
v_mfma_f32_16x16x32_bf16 v[88:91], v[8:11], v[164:167], v[88:91]
ds_read_b64_tr_b16 v[8:9], v25 offset:0
s_add_u32 m0, s57, 28672
s_add_u32 s59, s64, 128
v_mfma_f32_16x16x32_bf16 v[92:95], v[12:15], v[164:167], v[92:95]
ds_read_b64_tr_b16 v[10:11], v25 offset:512
buffer_load_dwordx4 v44, s[40:43], s59 offen lds
s_xor_b32 s73, s79, s75
v_mfma_f32_16x16x32_bf16 v[84:87], v[12:15], v[160:163], v[84:87]
ds_read_b64_tr_b16 v[12:13], v26 offset:0
s_add_u32 m0, s57, 65536
s_add_u32 s59, s64, 3670016
s_waitcnt lgkmcnt(5)
v_mfma_f32_16x16x32_bf16 v[96:99], v[16:19], v[160:163], v[96:99]
ds_read_b64_tr_b16 v[14:15], v26 offset:512
buffer_load_dwordx4 v44, s[40:43], s59 offen lds
s_mul_i32 s73, s73, 49152
v_mfma_f32_16x16x32_bf16 v[104:107], v[16:19], v[164:167], v[104:107]
ds_read_b64_tr_b16 v[16:17], v27 offset:0
s_add_u32 m0, s57, 69632
s_add_u32 s59, s64, 3670144
v_mfma_f32_16x16x32_bf16 v[108:111], v[20:23], v[164:167], v[108:111]
ds_read_b64_tr_b16 v[18:19], v27 offset:512
buffer_load_dwordx4 v44, s[40:43], s59 offen lds
s_sub_u32 s75, s75, 1
s_lshl_b32 s75, s75, 14
v_mfma_f32_16x16x32_bf16 v[100:103], v[20:23], v[160:163], v[100:103]
ds_read_b64_tr_b16 v[20:21], v24 offset:4096
s_add_u32 s60, s63, 4
s_and_b32 s60, s60, 7
s_lshl_b32 s60, s60, 7
s_waitcnt lgkmcnt(5)
v_mfma_f32_16x16x32_bf16 v[48:51], v[4:7], v[168:171], v[48:51]
ds_read_b64_tr_b16 v[22:23], v24 offset:4608
s_add_u32 m0, s57, 98304
s_add_u32 s59, s60, 0
buffer_load_dwordx4 v33, s[36:39], s59 offen lds
s_add_u32 s73, s73, s75
v_mfma_f32_16x16x32_bf16 v[56:59], v[4:7], v[172:175], v[56:59]
ds_read_b64_tr_b16 v[4:5], v25 offset:4096
s_add_u32 m0, s57, 102400
s_add_u32 s59, s60, 65536
v_mfma_f32_16x16x32_bf16 v[60:63], v[8:11], v[172:175], v[60:63]
ds_read_b64_tr_b16 v[6:7], v25 offset:4608
buffer_load_dwordx4 v33, s[36:39], s59 offen lds
s_add_u32 s86, s73, s72
s_mov_b32 s75, 1
v_mfma_f32_16x16x32_bf16 v[52:55], v[8:11], v[168:171], v[52:55]
ds_read_b64_tr_b16 v[8:9], v26 offset:4096
s_add_u32 m0, s57, 106496
s_add_u32 s59, s60, 131072
s_waitcnt lgkmcnt(5)
v_mfma_f32_16x16x32_bf16 v[64:67], v[12:15], v[168:171], v[64:67]
ds_read_b64_tr_b16 v[10:11], v26 offset:4608
buffer_load_dwordx4 v33, s[36:39], s59 offen lds
s_xor_b32 s73, s79, s75
v_mfma_f32_16x16x32_bf16 v[72:75], v[12:15], v[172:175], v[72:75]
ds_read_b64_tr_b16 v[12:13], v27 offset:4096
s_add_u32 m0, s57, 110592
s_add_u32 s59, s60, 196608
v_mfma_f32_16x16x32_bf16 v[76:79], v[16:19], v[172:175], v[76:79]
ds_read_b64_tr_b16 v[14:15], v27 offset:4608
buffer_load_dwordx4 v33, s[36:39], s59 offen lds
s_mul_i32 s73, s73, 49152
s_sub_u32 s75, s75, 1
v_mfma_f32_16x16x32_bf16 v[68:71], v[16:19], v[168:171], v[68:71]
s_waitcnt vmcnt(16)
s_barrier
ds_read_b128 v[176:179], v28 offset:49152
ds_read_b128 v[180:183], v28 offset:51200
ds_read_b64_tr_b16 v[16:17], v24 offset:8192
s_add_u32 m0, s57, 32768
s_add_u32 s59, s64, 229376
s_waitcnt lgkmcnt(7)
v_mfma_f32_16x16x32_bf16 v[80:83], v[20:23], v[168:171], v[80:83]
ds_read_b64_tr_b16 v[18:19], v24 offset:8704
buffer_load_dwordx4 v44, s[40:43], s59 offen lds
s_lshl_b32 s75, s75, 14
v_mfma_f32_16x16x32_bf16 v[88:91], v[20:23], v[172:175], v[88:91]
ds_read_b64_tr_b16 v[20:21], v25 offset:8192
s_add_u32 m0, s57, 36864
s_add_u32 s59, s64, 229504
v_mfma_f32_16x16x32_bf16 v[92:95], v[4:7], v[172:175], v[92:95]
ds_read_b64_tr_b16 v[22:23], v25 offset:8704
buffer_load_dwordx4 v44, s[40:43], s59 offen lds
s_add_u32 s73, s73, s75
v_mfma_f32_16x16x32_bf16 v[84:87], v[4:7], v[168:171], v[84:87]
ds_read_b64_tr_b16 v[4:5], v26 offset:8192
ds_read_b128 v[184:187], v29 offset:49152
s_add_u32 m0, s57, 73728
s_add_u32 s59, s64, 3899392
s_waitcnt lgkmcnt(8)
v_mfma_f32_16x16x32_bf16 v[96:99], v[8:11], v[168:171], v[96:99]
ds_read_b64_tr_b16 v[6:7], v26 offset:8704
buffer_load_dwordx4 v44, s[40:43], s59 offen lds
s_add_u32 s87, s73, s72
s_mov_b32 s82, 0xc0135761
v_mfma_f32_16x16x32_bf16 v[104:107], v[8:11], v[172:175], v[104:107]
ds_read_b64_tr_b16 v[8:9], v27 offset:8192
ds_read_b128 v[188:191], v29 offset:51200
s_add_u32 m0, s57, 77824
v_mfma_f32_16x16x32_bf16 v[108:111], v[12:15], v[172:175], v[108:111]
ds_read_b64_tr_b16 v[10:11], v27 offset:8704
s_add_u32 s59, s64, 3899520
buffer_load_dwordx4 v44, s[40:43], s59 offen lds
v_mov_b32 v32, 0xbdd2d3e8
v_mfma_f32_16x16x32_bf16 v[100:103], v[12:15], v[168:171], v[100:103]
ds_read_b64_tr_b16 v[12:13], v24 offset:12288
s_add_u32 s60, s66, 5
s_and_b32 s60, s60, 7
s_lshl_b32 s60, s60, 7
s_add_u32 m0, s57, 114688
s_waitcnt lgkmcnt(7)
v_mfma_f32_16x16x32_bf16 v[48:51], v[16:19], v[176:179], v[48:51]
ds_read_b64_tr_b16 v[14:15], v24 offset:12800
s_add_u32 s59, s60, 1024
buffer_load_dwordx4 v33, s[36:39], s59 offen lds
s_mov_b32 s83, 200000
s_mov_b32 s72, 0
v_mfma_f32_16x16x32_bf16 v[56:59], v[16:19], v[180:183], v[56:59]
ds_read_b64_tr_b16 v[16:17], v25 offset:12288
s_add_u32 m0, s57, 118784
s_add_u32 s59, s60, 66560
v_mfma_f32_16x16x32_bf16 v[60:63], v[20:23], v[180:183], v[60:63]
ds_read_b64_tr_b16 v[18:19], v25 offset:12800
buffer_load_dwordx4 v33, s[36:39], s59 offen lds
s_add_u32 s72, s72, 0
v_mfma_f32_16x16x32_bf16 v[52:55], v[20:23], v[176:179], v[52:55]
ds_read_b64_tr_b16 v[20:21], v26 offset:12288
s_add_u32 m0, s57, 122880
s_add_u32 s59, s60, 132096
s_waitcnt lgkmcnt(5)
v_mfma_f32_16x16x32_bf16 v[64:67], v[4:7], v[176:179], v[64:67]
ds_read_b64_tr_b16 v[22:23], v26 offset:12800
buffer_load_dwordx4 v33, s[36:39], s59 offen lds
s_add_u32 s81, s81, s72
s_add_u32 s88, s88, s72
v_mfma_f32_16x16x32_bf16 v[72:75], v[4:7], v[180:183], v[72:75]
ds_read_b64_tr_b16 v[4:5], v27 offset:12288
s_add_u32 m0, s57, 126976
s_add_u32 s59, s60, 197632
v_mfma_f32_16x16x32_bf16 v[76:79], v[8:11], v[180:183], v[76:79]
ds_read_b64_tr_b16 v[6:7], v27 offset:12800
buffer_load_dwordx4 v33, s[36:39], s59 offen lds
s_add_u32 s89, s89, s72
v_mfma_f32_16x16x32_bf16 v[68:71], v[8:11], v[176:179], v[68:71]
s_waitcnt vmcnt(16)
s_barrier
ds_read_b64_tr_b16 v[8:9], v24 offset:16384
s_add_u32 s64, s66, 5
s_and_b32 s64, s64, 7
s_mul_i32 s64, s64, 458752
s_add_u32 s64, s64, s67
s_add_u32 m0, s57, 0
s_waitcnt lgkmcnt(5)
v_mfma_f32_16x16x32_bf16 v[80:83], v[12:15], v[176:179], v[80:83]
ds_read_b64_tr_b16 v[10:11], v24 offset:16896
s_add_u32 s59, s64, 0
buffer_load_dwordx4 v44, s[40:43], s59 offen lds
s_add_u32 s90, s90, s72
s_add_u32 s92, s92, 0x7f4a7c15
s_addc_u32 s93, s93, 0x9e3779b9
v_mfma_f32_16x16x32_bf16 v[88:91], v[12:15], v[180:183], v[88:91]
ds_read_b64_tr_b16 v[12:13], v25 offset:16384
s_add_u32 m0, s57, 4096
s_add_u32 s59, s64, 128
v_mfma_f32_16x16x32_bf16 v[92:95], v[16:19], v[180:183], v[92:95]
ds_read_b64_tr_b16 v[14:15], v25 offset:16896
buffer_load_dwordx4 v44, s[40:43], s59 offen lds
)ASM"
R"ASM(
s_mul_i32 s92, s92, 0x85ebca6b
v_mfma_f32_16x16x32_bf16 v[84:87], v[16:19], v[176:179], v[84:87]
ds_read_b64_tr_b16 v[16:17], v26 offset:16384
s_add_u32 m0, s57, 40960
s_add_u32 s59, s64, 3670016
s_waitcnt lgkmcnt(5)
v_mfma_f32_16x16x32_bf16 v[96:99], v[20:23], v[176:179], v[96:99]
ds_read_b64_tr_b16 v[18:19], v26 offset:16896
buffer_load_dwordx4 v44, s[40:43], s59 offen lds
s_lshr_b32 s75, s92, 13
v_mfma_f32_16x16x32_bf16 v[104:107], v[20:23], v[180:183], v[104:107]
ds_read_b64_tr_b16 v[20:21], v27 offset:16384
s_add_u32 m0, s57, 45056
s_add_u32 s59, s64, 3670144
v_mfma_f32_16x16x32_bf16 v[108:111], v[4:7], v[180:183], v[108:111]
ds_read_b64_tr_b16 v[22:23], v27 offset:16896
buffer_load_dwordx4 v44, s[40:43], s59 offen lds
s_xor_b32 s92, s92, s75
s_xor_b32 s93, s93, s92
v_mfma_f32_16x16x32_bf16 v[100:103], v[4:7], v[176:179], v[100:103]
ds_read_b64_tr_b16 v[4:5], v24 offset:20480
s_add_u32 s60, s66, 5
s_and_b32 s60, s60, 7
s_lshl_b32 s60, s60, 7
s_waitcnt lgkmcnt(5)
v_mfma_f32_16x16x32_bf16 v[48:51], v[8:11], v[184:187], v[48:51]
ds_read_b64_tr_b16 v[6:7], v24 offset:20992
s_add_u32 m0, s57, 131072
s_add_u32 s59, s60, 0
buffer_load_dwordx4 v33, s[36:39], s59 offen lds
s_mul_i32 s93, s93, 0xc2b2ae35
v_mfma_f32_16x16x32_bf16 v[56:59], v[8:11], v[188:191], v[56:59]
ds_read_b64_tr_b16 v[8:9], v25 offset:20480
s_add_u32 m0, s57, 135168
s_add_u32 s59, s60, 65536
v_mfma_f32_16x16x32_bf16 v[60:63], v[12:15], v[188:191], v[60:63]
ds_read_b64_tr_b16 v[10:11], v25 offset:20992
buffer_load_dwordx4 v33, s[36:39], s59 offen lds
s_lshr_b32 s75, s93, 16
s_xor_b32 s93, s93, s75
v_mfma_f32_16x16x32_bf16 v[52:55], v[12:15], v[184:187], v[52:55]
ds_read_b64_tr_b16 v[12:13], v26 offset:20480
s_add_u32 m0, s57, 139264
s_add_u32 s59, s60, 131072
s_waitcnt lgkmcnt(5)
v_mfma_f32_16x16x32_bf16 v[64:67], v[16:19], v[184:187], v[64:67]
ds_read_b64_tr_b16 v[14:15], v26 offset:20992
buffer_load_dwordx4 v33, s[36:39], s59 offen lds
s_xor_b32 s92, s92, s93
v_mfma_f32_16x16x32_bf16 v[72:75], v[16:19], v[188:191], v[72:75]
ds_read_b64_tr_b16 v[16:17], v27 offset:20480
s_add_u32 m0, s57, 143360
s_add_u32 s59, s60, 196608
v_mfma_f32_16x16x32_bf16 v[76:79], v[20:23], v[188:191], v[76:79]
ds_read_b64_tr_b16 v[18:19], v27 offset:20992
buffer_load_dwordx4 v33, s[36:39], s59 offen lds
s_mul_i32 s92, s92, 0x27d4eb2f
s_lshr_b32 s75, s92, 15
v_mfma_f32_16x16x32_bf16 v[68:71], v[20:23], v[184:187], v[68:71]
s_waitcnt vmcnt(16)
s_barrier
ds_read_b128 v[192:195], v28 offset:16384
ds_read_b128 v[196:199], v28 offset:18432
ds_read_b64_tr_b16 v[20:21], v24 offset:24576
s_add_u32 m0, s57, 8192
s_add_u32 s59, s64, 229376
s_waitcnt lgkmcnt(7)
v_mfma_f32_16x16x32_bf16 v[80:83], v[4:7], v[184:187], v[80:83]
ds_read_b64_tr_b16 v[22:23], v24 offset:25088
buffer_load_dwordx4 v44, s[40:43], s59 offen lds
s_xor_b32 s92, s92, s75
v_mfma_f32_16x16x32_bf16 v[88:91], v[4:7], v[188:191], v[88:91]
ds_read_b64_tr_b16 v[4:5], v25 offset:24576
s_add_u32 m0, s57, 12288
s_add_u32 s59, s64, 229504
v_mfma_f32_16x16x32_bf16 v[92:95], v[8:11], v[188:191], v[92:95]
ds_read_b64_tr_b16 v[6:7], v25 offset:25088
buffer_load_dwordx4 v44, s[40:43], s59 offen lds
s_add_u32 s94, s94, 0x7f4a7c15
s_addc_u32 s95, s95, 0x9e3779b9
v_mfma_f32_16x16x32_bf16 v[84:87], v[8:11], v[184:187], v[84:87]
ds_read_b64_tr_b16 v[8:9], v26 offset:24576
ds_read_b128 v[200:203], v29 offset:16384
s_add_u32 m0, s57, 49152
s_add_u32 s59, s64, 3899392
s_waitcnt lgkmcnt(8)
v_mfma_f32_16x16x32_bf16 v[96:99], v[12:15], v[184:187], v[96:99]
ds_read_b64_tr_b16 v[10:11], v26 offset:25088
buffer_load_dwordx4 v44, s[40:43], s59 offen lds
s_mul_i32 s94, s94, 0x85ebca6b
s_lshr_b32 s75, s94, 13
v_mfma_f32_16x16x32_bf16 v[104:107], v[12:15], v[188:191], v[104:107]
ds_read_b64_tr_b16 v[12:13], v27 offset:24576
ds_read_b128 v[204:207], v29 offset:18432
s_add_u32 m0, s57, 53248
v_mfma_f32_16x16x32_bf16 v[108:111], v[16:19], v[188:191], v[108:111]
ds_read_b64_tr_b16 v[14:15], v27 offset:25088
s_add_u32 s59, s64, 3899520
buffer_load_dwordx4 v44, s[40:43], s59 offen lds
s_xor_b32 s94, s94, s75
v_mfma_f32_16x16x32_bf16 v[100:103], v[16:19], v[184:187], v[100:103]
ds_read_b64_tr_b16 v[16:17], v24 offset:28672
s_add_u32 s60, s63, 6
s_and_b32 s60, s60, 7
s_lshl_b32 s60, s60, 7
s_add_u32 m0, s57, 81920
s_waitcnt lgkmcnt(7)
v_mfma_f32_16x16x32_bf16 v[48:51], v[20:23], v[192:195], v[48:51]
ds_read_b64_tr_b16 v[18:19], v24 offset:29184
s_add_u32 s59, s60, 1024
buffer_load_dwordx4 v33, s[36:39], s59 offen lds
s_xor_b32 s95, s95, s94
s_mul_i32 s95, s95, 0xc2b2ae35
v_mfma_f32_16x16x32_bf16 v[56:59], v[20:23], v[196:199], v[56:59]
ds_read_b64_tr_b16 v[20:21], v25 offset:28672
s_add_u32 m0, s57, 86016
s_add_u32 s59, s60, 66560
v_mfma_f32_16x16x32_bf16 v[60:63], v[4:7], v[196:199], v[60:63]
ds_read_b64_tr_b16 v[22:23], v25 offset:29184
buffer_load_dwordx4 v33, s[36:39], s59 offen lds
s_lshr_b32 s75, s95, 16
v_mfma_f32_16x16x32_bf16 v[52:55], v[4:7], v[192:195], v[52:55]
ds_read_b64_tr_b16 v[4:5], v26 offset:28672
s_add_u32 m0, s57, 90112
s_add_u32 s59, s60, 132096
s_waitcnt lgkmcnt(5)
v_mfma_f32_16x16x32_bf16 v[64:67], v[8:11], v[192:195], v[64:67]
ds_read_b64_tr_b16 v[6:7], v26 offset:29184
buffer_load_dwordx4 v33, s[36:39], s59 offen lds
s_xor_b32 s95, s95, s75
s_xor_b32 s94, s94, s95
v_mfma_f32_16x16x32_bf16 v[72:75], v[8:11], v[196:199], v[72:75]
ds_read_b64_tr_b16 v[8:9], v27 offset:28672
s_add_u32 m0, s57, 94208
s_add_u32 s59, s60, 197632
v_mfma_f32_16x16x32_bf16 v[76:79], v[12:15], v[196:199], v[76:79]
ds_read_b64_tr_b16 v[10:11], v27 offset:29184
buffer_load_dwordx4 v33, s[36:39], s59 offen lds
s_mul_i32 s94, s94, 0x27d4eb2f
v_mfma_f32_16x16x32_bf16 v[68:71], v[12:15], v[192:195], v[68:71]
s_waitcnt vmcnt(16)
s_barrier
ds_read_b64_tr_b16 v[12:13], v24 offset:32768
s_add_u32 s64, s63, 6
s_and_b32 s64, s64, 7
s_mul_i32 s64, s64, 458752
s_add_u32 s64, s64, s67
s_waitcnt lgkmcnt(5)
v_mfma_f32_16x16x32_bf16 v[80:83], v[16:19], v[192:195], v[80:83]
ds_read_b64_tr_b16 v[14:15], v24 offset:33280
s_add_u32 m0, s57, 16384
s_add_u32 s59, s64, 0
buffer_load_dwordx4 v44, s[40:43], s59 offen lds
s_lshr_b32 s75, s94, 15
v_mfma_f32_16x16x32_bf16 v[88:91], v[16:19], v[196:199], v[88:91]
ds_read_b64_tr_b16 v[16:17], v25 offset:32768
s_add_u32 m0, s57, 20480
s_add_u32 s59, s64, 128
buffer_load_dwordx4 v44, s[40:43], s59 offen lds
v_mfma_f32_16x16x32_bf16 v[92:95], v[20:23], v[196:199], v[92:95]
ds_read_b64_tr_b16 v[18:19], v25 offset:33280
s_xor_b32 s94, s94, s75
s_add_u32 s96, s96, 0x7f4a7c15
s_addc_u32 s97, s97, 0x9e3779b9
v_mfma_f32_16x16x32_bf16 v[84:87], v[20:23], v[192:195], v[84:87]
ds_read_b64_tr_b16 v[20:21], v26 offset:32768
s_add_u32 m0, s57, 57344
s_add_u32 s59, s64, 3670016
s_waitcnt lgkmcnt(5)
v_mfma_f32_16x16x32_bf16 v[96:99], v[4:7], v[192:195], v[96:99]
ds_read_b64_tr_b16 v[22:23], v26 offset:33280
buffer_load_dwordx4 v44, s[40:43], s59 offen lds
s_mul_i32 s96, s96, 0x85ebca6b
v_mfma_f32_16x16x32_bf16 v[104:107], v[4:7], v[196:199], v[104:107]
ds_read_b64_tr_b16 v[4:5], v27 offset:32768
s_add_u32 m0, s57, 61440
s_add_u32 s59, s64, 3670144
v_mfma_f32_16x16x32_bf16 v[108:111], v[8:11], v[196:199], v[108:111]
ds_read_b64_tr_b16 v[6:7], v27 offset:33280
buffer_load_dwordx4 v44, s[40:43], s59 offen lds
s_lshr_b32 s75, s96, 13
s_xor_b32 s96, s96, s75
v_mfma_f32_16x16x32_bf16 v[100:103], v[8:11], v[192:195], v[100:103]
ds_read_b64_tr_b16 v[8:9], v24 offset:36864
s_add_u32 s60, s63, 6
s_and_b32 s60, s60, 7
s_lshl_b32 s60, s60, 7
s_waitcnt lgkmcnt(5)
v_mfma_f32_16x16x32_bf16 v[48:51], v[12:15], v[200:203], v[48:51]
ds_read_b64_tr_b16 v[10:11], v24 offset:37376
s_add_u32 m0, s57, 98304
s_add_u32 s59, s60, 0
buffer_load_dwordx4 v33, s[36:39], s59 offen lds
s_xor_b32 s97, s97, s96
v_mfma_f32_16x16x32_bf16 v[56:59], v[12:15], v[204:207], v[56:59]
ds_read_b64_tr_b16 v[12:13], v25 offset:36864
s_add_u32 m0, s57, 102400
s_add_u32 s59, s60, 65536
v_mfma_f32_16x16x32_bf16 v[60:63], v[16:19], v[204:207], v[60:63]
ds_read_b64_tr_b16 v[14:15], v25 offset:37376
)ASM"
R"ASM(
buffer_load_dwordx4 v33, s[36:39], s59 offen lds
s_mul_i32 s97, s97, 0xc2b2ae35
s_lshr_b32 s75, s97, 16
v_mfma_f32_16x16x32_bf16 v[52:55], v[16:19], v[200:203], v[52:55]
ds_read_b64_tr_b16 v[16:17], v26 offset:36864
s_add_u32 m0, s57, 106496
s_add_u32 s59, s60, 131072
s_waitcnt lgkmcnt(5)
v_mfma_f32_16x16x32_bf16 v[64:67], v[20:23], v[200:203], v[64:67]
ds_read_b64_tr_b16 v[18:19], v26 offset:37376
buffer_load_dwordx4 v33, s[36:39], s59 offen lds
s_xor_b32 s97, s97, s75
v_mfma_f32_16x16x32_bf16 v[72:75], v[20:23], v[204:207], v[72:75]
ds_read_b64_tr_b16 v[20:21], v27 offset:36864
s_add_u32 m0, s57, 110592
s_add_u32 s59, s60, 196608
v_mfma_f32_16x16x32_bf16 v[76:79], v[4:7], v[204:207], v[76:79]
ds_read_b64_tr_b16 v[22:23], v27 offset:37376
buffer_load_dwordx4 v33, s[36:39], s59 offen lds
s_xor_b32 s96, s96, s97
s_mul_i32 s96, s96, 0x27d4eb2f
v_mfma_f32_16x16x32_bf16 v[68:71], v[4:7], v[200:203], v[68:71]
s_waitcnt vmcnt(16)
s_barrier
ds_read_b128 v[208:211], v28 offset:49152
ds_read_b128 v[212:215], v28 offset:51200
ds_read_b64_tr_b16 v[4:5], v24 offset:0
s_add_u32 m0, s57, 24576
s_add_u32 s59, s64, 229376
s_waitcnt lgkmcnt(7)
v_mfma_f32_16x16x32_bf16 v[80:83], v[8:11], v[200:203], v[80:83]
ds_read_b64_tr_b16 v[6:7], v24 offset:512
buffer_load_dwordx4 v44, s[40:43], s59 offen lds
s_lshr_b32 s75, s96, 15
v_mfma_f32_16x16x32_bf16 v[88:91], v[8:11], v[204:207], v[88:91]
ds_read_b64_tr_b16 v[8:9], v25 offset:0
s_add_u32 m0, s57, 28672
s_add_u32 s59, s64, 229504
v_mfma_f32_16x16x32_bf16 v[92:95], v[12:15], v[204:207], v[92:95]
ds_read_b64_tr_b16 v[10:11], v25 offset:512
buffer_load_dwordx4 v44, s[40:43], s59 offen lds
s_xor_b32 s96, s96, s75
v_mfma_f32_16x16x32_bf16 v[84:87], v[12:15], v[200:203], v[84:87]
ds_read_b64_tr_b16 v[12:13], v26 offset:0
ds_read_b128 v[216:219], v29 offset:49152
s_add_u32 m0, s57, 65536
s_add_u32 s59, s64, 3899392
s_waitcnt lgkmcnt(8)
v_mfma_f32_16x16x32_bf16 v[96:99], v[16:19], v[200:203], v[96:99]
ds_read_b64_tr_b16 v[14:15], v26 offset:512
buffer_load_dwordx4 v44, s[40:43], s59 offen lds
s_add_u32 s98, s98, 0x7f4a7c15
s_addc_u32 s99, s99, 0x9e3779b9
s_mul_i32 s98, s98, 0x85ebca6b
v_mfma_f32_16x16x32_bf16 v[104:107], v[16:19], v[204:207], v[104:107]
ds_read_b64_tr_b16 v[16:17], v27 offset:0
ds_read_b128 v[220:223], v29 offset:51200
s_add_u32 m0, s57, 69632
v_mfma_f32_16x16x32_bf16 v[108:111], v[20:23], v[204:207], v[108:111]
ds_read_b64_tr_b16 v[18:19], v27 offset:512
s_add_u32 s59, s64, 3899520
buffer_load_dwordx4 v44, s[40:43], s59 offen lds
s_lshr_b32 s75, s98, 13
v_mfma_f32_16x16x32_bf16 v[100:103], v[20:23], v[200:203], v[100:103]
ds_read_b64_tr_b16 v[20:21], v24 offset:4096
s_add_u32 s60, s66, 7
s_and_b32 s60, s60, 7
s_lshl_b32 s60, s60, 7
s_add_u32 m0, s57, 114688
s_waitcnt lgkmcnt(7)
v_mfma_f32_16x16x32_bf16 v[48:51], v[4:7], v[208:211], v[48:51]
ds_read_b64_tr_b16 v[22:23], v24 offset:4608
s_add_u32 s59, s60, 1024
buffer_load_dwordx4 v33, s[36:39], s59 offen lds
s_xor_b32 s98, s98, s75
s_xor_b32 s99, s99, s98
v_mfma_f32_16x16x32_bf16 v[56:59], v[4:7], v[212:215], v[56:59]
ds_read_b64_tr_b16 v[4:5], v25 offset:4096
s_add_u32 m0, s57, 118784
s_add_u32 s59, s60, 66560
v_mfma_f32_16x16x32_bf16 v[60:63], v[8:11], v[212:215], v[60:63]
ds_read_b64_tr_b16 v[6:7], v25 offset:4608
buffer_load_dwordx4 v33, s[36:39], s59 offen lds
s_mul_i32 s99, s99, 0xc2b2ae35
v_mfma_f32_16x16x32_bf16 v[52:55], v[8:11], v[208:211], v[52:55]
ds_read_b64_tr_b16 v[8:9], v26 offset:4096
s_add_u32 m0, s57, 122880
s_add_u32 s59, s60, 132096
s_waitcnt lgkmcnt(5)
v_mfma_f32_16x16x32_bf16 v[64:67], v[12:15], v[208:211], v[64:67]
ds_read_b64_tr_b16 v[10:11], v26 offset:4608
buffer_load_dwordx4 v33, s[36:39], s59 offen lds
s_lshr_b32 s75, s99, 16
s_xor_b32 s99, s99, s75
v_mfma_f32_16x16x32_bf16 v[72:75], v[12:15], v[212:215], v[72:75]
ds_read_b64_tr_b16 v[12:13], v27 offset:4096
s_add_u32 m0, s57, 126976
s_add_u32 s59, s60, 197632
v_mfma_f32_16x16x32_bf16 v[76:79], v[16:19], v[212:215], v[76:79]
ds_read_b64_tr_b16 v[14:15], v27 offset:4608
buffer_load_dwordx4 v33, s[36:39], s59 offen lds
s_xor_b32 s98, s98, s99
v_mfma_f32_16x16x32_bf16 v[68:71], v[16:19], v[208:211], v[68:71]
s_waitcnt vmcnt(16)
s_barrier
ds_read_b64_tr_b16 v[16:17], v24 offset:8192
s_add_u32 s64, s66, 7
s_and_b32 s64, s64, 7
s_mul_i32 s64, s64, 458752
s_add_u32 s64, s64, s67
s_waitcnt lgkmcnt(5)
v_mfma_f32_16x16x32_bf16 v[80:83], v[20:23], v[208:211], v[80:83]
ds_read_b64_tr_b16 v[18:19], v24 offset:8704
s_add_u32 m0, s57, 32768
s_add_u32 s59, s64, 0
buffer_load_dwordx4 v44, s[40:43], s59 offen lds
s_mul_i32 s98, s98, 0x27d4eb2f
v_mfma_f32_16x16x32_bf16 v[88:91], v[20:23], v[212:215], v[88:91]
ds_read_b64_tr_b16 v[20:21], v25 offset:8192
s_lshr_b32 s75, s98, 15
v_mfma_f32_16x16x32_bf16 v[92:95], v[4:7], v[212:215], v[92:95]
ds_read_b64_tr_b16 v[22:23], v25 offset:8704
s_xor_b32 s98, s98, s75
v_mfma_f32_16x16x32_bf16 v[84:87], v[4:7], v[208:211], v[84:87]
ds_read_b64_tr_b16 v[4:5], v26 offset:8192
s_add_u32 m0, s57, 36864
s_add_u32 s59, s64, 128
s_waitcnt lgkmcnt(5)
v_mfma_f32_16x16x32_bf16 v[96:99], v[8:11], v[208:211], v[96:99]
ds_read_b64_tr_b16 v[6:7], v26 offset:8704
buffer_load_dwordx4 v44, s[40:43], s59 offen lds
v_mov_b32 v2, s92
v_mfma_f32_16x16x32_bf16 v[104:107], v[8:11], v[212:215], v[104:107]
ds_read_b64_tr_b16 v[8:9], v27 offset:8192
v_mov_b32 v3, s93
v_mfma_f32_16x16x32_bf16 v[108:111], v[12:15], v[212:215], v[108:111]
ds_read_b64_tr_b16 v[10:11], v27 offset:8704
s_lshr_b32 s79, s56, 1
v_mfma_f32_16x16x32_bf16 v[100:103], v[12:15], v[208:211], v[100:103]
ds_read_b64_tr_b16 v[12:13], v24 offset:12288
s_add_u32 m0, s57, 73728
s_add_u32 s59, s64, 3670016
s_waitcnt lgkmcnt(5)
v_mfma_f32_16x16x32_bf16 v[48:51], v[16:19], v[216:219], v[48:51]
ds_read_b64_tr_b16 v[14:15], v24 offset:12800
buffer_load_dwordx4 v44, s[40:43], s59 offen lds
s_lshl_b32 s79, s79, 12
v_mfma_f32_16x16x32_bf16 v[56:59], v[16:19], v[220:223], v[56:59]
ds_read_b64_tr_b16 v[16:17], v25 offset:12288
s_and_b32 s72, s56, 1
v_mfma_f32_16x16x32_bf16 v[60:63], v[20:23], v[220:223], v[60:63]
ds_read_b64_tr_b16 v[18:19], v25 offset:12800
s_lshl_b32 s72, s72, 6
v_mfma_f32_16x16x32_bf16 v[52:55], v[20:23], v[216:219], v[52:55]
ds_read_b64_tr_b16 v[20:21], v26 offset:12288
s_add_u32 m0, s57, 77824
s_add_u32 s59, s64, 3670144
s_waitcnt lgkmcnt(5)
v_mfma_f32_16x16x32_bf16 v[64:67], v[4:7], v[216:219], v[64:67]
ds_read_b64_tr_b16 v[22:23], v26 offset:12800
buffer_load_dwordx4 v44, s[40:43], s59 offen lds
s_add_u32 s79, s79, s72
v_mfma_f32_16x16x32_bf16 v[72:75], v[4:7], v[220:223], v[72:75]
ds_read_b64_tr_b16 v[4:5], v27 offset:12288
v_mfma_f32_16x16x32_bf16 v[76:79], v[8:11], v[220:223], v[76:79]
ds_read_b64_tr_b16 v[6:7], v27 offset:12800
s_add_u32 s79, s79, 81920
v_mfma_f32_16x16x32_bf16 v[68:71], v[8:11], v[216:219], v[68:71]
s_waitcnt vmcnt(12)
s_barrier
ds_read_b128 v[224:227], v28 offset:16384
ds_read_b128 v[228:231], v28 offset:18432
ds_read_b64_tr_b16 v[8:9], v24 offset:16384
s_add_u32 s60, s66, 7
s_and_b32 s60, s60, 7
s_lshl_b32 s60, s60, 7
s_waitcnt lgkmcnt(7)
v_mfma_f32_16x16x32_bf16 v[80:83], v[12:15], v[216:219], v[80:83]
ds_read_b64_tr_b16 v[10:11], v24 offset:16896
s_add_u32 m0, s57, 81920
s_add_u32 s59, s60, 0
buffer_load_dwordx4 v33, s[36:39], s59 offen lds
v_mfma_f32_16x16x32_bf16 v[88:91], v[12:15], v[220:223], v[88:91]
ds_read_b64_tr_b16 v[12:13], v25 offset:16384
s_add_u32 m0, s57, 86016
v_mfma_f32_16x16x32_bf16 v[92:95], v[16:19], v[220:223], v[92:95]
ds_read_b64_tr_b16 v[14:15], v25 offset:16896
s_add_u32 s59, s60, 65536
buffer_load_dwordx4 v33, s[36:39], s59 offen lds
v_mfma_f32_16x16x32_bf16 v[84:87], v[16:19], v[216:219], v[84:87]
ds_read_b64_tr_b16 v[16:17], v26 offset:16384
ds_read_b128 v[232:235], v29 offset:16384
s_add_u32 m0, s57, 90112
s_waitcnt lgkmcnt(8)
v_mfma_f32_16x16x32_bf16 v[96:99], v[20:23], v[216:219], v[96:99]
ds_read_b64_tr_b16 v[18:19], v26 offset:16896
s_add_u32 s59, s60, 131072
buffer_load_dwordx4 v33, s[36:39], s59 offen lds
v_mfma_f32_16x16x32_bf16 v[104:107], v[20:23], v[220:223], v[104:107]
ds_read_b64_tr_b16 v[20:21], v27 offset:16384
)ASM"
R"ASM(
ds_read_b128 v[236:239], v29 offset:18432
s_add_u32 m0, s57, 94208
v_mfma_f32_16x16x32_bf16 v[108:111], v[4:7], v[220:223], v[108:111]
ds_read_b64_tr_b16 v[22:23], v27 offset:16896
s_add_u32 s59, s60, 196608
buffer_load_dwordx4 v33, s[36:39], s59 offen lds
v_mfma_f32_16x16x32_bf16 v[100:103], v[4:7], v[216:219], v[100:103]
ds_read_b64_tr_b16 v[4:5], v24 offset:20480
s_add_u32 m0, s57, 0
s_waitcnt lgkmcnt(7)
v_mfma_f32_16x16x32_bf16 v[48:51], v[8:11], v[224:227], v[48:51]
ds_read_b64_tr_b16 v[6:7], v24 offset:20992
s_add_u32 s59, s64, 229376
buffer_load_dwordx4 v44, s[40:43], s59 offen lds
v_mfma_f32_16x16x32_bf16 v[56:59], v[8:11], v[228:231], v[56:59]
ds_read_b64_tr_b16 v[8:9], v25 offset:20480
s_add_u32 m0, s57, 4096
v_mfma_f32_16x16x32_bf16 v[60:63], v[12:15], v[228:231], v[60:63]
ds_read_b64_tr_b16 v[10:11], v25 offset:20992
s_add_u32 s59, s64, 229504
buffer_load_dwordx4 v44, s[40:43], s59 offen lds
v_mfma_f32_16x16x32_bf16 v[52:55], v[12:15], v[224:227], v[52:55]
ds_read_b64_tr_b16 v[12:13], v26 offset:20480
s_add_u32 m0, s57, 40960
s_waitcnt lgkmcnt(5)
v_mfma_f32_16x16x32_bf16 v[64:67], v[16:19], v[224:227], v[64:67]
ds_read_b64_tr_b16 v[14:15], v26 offset:20992
s_add_u32 s59, s64, 3899392
buffer_load_dwordx4 v44, s[40:43], s59 offen lds
v_mfma_f32_16x16x32_bf16 v[72:75], v[16:19], v[228:231], v[72:75]
ds_read_b64_tr_b16 v[16:17], v27 offset:20480
s_add_u32 m0, s57, 45056
v_mfma_f32_16x16x32_bf16 v[76:79], v[20:23], v[228:231], v[76:79]
ds_read_b64_tr_b16 v[18:19], v27 offset:20992
s_add_u32 s59, s64, 3899520
buffer_load_dwordx4 v44, s[40:43], s59 offen lds
v_mfma_f32_16x16x32_bf16 v[68:71], v[20:23], v[224:227], v[68:71]
s_waitcnt vmcnt(12)
s_barrier
ds_read_b64_tr_b16 v[20:21], v24 offset:24576
s_add_u32 s67, s67, s68
s_add_u32 s64, s63, 0
s_and_b32 s64, s64, 7
s_mul_i32 s64, s64, 458752
s_add_u32 s64, s64, s67
s_add_u32 s65, s66, 1
s_waitcnt lgkmcnt(5)
v_mfma_f32_16x16x32_bf16 v[80:83], v[4:7], v[224:227], v[80:83]
ds_read_b64_tr_b16 v[22:23], v24 offset:25088
s_and_b32 s65, s65, 7
s_mul_i32 s65, s65, 458752
s_add_u32 s65, s65, s67
s_add_u32 m0, s57, 8192
s_add_u32 s59, s64, 0
buffer_load_dwordx4 v45, s[40:43], s59 offen lds
v_mfma_f32_16x16x32_bf16 v[88:91], v[4:7], v[228:231], v[88:91]
ds_read_b64_tr_b16 v[4:5], v25 offset:24576
v_mfma_f32_16x16x32_bf16 v[92:95], v[8:11], v[228:231], v[92:95]
ds_read_b64_tr_b16 v[6:7], v25 offset:25088
v_mfma_f32_16x16x32_bf16 v[84:87], v[8:11], v[224:227], v[84:87]
ds_read_b64_tr_b16 v[8:9], v26 offset:24576
s_add_u32 m0, s57, 12288
s_add_u32 s59, s65, 0
buffer_load_dwordx4 v45, s[40:43], s59 offen lds
s_lshl_b32 s72, s77, 2
s_add_u32 s72, s72, s76
s_mul_i32 s72, s72, 458752
s_add_u32 s72, s72, 2097152
s_lshr_b32 s73, s76, 1
s_mul_i32 s73, s73, 229376
s_sub_u32 s60, s72, s73
s_add_u32 s60, s60, 229376
s_add_u32 s72, s72, s73
s_waitcnt lgkmcnt(5)
v_mfma_f32_16x16x32_bf16 v[96:99], v[12:15], v[224:227], v[96:99]
ds_read_b64_tr_b16 v[10:11], v26 offset:25088
s_lshr_b32 s73, s56, 1
s_mul_i32 s73, s73, 114688
s_add_u32 s60, s60, s73
s_and_b32 s73, s56, 1
s_lshl_b32 s73, s73, 12
s_add_u32 s60, s60, s73
s_mul_i32 s73, s56, 57344
s_add_u32 s72, s72, s73
s_and_b32 s73, s76, 1
s_lshl_b32 s73, s73, 12
s_add_u32 s62, s72, s73
s_xor_b32 s73, s73, 4096
s_add_u32 s61, s72, s73
v_mfma_f32_16x16x32_bf16 v[104:107], v[12:15], v[228:231], v[104:107]
ds_read_b64_tr_b16 v[12:13], v27 offset:24576
v_mfma_f32_16x16x32_bf16 v[108:111], v[16:19], v[228:231], v[108:111]
ds_read_b64_tr_b16 v[14:15], v27 offset:25088
v_mfma_f32_16x16x32_bf16 v[100:103], v[16:19], v[224:227], v[100:103]
ds_read_b64_tr_b16 v[16:17], v24 offset:28672
s_add_u32 m0, s57, 49152
s_waitcnt lgkmcnt(5)
v_mfma_f32_16x16x32_bf16 v[48:51], v[20:23], v[232:235], v[48:51]
ds_read_b64_tr_b16 v[18:19], v24 offset:29184
s_add_u32 s59, s64, 3670016
buffer_load_dwordx4 v45, s[40:43], s59 offen lds
v_mfma_f32_16x16x32_bf16 v[56:59], v[20:23], v[236:239], v[56:59]
ds_read_b64_tr_b16 v[20:21], v25 offset:28672
v_mfma_f32_16x16x32_bf16 v[60:63], v[4:7], v[236:239], v[60:63]
ds_read_b64_tr_b16 v[22:23], v25 offset:29184
v_mfma_f32_16x16x32_bf16 v[52:55], v[4:7], v[232:235], v[52:55]
ds_read_b64_tr_b16 v[4:5], v26 offset:28672
s_add_u32 m0, s57, 53248
s_waitcnt lgkmcnt(5)
v_mfma_f32_16x16x32_bf16 v[64:67], v[8:11], v[232:235], v[64:67]
ds_read_b64_tr_b16 v[6:7], v26 offset:29184
s_add_u32 s59, s65, 3670016
buffer_load_dwordx4 v45, s[40:43], s59 offen lds
v_mfma_f32_16x16x32_bf16 v[72:75], v[8:11], v[236:239], v[72:75]
ds_read_b64_tr_b16 v[8:9], v27 offset:28672
v_mfma_f32_16x16x32_bf16 v[76:79], v[12:15], v[236:239], v[76:79]
ds_read_b64_tr_b16 v[10:11], v27 offset:29184
v_mfma_f32_16x16x32_bf16 v[68:71], v[12:15], v[232:235], v[68:71]
s_waitcnt vmcnt(8)
s_barrier
ds_read_b128 v[240:243], v28 offset:0
ds_read_b128 v[244:247], v28 offset:2048
ds_read_b64_tr_b16 v[12:13], v24 offset:32768
s_add_u32 s64, s63, 2
s_and_b32 s64, s64, 7
s_mul_i32 s64, s64, 458752
s_add_u32 s64, s64, s67
s_add_u32 s65, s66, 3
s_waitcnt lgkmcnt(7)
v_mfma_f32_16x16x32_bf16 v[80:83], v[16:19], v[232:235], v[80:83]
ds_read_b64_tr_b16 v[14:15], v24 offset:33280
s_and_b32 s65, s65, 7
s_mul_i32 s65, s65, 458752
s_add_u32 s65, s65, s67
s_add_u32 m0, s57, 16384
s_add_u32 s59, s64, 0
buffer_load_dwordx4 v45, s[40:43], s59 offen lds
v_mfma_f32_16x16x32_bf16 v[88:91], v[16:19], v[236:239], v[88:91]
ds_read_b64_tr_b16 v[16:17], v25 offset:32768
v_mfma_f32_16x16x32_bf16 v[92:95], v[20:23], v[236:239], v[92:95]
ds_read_b64_tr_b16 v[18:19], v25 offset:33280
v_mfma_f32_16x16x32_bf16 v[84:87], v[20:23], v[232:235], v[84:87]
ds_read_b64_tr_b16 v[20:21], v26 offset:32768
ds_read_b128 v[248:251], v29 offset:0
s_add_u32 m0, s57, 20480
s_waitcnt lgkmcnt(8)
v_mfma_f32_16x16x32_bf16 v[96:99], v[4:7], v[232:235], v[96:99]
ds_read_b64_tr_b16 v[22:23], v26 offset:33280
s_add_u32 s59, s65, 0
buffer_load_dwordx4 v45, s[40:43], s59 offen lds
v_mfma_f32_16x16x32_bf16 v[104:107], v[4:7], v[236:239], v[104:107]
ds_read_b64_tr_b16 v[4:5], v27 offset:32768
v_mfma_f32_16x16x32_bf16 v[108:111], v[8:11], v[236:239], v[108:111]
ds_read_b64_tr_b16 v[6:7], v27 offset:33280
ds_read_b128 v[252:255], v29 offset:2048
v_mfma_f32_16x16x32_bf16 v[100:103], v[8:11], v[232:235], v[100:103]
ds_read_b64_tr_b16 v[8:9], v24 offset:36864
s_add_u32 m0, s57, 57344
s_waitcnt lgkmcnt(7)
v_mfma_f32_16x16x32_bf16 v[48:51], v[12:15], v[240:243], v[48:51]
ds_read_b64_tr_b16 v[10:11], v24 offset:37376
s_add_u32 s59, s64, 3670016
buffer_load_dwordx4 v45, s[40:43], s59 offen lds
v_mfma_f32_16x16x32_bf16 v[56:59], v[12:15], v[244:247], v[56:59]
ds_read_b64_tr_b16 v[12:13], v25 offset:36864
v_mfma_f32_16x16x32_bf16 v[60:63], v[16:19], v[244:247], v[60:63]
ds_read_b64_tr_b16 v[14:15], v25 offset:37376
v_mfma_f32_16x16x32_bf16 v[52:55], v[16:19], v[240:243], v[52:55]
ds_read_b64_tr_b16 v[16:17], v26 offset:36864
s_add_u32 m0, s57, 61440
s_waitcnt lgkmcnt(6)
v_mfma_f32_16x16x32_bf16 v[64:67], v[20:23], v[240:243], v[64:67]
ds_read_b64_tr_b16 v[18:19], v26 offset:37376
s_add_u32 s59, s65, 3670016
buffer_load_dwordx4 v45, s[40:43], s59 offen lds
v_mfma_f32_16x16x32_bf16 v[72:75], v[20:23], v[244:247], v[72:75]
ds_read_b64_tr_b16 v[20:21], v27 offset:36864
v_mfma_f32_16x16x32_bf16 v[76:79], v[4:7], v[244:247], v[76:79]
ds_read_b64_tr_b16 v[22:23], v27 offset:37376
v_mfma_f32_16x16x32_bf16 v[68:71], v[4:7], v[240:243], v[68:71]
s_waitcnt lgkmcnt(8)
s_waitcnt vmcnt(8)
s_barrier
ds_read_b64_tr_b16 v[4:5], v24 offset:0
v_bfe_u32 v1, v0, 4, 2
v_lshlrev_b32 v28, 9, v1
s_add_u32 s64, s63, 4
s_and_b32 s64, s64, 7
s_mul_i32 s64, s64, 458752
s_add_u32 s64, s64, s67
s_waitcnt lgkmcnt(5)
v_mfma_f32_16x16x32_bf16 v[80:83], v[8:11], v[240:243], v[80:83]
ds_read_b64_tr_b16 v[6:7], v24 offset:512
s_add_u32 s65, s66, 5
s_and_b32 s65, s65, 7
s_mul_i32 s65, s65, 458752
s_add_u32 s65, s65, s67
s_add_u32 m0, s57, 24576
s_add_u32 s59, s64, 0
buffer_load_dwordx4 v45, s[40:43], s59 offen lds
)ASM"
R"ASM(
v_mfma_f32_16x16x32_bf16 v[88:91], v[8:11], v[244:247], v[88:91]
ds_read_b64_tr_b16 v[8:9], v25 offset:0
v_mfma_f32_16x16x32_bf16 v[92:95], v[12:15], v[244:247], v[92:95]
ds_read_b64_tr_b16 v[10:11], v25 offset:512
v_bfe_u32 v1, v0, 2, 2
v_mfma_f32_16x16x32_bf16 v[84:87], v[12:15], v[240:243], v[84:87]
ds_read_b64_tr_b16 v[12:13], v26 offset:0
v_lshl_add_u32 v28, v1, 6, v28
s_add_u32 m0, s57, 28672
s_waitcnt lgkmcnt(5)
v_mfma_f32_16x16x32_bf16 v[96:99], v[16:19], v[240:243], v[96:99]
ds_read_b64_tr_b16 v[14:15], v26 offset:512
s_add_u32 s59, s65, 0
buffer_load_dwordx4 v45, s[40:43], s59 offen lds
v_mfma_f32_16x16x32_bf16 v[104:107], v[16:19], v[244:247], v[104:107]
ds_read_b64_tr_b16 v[16:17], v27 offset:0
v_mfma_f32_16x16x32_bf16 v[108:111], v[20:23], v[244:247], v[108:111]
ds_read_b64_tr_b16 v[18:19], v27 offset:512
v_bfe_u32 v1, v0, 4, 1
v_mfma_f32_16x16x32_bf16 v[100:103], v[20:23], v[240:243], v[100:103]
ds_read_b64_tr_b16 v[20:21], v24 offset:4096
v_lshl_add_u32 v28, v1, 5, v28
v_bfe_u32 v1, v0, 0, 2
s_waitcnt lgkmcnt(5)
v_mfma_f32_16x16x32_bf16 v[48:51], v[4:7], v[248:251], v[48:51]
ds_read_b64_tr_b16 v[22:23], v24 offset:4608
s_add_u32 m0, s57, 65536
s_add_u32 s59, s64, 3670016
buffer_load_dwordx4 v45, s[40:43], s59 offen lds
v_mfma_f32_16x16x32_bf16 v[56:59], v[4:7], v[252:255], v[56:59]
ds_read_b64_tr_b16 v[4:5], v25 offset:4096
v_mfma_f32_16x16x32_bf16 v[60:63], v[8:11], v[252:255], v[60:63]
ds_read_b64_tr_b16 v[6:7], v25 offset:4608
v_lshl_add_u32 v28, v1, 3, v28
v_mfma_f32_16x16x32_bf16 v[52:55], v[8:11], v[248:251], v[52:55]
ds_read_b64_tr_b16 v[8:9], v26 offset:4096
v_add_u32 v28, 0, v28
s_nop 0
ds_write_b128 v30, v[56:59] offset:34816
s_waitcnt lgkmcnt(6)
v_mfma_f32_16x16x32_bf16 v[64:67], v[12:15], v[248:251], v[64:67]
ds_read_b64_tr_b16 v[10:11], v26 offset:4608
s_add_u32 m0, s57, 69632
s_add_u32 s59, s65, 3670016
buffer_load_dwordx4 v45, s[40:43], s59 offen lds
v_mfma_f32_16x16x32_bf16 v[72:75], v[12:15], v[252:255], v[72:75]
ds_read_b64_tr_b16 v[12:13], v27 offset:4096
v_xor_b32 v29, 32, v28
v_mfma_f32_16x16x32_bf16 v[76:79], v[16:19], v[252:255], v[76:79]
ds_read_b64_tr_b16 v[14:15], v27 offset:4608
ds_write_b128 v30, v[60:63] offset:35840
v_mfma_f32_16x16x32_bf16 v[68:71], v[16:19], v[248:251], v[68:71]
s_waitcnt vmcnt(8)
s_waitcnt lgkmcnt(0)
s_barrier
ds_read_b64_tr_b16 v[16:17], v28 offset:8192
ds_read_b128 v[56:59], v30 offset:32768
ds_read_b128 v[60:63], v30 offset:33792
ds_write_b128 v30, v[72:75] offset:51200
s_add_u32 s64, s63, 6
s_and_b32 s64, s64, 7
s_mul_i32 s64, s64, 458752
s_add_u32 s64, s64, s67
v_mfma_f32_16x16x32_bf16 v[80:83], v[20:23], v[248:251], v[80:83]
ds_read_b64_tr_b16 v[18:19], v28 offset:8448
s_add_u32 s65, s66, 7
s_and_b32 s65, s65, 7
s_mul_i32 s65, s65, 458752
s_add_u32 s65, s65, s67
s_add_u32 m0, s57, 32768
s_add_u32 s59, s64, 0
buffer_load_dwordx4 v45, s[40:43], s59 offen lds
v_mfma_f32_16x16x32_bf16 v[88:91], v[20:23], v[252:255], v[88:91]
ds_read_b64_tr_b16 v[20:21], v29 offset:8192
ds_write_b128 v30, v[76:79] offset:52224
s_add_u32 m0, s57, 36864
v_mfma_f32_16x16x32_bf16 v[92:95], v[4:7], v[252:255], v[92:95]
ds_read_b64_tr_b16 v[22:23], v29 offset:8448
s_add_u32 s59, s65, 0
buffer_load_dwordx4 v45, s[40:43], s59 offen lds
v_mfma_f32_16x16x32_bf16 v[84:87], v[4:7], v[248:251], v[84:87]
ds_read_b64_tr_b16 v[4:5], v28 offset:10240
v_mfma_f32_16x16x32_bf16 v[96:99], v[8:11], v[248:251], v[96:99]
ds_read_b64_tr_b16 v[6:7], v28 offset:10496
v_mfma_f32_16x16x32_bf16 v[104:107], v[8:11], v[252:255], v[104:107]
ds_read_b64_tr_b16 v[8:9], v29 offset:10240
s_add_u32 m0, s57, 73728
v_mfma_f32_16x16x32_bf16 v[108:111], v[12:15], v[252:255], v[108:111]
ds_read_b64_tr_b16 v[10:11], v29 offset:10496
s_add_u32 s59, s64, 3670016
buffer_load_dwordx4 v45, s[40:43], s59 offen lds
v_mfma_f32_16x16x32_bf16 v[100:103], v[12:15], v[248:251], v[100:103]
ds_read_b64_tr_b16 v[12:13], v28 offset:12288
s_waitcnt lgkmcnt(11)
v_add_f32 v48, v48, v56
v_add_f32 v49, v49, v57
v_add_f32 v50, v50, v58
v_add_f32 v51, v51, v59
s_waitcnt lgkmcnt(5)
v_mfma_f32_16x16x32_bf16 v[112:115], v[16:19], v[128:131], 0
ds_read_b64_tr_b16 v[14:15], v28 offset:12544
v_add_f32 v52, v52, v60
v_add_f32 v53, v53, v61
s_add_u32 m0, s57, 77824
s_add_u32 s59, s65, 3670016
buffer_load_dwordx4 v45, s[40:43], s59 offen lds
v_mfma_f32_16x16x32_bf16 v[120:123], v[16:19], v[132:135], 0
ds_read_b64_tr_b16 v[16:17], v29 offset:12288
v_add_f32 v54, v54, v62
v_add_f32 v55, v55, v63
v_mul_f32 v56, v48, v48
v_mfma_f32_16x16x32_bf16 v[124:127], v[20:23], v[132:135], 0
ds_read_b64_tr_b16 v[18:19], v29 offset:12544
v_mul_f32 v57, v49, v49
v_mul_f32 v58, v50, v50
v_mul_f32 v59, v51, v51
v_mfma_f32_16x16x32_bf16 v[116:119], v[20:23], v[128:131], 0
ds_read_b64_tr_b16 v[20:21], v28 offset:14336
v_mul_f32 v60, v52, v52
v_mul_f32 v61, v53, v53
s_waitcnt lgkmcnt(5)
v_mfma_f32_16x16x32_bf16 v[112:115], v[4:7], v[136:139], v[112:115]
ds_read_b64_tr_b16 v[22:23], v28 offset:14592
v_mul_f32 v62, v54, v54
v_mul_f32 v63, v55, v55
v_fma_f32 v56, v56, v32, s82
v_mfma_f32_16x16x32_bf16 v[120:123], v[4:7], v[140:143], v[120:123]
ds_read_b64_tr_b16 v[4:5], v29 offset:14336
v_fma_f32 v57, v57, v32, s82
v_fma_f32 v58, v58, v32, s82
v_fma_f32 v59, v59, v32, s82
v_mfma_f32_16x16x32_bf16 v[124:127], v[8:11], v[140:143], v[124:127]
ds_read_b64_tr_b16 v[6:7], v29 offset:14592
v_fma_f32 v60, v60, v32, s82
v_fma_f32 v61, v61, v32, s82
v_fma_f32 v62, v62, v32, s82
v_mfma_f32_16x16x32_bf16 v[116:119], v[8:11], v[136:139], v[116:119]
s_waitcnt vmcnt(8)
s_barrier
ds_read_b64_tr_b16 v[8:9], v28 offset:16384
v_fma_f32 v63, v63, v32, s82
v_mul_f32 v56, v48, v56
v_mul_f32 v57, v49, v57
v_exp_f32 v56, v56
v_mul_f32 v58, v50, v58
v_exp_f32 v57, v57
ds_read_b128 v[72:75], v30 offset:49152
ds_read_b128 v[76:79], v30 offset:50176
s_waitcnt lgkmcnt(7)
v_mfma_f32_16x16x32_bf16 v[112:115], v[12:15], v[144:147], v[112:115]
ds_read_b64_tr_b16 v[10:11], v28 offset:16640
ds_write_b128 v30, v[88:91] offset:34816
s_add_u32 s67, s67, s69
s_add_u32 s64, s63, 0
s_and_b32 s64, s64, 7
s_mul_i32 s64, s64, 458752
s_add_u32 s64, s64, s67
s_add_u32 m0, s57, 0
s_add_u32 s59, s64, 0
buffer_load_dwordx4 v44, s[40:43], s59 offen lds
v_mfma_f32_16x16x32_bf16 v[120:123], v[12:15], v[148:151], v[120:123]
ds_read_b64_tr_b16 v[12:13], v29 offset:16384
v_mul_f32 v59, v51, v59
v_exp_f32 v58, v58
v_mul_f32 v60, v52, v60
v_exp_f32 v59, v59
v_mfma_f32_16x16x32_bf16 v[124:127], v[16:19], v[148:151], v[124:127]
ds_read_b64_tr_b16 v[14:15], v29 offset:16640
v_mul_f32 v61, v53, v61
ds_write_b128 v30, v[92:95] offset:35840
s_add_u32 m0, s57, 4096
s_add_u32 s59, s64, 229376
buffer_load_dwordx4 v44, s[40:43], s59 offen lds
v_mfma_f32_16x16x32_bf16 v[116:119], v[16:19], v[144:147], v[116:119]
ds_read_b64_tr_b16 v[16:17], v28 offset:18432
v_exp_f32 v60, v60
v_mul_f32 v62, v54, v62
v_exp_f32 v61, v61
s_waitcnt lgkmcnt(9)
v_mfma_f32_16x16x32_bf16 v[112:115], v[20:23], v[152:155], v[112:115]
ds_read_b64_tr_b16 v[18:19], v28 offset:18688
v_mul_f32 v63, v55, v63
v_exp_f32 v62, v62
v_exp_f32 v63, v63
v_mfma_f32_16x16x32_bf16 v[120:123], v[20:23], v[156:159], v[120:123]
ds_read_b64_tr_b16 v[20:21], v29 offset:18432
v_add_f32 v56, 1.0, v56
v_add_f32 v57, 1.0, v57
v_rcp_f32 v56, v56
v_add_f32 v58, 1.0, v58
v_mfma_f32_16x16x32_bf16 v[124:127], v[4:7], v[156:159], v[124:127]
ds_read_b64_tr_b16 v[22:23], v29 offset:18688
v_rcp_f32 v57, v57
v_add_f32 v59, 1.0, v59
s_add_u32 m0, s57, 40960
s_add_u32 s59, s64, 3670016
buffer_load_dwordx4 v44, s[40:43], s59 offen lds
v_mfma_f32_16x16x32_bf16 v[116:119], v[4:7], v[152:155], v[116:119]
ds_read_b64_tr_b16 v[4:5], v28 offset:20480
v_rcp_f32 v58, v58
)ASM"
R"ASM(
v_add_f32 v60, 1.0, v60
v_rcp_f32 v59, v59
v_add_f32 v61, 1.0, v61
s_waitcnt lgkmcnt(6)
v_mfma_f32_16x16x32_bf16 v[112:115], v[8:11], v[160:163], v[112:115]
ds_read_b64_tr_b16 v[6:7], v28 offset:20736
v_rcp_f32 v60, v60
s_add_u32 m0, s57, 45056
s_add_u32 s59, s64, 3899392
buffer_load_dwordx4 v44, s[40:43], s59 offen lds
v_mfma_f32_16x16x32_bf16 v[120:123], v[8:11], v[164:167], v[120:123]
ds_read_b64_tr_b16 v[8:9], v29 offset:20480
v_add_f32 v62, 1.0, v62
v_rcp_f32 v61, v61
v_add_f32 v63, 1.0, v63
v_mfma_f32_16x16x32_bf16 v[124:127], v[12:15], v[164:167], v[124:127]
ds_read_b64_tr_b16 v[10:11], v29 offset:20736
v_rcp_f32 v62, v62
v_rcp_f32 v63, v63
v_mul_f32 v48, v48, v56
v_mfma_f32_16x16x32_bf16 v[116:119], v[12:15], v[160:163], v[116:119]
ds_read_b64_tr_b16 v[12:13], v28 offset:22528
v_mul_f32 v49, v49, v57
v_mul_f32 v50, v50, v58
v_mul_f32 v51, v51, v59
s_waitcnt lgkmcnt(5)
v_mfma_f32_16x16x32_bf16 v[112:115], v[16:19], v[168:171], v[112:115]
ds_read_b64_tr_b16 v[14:15], v28 offset:22784
v_mul_f32 v52, v52, v60
v_mul_f32 v53, v53, v61
v_mul_f32 v54, v54, v62
v_mfma_f32_16x16x32_bf16 v[120:123], v[16:19], v[172:175], v[120:123]
ds_read_b64_tr_b16 v[16:17], v29 offset:22528
v_mul_f32 v55, v55, v63
v_cvt_pk_bf16_f32 v48, v48, v49
v_cvt_pk_bf16_f32 v49, v50, v51
v_mfma_f32_16x16x32_bf16 v[124:127], v[20:23], v[172:175], v[124:127]
ds_read_b64_tr_b16 v[18:19], v29 offset:22784
v_cvt_pk_bf16_f32 v50, v52, v53
v_cvt_pk_bf16_f32 v51, v54, v55
s_add_u32 s74, s80, 0
buffer_store_dwordx4 v[48:51], v31, s[48:51], s74 offen sc1
v_mfma_f32_16x16x32_bf16 v[116:119], v[20:23], v[168:171], v[116:119]
s_waitcnt vmcnt(9)
s_barrier
ds_read_b64_tr_b16 v[20:21], v28 offset:24576
v_add_f32 v64, v64, v72
v_add_f32 v65, v65, v73
v_add_f32 v66, v66, v74
v_add_f32 v67, v67, v75
v_add_f32 v68, v68, v76
v_add_f32 v69, v69, v77
ds_read_b128 v[88:91], v30 offset:32768
ds_read_b128 v[92:95], v30 offset:33792
s_waitcnt lgkmcnt(7)
v_mfma_f32_16x16x32_bf16 v[112:115], v[4:7], v[176:179], v[112:115]
ds_read_b64_tr_b16 v[22:23], v28 offset:24832
ds_write_b128 v30, v[104:107] offset:51200
s_add_u32 s64, s66, 1
s_and_b32 s64, s64, 7
s_mul_i32 s64, s64, 458752
s_add_u32 s64, s64, s67
s_add_u32 m0, s57, 8192
s_add_u32 s59, s64, 0
buffer_load_dwordx4 v44, s[40:43], s59 offen lds
v_mfma_f32_16x16x32_bf16 v[120:123], v[4:7], v[180:183], v[120:123]
ds_read_b64_tr_b16 v[4:5], v29 offset:24576
v_add_f32 v70, v70, v78
v_add_f32 v71, v71, v79
v_mul_f32 v72, v64, v64
v_mul_f32 v73, v65, v65
v_mul_f32 v74, v66, v66
v_mfma_f32_16x16x32_bf16 v[124:127], v[8:11], v[180:183], v[124:127]
ds_read_b64_tr_b16 v[6:7], v29 offset:24832
v_mul_f32 v75, v67, v67
ds_write_b128 v30, v[108:111] offset:52224
s_add_u32 m0, s57, 12288
s_add_u32 s59, s64, 229376
buffer_load_dwordx4 v44, s[40:43], s59 offen lds
v_mfma_f32_16x16x32_bf16 v[116:119], v[8:11], v[176:179], v[116:119]
ds_read_b64_tr_b16 v[8:9], v28 offset:26624
v_mul_f32 v76, v68, v68
v_mul_f32 v77, v69, v69
s_waitcnt lgkmcnt(9)
v_mfma_f32_16x16x32_bf16 v[112:115], v[12:15], v[184:187], v[112:115]
ds_read_b64_tr_b16 v[10:11], v28 offset:26880
v_mul_f32 v78, v70, v70
v_mul_f32 v79, v71, v71
v_fma_f32 v72, v72, v32, s82
v_mfma_f32_16x16x32_bf16 v[120:123], v[12:15], v[188:191], v[120:123]
ds_read_b64_tr_b16 v[12:13], v29 offset:26624
v_fma_f32 v73, v73, v32, s82
v_fma_f32 v74, v74, v32, s82
v_fma_f32 v75, v75, v32, s82
v_fma_f32 v76, v76, v32, s82
v_mfma_f32_16x16x32_bf16 v[124:127], v[16:19], v[188:191], v[124:127]
ds_read_b64_tr_b16 v[14:15], v29 offset:26880
v_fma_f32 v77, v77, v32, s82
v_fma_f32 v78, v78, v32, s82
s_add_u32 m0, s57, 49152
s_add_u32 s59, s64, 3670016
buffer_load_dwordx4 v44, s[40:43], s59 offen lds
v_mfma_f32_16x16x32_bf16 v[116:119], v[16:19], v[184:187], v[116:119]
ds_read_b64_tr_b16 v[16:17], v28 offset:28672
v_fma_f32 v79, v79, v32, s82
v_mul_f32 v72, v64, v72
v_mul_f32 v73, v65, v73
v_exp_f32 v72, v72
s_waitcnt lgkmcnt(6)
v_mfma_f32_16x16x32_bf16 v[112:115], v[20:23], v[192:195], v[112:115]
ds_read_b64_tr_b16 v[18:19], v28 offset:28928
v_mul_f32 v74, v66, v74
v_exp_f32 v73, v73
s_add_u32 m0, s57, 53248
s_add_u32 s59, s64, 3899392
buffer_load_dwordx4 v44, s[40:43], s59 offen lds
v_mfma_f32_16x16x32_bf16 v[120:123], v[20:23], v[196:199], v[120:123]
ds_read_b64_tr_b16 v[20:21], v29 offset:28672
v_mul_f32 v75, v67, v75
v_exp_f32 v74, v74
v_mfma_f32_16x16x32_bf16 v[124:127], v[4:7], v[196:199], v[124:127]
ds_read_b64_tr_b16 v[22:23], v29 offset:28928
v_mul_f32 v76, v68, v76
v_exp_f32 v75, v75
v_mul_f32 v77, v69, v77
v_mfma_f32_16x16x32_bf16 v[116:119], v[4:7], v[192:195], v[116:119]
ds_read_b64_tr_b16 v[4:5], v28 offset:30720
v_exp_f32 v76, v76
v_mul_f32 v78, v70, v78
v_exp_f32 v77, v77
s_waitcnt lgkmcnt(5)
v_mfma_f32_16x16x32_bf16 v[112:115], v[8:11], v[200:203], v[112:115]
ds_read_b64_tr_b16 v[6:7], v28 offset:30976
v_mul_f32 v79, v71, v79
v_exp_f32 v78, v78
v_exp_f32 v79, v79
v_mfma_f32_16x16x32_bf16 v[120:123], v[8:11], v[204:207], v[120:123]
ds_read_b64_tr_b16 v[8:9], v29 offset:30720
v_add_f32 v72, 1.0, v72
v_add_f32 v73, 1.0, v73
v_rcp_f32 v72, v72
v_mfma_f32_16x16x32_bf16 v[124:127], v[12:15], v[204:207], v[124:127]
ds_read_b64_tr_b16 v[10:11], v29 offset:30976
v_add_f32 v74, 1.0, v74
v_rcp_f32 v73, v73
v_add_f32 v75, 1.0, v75
v_mfma_f32_16x16x32_bf16 v[116:119], v[12:15], v[200:203], v[116:119]
s_waitcnt vmcnt(9)
s_barrier
ds_read_b64_tr_b16 v[12:13], v28 offset:32768
v_rcp_f32 v74, v74
v_add_f32 v76, 1.0, v76
v_rcp_f32 v75, v75
v_add_f32 v77, 1.0, v77
v_rcp_f32 v76, v76
ds_read_b128 v[104:107], v30 offset:49152
ds_read_b128 v[108:111], v30 offset:50176
s_waitcnt lgkmcnt(7)
v_mfma_f32_16x16x32_bf16 v[112:115], v[16:19], v[208:211], v[112:115]
ds_read_b64_tr_b16 v[14:15], v28 offset:33024
s_add_u32 s64, s63, 2
s_and_b32 s64, s64, 7
s_mul_i32 s64, s64, 458752
s_add_u32 s64, s64, s67
s_add_u32 m0, s57, 16384
s_add_u32 s59, s64, 0
buffer_load_dwordx4 v44, s[40:43], s59 offen lds
v_mfma_f32_16x16x32_bf16 v[120:123], v[16:19], v[212:215], v[120:123]
ds_read_b64_tr_b16 v[16:17], v29 offset:32768
v_add_f32 v78, 1.0, v78
v_rcp_f32 v77, v77
v_add_f32 v79, 1.0, v79
v_rcp_f32 v78, v78
v_mfma_f32_16x16x32_bf16 v[124:127], v[20:23], v[212:215], v[124:127]
ds_read_b64_tr_b16 v[18:19], v29 offset:33024
v_rcp_f32 v79, v79
v_mul_f32 v64, v64, v72
s_add_u32 m0, s57, 20480
s_add_u32 s59, s64, 229376
buffer_load_dwordx4 v44, s[40:43], s59 offen lds
v_mfma_f32_16x16x32_bf16 v[116:119], v[20:23], v[208:211], v[116:119]
ds_read_b64_tr_b16 v[20:21], v28 offset:34816
v_mul_f32 v65, v65, v73
v_mul_f32 v66, v66, v74
v_mul_f32 v67, v67, v75
s_waitcnt lgkmcnt(7)
v_mfma_f32_16x16x32_bf16 v[112:115], v[4:7], v[216:219], v[112:115]
ds_read_b64_tr_b16 v[22:23], v28 offset:35072
v_mul_f32 v68, v68, v76
v_mul_f32 v69, v69, v77
v_mul_f32 v70, v70, v78
v_mfma_f32_16x16x32_bf16 v[120:123], v[4:7], v[220:223], v[120:123]
ds_read_b64_tr_b16 v[4:5], v29 offset:34816
v_mul_f32 v71, v71, v79
v_cvt_pk_bf16_f32 v64, v64, v65
v_cvt_pk_bf16_f32 v65, v66, v67
v_cvt_pk_bf16_f32 v66, v68, v69
v_cvt_pk_bf16_f32 v67, v70, v71
v_mfma_f32_16x16x32_bf16 v[124:127], v[8:11], v[220:223], v[124:127]
ds_read_b64_tr_b16 v[6:7], v29 offset:35072
)ASM"
R"ASM(
s_add_u32 s74, s80, 8192
buffer_store_dwordx4 v[64:67], v31, s[48:51], s74 offen sc1
s_add_u32 m0, s57, 57344
s_add_u32 s59, s64, 3670016
buffer_load_dwordx4 v44, s[40:43], s59 offen lds
v_mfma_f32_16x16x32_bf16 v[116:119], v[8:11], v[216:219], v[116:119]
ds_read_b64_tr_b16 v[8:9], v28 offset:36864
v_add_f32 v80, v80, v88
v_add_f32 v81, v81, v89
v_add_f32 v82, v82, v90
v_add_f32 v83, v83, v91
s_waitcnt lgkmcnt(5)
v_mfma_f32_16x16x32_bf16 v[112:115], v[12:15], v[224:227], v[112:115]
ds_read_b64_tr_b16 v[10:11], v28 offset:37120
v_add_f32 v84, v84, v92
v_add_f32 v85, v85, v93
s_add_u32 m0, s57, 61440
s_add_u32 s59, s64, 3899392
buffer_load_dwordx4 v44, s[40:43], s59 offen lds
v_mfma_f32_16x16x32_bf16 v[120:123], v[12:15], v[228:231], v[120:123]
ds_read_b64_tr_b16 v[12:13], v29 offset:36864
v_add_f32 v86, v86, v94
v_add_f32 v87, v87, v95
v_mul_f32 v88, v80, v80
v_mfma_f32_16x16x32_bf16 v[124:127], v[16:19], v[228:231], v[124:127]
ds_read_b64_tr_b16 v[14:15], v29 offset:37120
v_mul_f32 v89, v81, v81
v_mul_f32 v90, v82, v82
v_mul_f32 v91, v83, v83
v_mfma_f32_16x16x32_bf16 v[116:119], v[16:19], v[224:227], v[116:119]
ds_read_b64_tr_b16 v[16:17], v28 offset:38912
v_mul_f32 v92, v84, v84
v_mul_f32 v93, v85, v85
s_waitcnt lgkmcnt(5)
v_mfma_f32_16x16x32_bf16 v[112:115], v[20:23], v[232:235], v[112:115]
ds_read_b64_tr_b16 v[18:19], v28 offset:39168
v_mul_f32 v94, v86, v86
v_mul_f32 v95, v87, v87
v_fma_f32 v88, v88, v32, s82
v_mfma_f32_16x16x32_bf16 v[120:123], v[20:23], v[236:239], v[120:123]
ds_read_b64_tr_b16 v[20:21], v29 offset:38912
v_fma_f32 v89, v89, v32, s82
v_fma_f32 v90, v90, v32, s82
v_fma_f32 v91, v91, v32, s82
v_mfma_f32_16x16x32_bf16 v[124:127], v[4:7], v[236:239], v[124:127]
ds_read_b64_tr_b16 v[22:23], v29 offset:39168
v_fma_f32 v92, v92, v32, s82
v_fma_f32 v93, v93, v32, s82
v_fma_f32 v94, v94, v32, s82
v_mfma_f32_16x16x32_bf16 v[116:119], v[4:7], v[232:235], v[116:119]
s_waitcnt vmcnt(10)
s_waitcnt vmcnt(9)
s_barrier
ds_read_b64_tr_b16 v[4:5], v24 offset:0
v_bfe_u32 v1, v0, 2, 4
v_fma_f32 v95, v95, v32, s82
v_mul_f32 v88, v80, v88
v_mul_f32 v89, v81, v89
v_exp_f32 v88, v88
v_mul_f32 v90, v82, v90
v_exp_f32 v89, v89
s_waitcnt lgkmcnt(5)
v_mfma_f32_16x16x32_bf16 v[112:115], v[8:11], v[240:243], v[112:115]
ds_read_b64_tr_b16 v[6:7], v24 offset:512
s_add_u32 s64, s66, 3
s_and_b32 s64, s64, 7
s_mul_i32 s64, s64, 458752
s_add_u32 s64, s64, s67
s_add_u32 m0, s57, 24576
s_add_u32 s59, s64, 0
buffer_load_dwordx4 v44, s[40:43], s59 offen lds
v_mfma_f32_16x16x32_bf16 v[120:123], v[8:11], v[244:247], v[120:123]
ds_read_b64_tr_b16 v[8:9], v25 offset:0
v_lshlrev_b32 v28, 7, v1
v_mul_f32 v91, v83, v91
v_exp_f32 v90, v90
v_mul_f32 v92, v84, v92
v_mfma_f32_16x16x32_bf16 v[124:127], v[12:15], v[244:247], v[124:127]
ds_read_b64_tr_b16 v[10:11], v25 offset:512
v_exp_f32 v91, v91
v_mul_f32 v93, v85, v93
s_add_u32 m0, s57, 28672
s_add_u32 s59, s64, 229376
buffer_load_dwordx4 v44, s[40:43], s59 offen lds
v_mfma_f32_16x16x32_bf16 v[116:119], v[12:15], v[240:243], v[116:119]
ds_read_b64_tr_b16 v[12:13], v26 offset:0
v_bfe_u32 v1, v0, 3, 1
v_exp_f32 v92, v92
v_mul_f32 v94, v86, v94
s_waitcnt lgkmcnt(5)
v_mfma_f32_16x16x32_bf16 v[112:115], v[16:19], v[248:251], v[112:115]
ds_read_b64_tr_b16 v[14:15], v26 offset:512
v_exp_f32 v93, v93
v_mul_f32 v95, v87, v95
v_exp_f32 v94, v94
v_exp_f32 v95, v95
v_mfma_f32_16x16x32_bf16 v[120:123], v[16:19], v[252:255], v[120:123]
ds_read_b64_tr_b16 v[16:17], v27 offset:0
v_lshl_add_u32 v28, v1, 6, v28
v_add_f32 v88, 1.0, v88
v_add_f32 v89, 1.0, v89
v_rcp_f32 v88, v88
v_add_f32 v90, 1.0, v90
v_mfma_f32_16x16x32_bf16 v[124:127], v[20:23], v[252:255], v[124:127]
ds_read_b64_tr_b16 v[18:19], v27 offset:512
v_rcp_f32 v89, v89
v_add_f32 v91, 1.0, v91
s_add_u32 m0, s57, 65536
s_add_u32 s59, s64, 3670016
buffer_load_dwordx4 v44, s[40:43], s59 offen lds
v_mfma_f32_16x16x32_bf16 v[116:119], v[20:23], v[248:251], v[116:119]
ds_read_b64_tr_b16 v[20:21], v24 offset:4096
v_bfe_u32 v1, v0, 0, 2
v_rcp_f32 v90, v90
v_add_f32 v92, 1.0, v92
v_rcp_f32 v91, v91
v_add_f32 v93, 1.0, v93
s_waitcnt lgkmcnt(5)
v_mfma_f32_16x16x32_bf16 v[48:51], v[4:7], v[128:131], 0
ds_read_b64_tr_b16 v[22:23], v24 offset:4608
v_rcp_f32 v92, v92
ds_write_b128 v30, v[120:123] offset:34816
s_add_u32 m0, s57, 69632
s_add_u32 s59, s64, 3899392
buffer_load_dwordx4 v44, s[40:43], s59 offen lds
v_mfma_f32_16x16x32_bf16 v[56:59], v[4:7], v[132:135], 0
ds_read_b64_tr_b16 v[4:5], v25 offset:4096
v_lshl_add_u32 v28, v1, 4, v28
v_add_f32 v94, 1.0, v94
v_rcp_f32 v93, v93
v_add_f32 v95, 1.0, v95
v_mfma_f32_16x16x32_bf16 v[60:63], v[8:11], v[132:135], 0
ds_read_b64_tr_b16 v[6:7], v25 offset:4608
v_rcp_f32 v94, v94
v_rcp_f32 v95, v95
v_mul_f32 v80, v80, v88
ds_write_b128 v30, v[124:127] offset:35840
v_mfma_f32_16x16x32_bf16 v[52:55], v[8:11], v[128:131], 0
ds_read_b64_tr_b16 v[8:9], v26 offset:4096
v_xor_b32 v28, s79, v28
v_mul_f32 v81, v81, v89
v_mul_f32 v82, v82, v90
s_waitcnt lgkmcnt(7)
v_mfma_f32_16x16x32_bf16 v[64:67], v[12:15], v[128:131], 0
ds_read_b64_tr_b16 v[10:11], v26 offset:4608
v_mul_f32 v83, v83, v91
v_mul_f32 v84, v84, v92
v_mul_f32 v85, v85, v93
v_mul_f32 v86, v86, v94
v_mfma_f32_16x16x32_bf16 v[72:75], v[12:15], v[132:135], 0
ds_read_b64_tr_b16 v[12:13], v27 offset:4096
v_mul_f32 v87, v87, v95
v_cvt_pk_bf16_f32 v80, v80, v81
v_cvt_pk_bf16_f32 v81, v82, v83
v_mfma_f32_16x16x32_bf16 v[76:79], v[16:19], v[132:135], 0
ds_read_b64_tr_b16 v[14:15], v27 offset:4608
v_cvt_pk_bf16_f32 v82, v84, v85
v_cvt_pk_bf16_f32 v83, v86, v87
s_add_u32 s74, s80, 16384
buffer_store_dwordx4 v[80:83], v31, s[48:51], s74 offen sc1
v_mfma_f32_16x16x32_bf16 v[68:71], v[16:19], v[128:131], 0
s_waitcnt vmcnt(10)
s_waitcnt lgkmcnt(4)
s_barrier
ds_read_b64_tr_b16 v[16:17], v24 offset:8192
v_add_f32 v96, v96, v104
v_add_f32 v97, v97, v105
v_add_f32 v98, v98, v106
v_add_f32 v99, v99, v107
v_add_f32 v100, v100, v108
v_add_f32 v101, v101, v109
ds_read_b128 v[120:123], v30 offset:32768
v_mfma_f32_16x16x32_bf16 v[48:51], v[20:23], v[136:139], v[48:51]
ds_read_b64_tr_b16 v[18:19], v24 offset:8704
ds_read_b128 v[124:127], v30 offset:33792
s_add_u32 s64, s63, 4
s_and_b32 s64, s64, 7
s_mul_i32 s64, s64, 458752
s_add_u32 s64, s64, s67
s_add_u32 m0, s57, 32768
s_add_u32 s59, s64, 0
buffer_load_dwordx4 v44, s[40:43], s59 offen lds
v_mfma_f32_16x16x32_bf16 v[56:59], v[20:23], v[140:143], v[56:59]
ds_read_b64_tr_b16 v[20:21], v25 offset:8192
v_add_f32 v102, v102, v110
v_add_f32 v103, v103, v111
v_mul_f32 v104, v96, v96
v_mul_f32 v105, v97, v97
v_mfma_f32_16x16x32_bf16 v[60:63], v[4:7], v[140:143], v[60:63]
ds_read_b64_tr_b16 v[22:23], v25 offset:8704
v_mul_f32 v106, v98, v98
v_mul_f32 v107, v99, v99
s_add_u32 m0, s57, 36864
s_add_u32 s59, s64, 229376
buffer_load_dwordx4 v44, s[40:43], s59 offen lds
v_mfma_f32_16x16x32_bf16 v[52:55], v[4:7], v[136:139], v[52:55]
ds_read_b64_tr_b16 v[4:5], v26 offset:8192
v_mul_f32 v108, v100, v100
v_mul_f32 v109, v101, v101
s_waitcnt lgkmcnt(7)
)ASM"
R"ASM(
v_mfma_f32_16x16x32_bf16 v[64:67], v[8:11], v[136:139], v[64:67]
ds_read_b64_tr_b16 v[6:7], v26 offset:8704
v_mul_f32 v110, v102, v102
v_mul_f32 v111, v103, v103
v_fma_f32 v104, v104, v32, s82
v_mfma_f32_16x16x32_bf16 v[72:75], v[8:11], v[140:143], v[72:75]
ds_read_b64_tr_b16 v[8:9], v27 offset:8192
v_fma_f32 v105, v105, v32, s82
v_fma_f32 v106, v106, v32, s82
v_fma_f32 v107, v107, v32, s82
v_fma_f32 v108, v108, v32, s82
v_mfma_f32_16x16x32_bf16 v[76:79], v[12:15], v[140:143], v[76:79]
ds_read_b64_tr_b16 v[10:11], v27 offset:8704
v_fma_f32 v109, v109, v32, s82
v_fma_f32 v110, v110, v32, s82
s_add_u32 m0, s57, 73728
s_add_u32 s59, s64, 3670016
buffer_load_dwordx4 v44, s[40:43], s59 offen lds
v_mfma_f32_16x16x32_bf16 v[68:71], v[12:15], v[136:139], v[68:71]
ds_read_b64_tr_b16 v[12:13], v24 offset:12288
v_fma_f32 v111, v111, v32, s82
v_mul_f32 v104, v96, v104
v_mul_f32 v105, v97, v105
v_exp_f32 v104, v104
s_waitcnt lgkmcnt(5)
v_mfma_f32_16x16x32_bf16 v[48:51], v[16:19], v[144:147], v[48:51]
ds_read_b64_tr_b16 v[14:15], v24 offset:12800
v_mul_f32 v106, v98, v106
v_exp_f32 v105, v105
s_add_u32 m0, s57, 77824
s_add_u32 s59, s64, 3899392
buffer_load_dwordx4 v44, s[40:43], s59 offen lds
v_mfma_f32_16x16x32_bf16 v[56:59], v[16:19], v[148:151], v[56:59]
ds_read_b64_tr_b16 v[16:17], v25 offset:12288
v_mul_f32 v107, v99, v107
v_exp_f32 v106, v106
v_mfma_f32_16x16x32_bf16 v[60:63], v[20:23], v[148:151], v[60:63]
ds_read_b64_tr_b16 v[18:19], v25 offset:12800
v_mul_f32 v108, v100, v108
v_exp_f32 v107, v107
v_mul_f32 v109, v101, v109
v_mfma_f32_16x16x32_bf16 v[52:55], v[20:23], v[144:147], v[52:55]
ds_read_b64_tr_b16 v[20:21], v26 offset:12288
v_exp_f32 v108, v108
v_mul_f32 v110, v102, v110
v_exp_f32 v109, v109
s_waitcnt lgkmcnt(5)
v_mfma_f32_16x16x32_bf16 v[64:67], v[4:7], v[144:147], v[64:67]
ds_read_b64_tr_b16 v[22:23], v26 offset:12800
v_mul_f32 v111, v103, v111
v_exp_f32 v110, v110
v_exp_f32 v111, v111
v_mfma_f32_16x16x32_bf16 v[72:75], v[4:7], v[148:151], v[72:75]
ds_read_b64_tr_b16 v[4:5], v27 offset:12288
v_add_f32 v104, 1.0, v104
v_add_f32 v105, 1.0, v105
v_rcp_f32 v104, v104
v_mfma_f32_16x16x32_bf16 v[76:79], v[8:11], v[148:151], v[76:79]
ds_read_b64_tr_b16 v[6:7], v27 offset:12800
v_add_f32 v106, 1.0, v106
v_rcp_f32 v105, v105
v_add_f32 v107, 1.0, v107
v_mfma_f32_16x16x32_bf16 v[68:71], v[8:11], v[144:147], v[68:71]
s_waitcnt vmcnt(9)
s_barrier
ds_read_b64_tr_b16 v[8:9], v24 offset:16384
v_rcp_f32 v106, v106
v_add_f32 v108, 1.0, v108
v_rcp_f32 v107, v107
v_add_f32 v109, 1.0, v109
v_rcp_f32 v108, v108
s_add_u32 s64, s66, 5
s_waitcnt lgkmcnt(5)
v_mfma_f32_16x16x32_bf16 v[48:51], v[12:15], v[152:155], v[48:51]
ds_read_b64_tr_b16 v[10:11], v24 offset:16896
s_and_b32 s64, s64, 7
s_mul_i32 s64, s64, 458752
s_add_u32 s64, s64, s67
s_add_u32 m0, s57, 0
s_add_u32 s59, s64, 0
buffer_load_dwordx4 v44, s[40:43], s59 offen lds
v_mfma_f32_16x16x32_bf16 v[56:59], v[12:15], v[156:159], v[56:59]
ds_read_b64_tr_b16 v[12:13], v25 offset:16384
v_add_f32 v110, 1.0, v110
v_rcp_f32 v109, v109
v_add_f32 v111, 1.0, v111
v_rcp_f32 v110, v110
v_mfma_f32_16x16x32_bf16 v[60:63], v[16:19], v[156:159], v[60:63]
ds_read_b64_tr_b16 v[14:15], v25 offset:16896
v_rcp_f32 v111, v111
v_mul_f32 v96, v96, v104
s_add_u32 m0, s57, 4096
s_add_u32 s59, s64, 229376
buffer_load_dwordx4 v44, s[40:43], s59 offen lds
v_mfma_f32_16x16x32_bf16 v[52:55], v[16:19], v[152:155], v[52:55]
ds_read_b64_tr_b16 v[16:17], v26 offset:16384
v_mul_f32 v97, v97, v105
v_mul_f32 v98, v98, v106
v_mul_f32 v99, v99, v107
s_waitcnt lgkmcnt(5)
v_mfma_f32_16x16x32_bf16 v[64:67], v[20:23], v[152:155], v[64:67]
ds_read_b64_tr_b16 v[18:19], v26 offset:16896
v_mul_f32 v100, v100, v108
v_mul_f32 v101, v101, v109
v_mul_f32 v102, v102, v110
v_mfma_f32_16x16x32_bf16 v[72:75], v[20:23], v[156:159], v[72:75]
ds_read_b64_tr_b16 v[20:21], v27 offset:16384
v_mul_f32 v103, v103, v111
v_cvt_pk_bf16_f32 v96, v96, v97
v_cvt_pk_bf16_f32 v97, v98, v99
v_cvt_pk_bf16_f32 v98, v100, v101
v_mfma_f32_16x16x32_bf16 v[76:79], v[4:7], v[156:159], v[76:79]
ds_read_b64_tr_b16 v[22:23], v27 offset:16896
v_cvt_pk_bf16_f32 v99, v102, v103
s_add_u32 m0, s57, 40960
s_add_u32 s59, s64, 3670016
buffer_load_dwordx4 v44, s[40:43], s59 offen lds
v_mfma_f32_16x16x32_bf16 v[68:71], v[4:7], v[152:155], v[68:71]
ds_read_b64_tr_b16 v[4:5], v24 offset:20480
v_add_f32 v112, v112, v120
v_add_f32 v113, v113, v121
v_add_f32 v114, v114, v122
v_add_f32 v115, v115, v123
s_waitcnt lgkmcnt(5)
v_mfma_f32_16x16x32_bf16 v[48:51], v[8:11], v[160:163], v[48:51]
ds_read_b64_tr_b16 v[6:7], v24 offset:20992
v_add_f32 v116, v116, v124
v_add_f32 v117, v117, v125
s_add_u32 m0, s57, 45056
s_add_u32 s59, s64, 3899392
buffer_load_dwordx4 v44, s[40:43], s59 offen lds
v_mfma_f32_16x16x32_bf16 v[56:59], v[8:11], v[164:167], v[56:59]
ds_read_b64_tr_b16 v[8:9], v25 offset:20480
v_add_f32 v118, v118, v126
v_add_f32 v119, v119, v127
v_mul_f32 v120, v112, v112
v_mfma_f32_16x16x32_bf16 v[60:63], v[12:15], v[164:167], v[60:63]
ds_read_b64_tr_b16 v[10:11], v25 offset:20992
v_mul_f32 v121, v113, v113
v_mul_f32 v122, v114, v114
v_mul_f32 v123, v115, v115
v_mfma_f32_16x16x32_bf16 v[52:55], v[12:15], v[160:163], v[52:55]
ds_read_b64_tr_b16 v[12:13], v26 offset:20480
s_add_u32 s74, s80, 24576
buffer_store_dwordx4 v[96:99], v31, s[48:51], s74 offen sc1
v_mul_f32 v124, v116, v116
s_waitcnt lgkmcnt(5)
v_mfma_f32_16x16x32_bf16 v[64:67], v[16:19], v[160:163], v[64:67]
ds_read_b64_tr_b16 v[14:15], v26 offset:20992
v_mul_f32 v125, v117, v117
v_mul_f32 v126, v118, v118
v_mul_f32 v127, v119, v119
v_fma_f32 v120, v120, v32, s82
v_mfma_f32_16x16x32_bf16 v[72:75], v[16:19], v[164:167], v[72:75]
ds_read_b64_tr_b16 v[16:17], v27 offset:20480
v_fma_f32 v121, v121, v32, s82
v_fma_f32 v122, v122, v32, s82
v_fma_f32 v123, v123, v32, s82
v_mfma_f32_16x16x32_bf16 v[76:79], v[20:23], v[164:167], v[76:79]
ds_read_b64_tr_b16 v[18:19], v27 offset:20992
v_fma_f32 v124, v124, v32, s82
v_fma_f32 v125, v125, v32, s82
v_fma_f32 v126, v126, v32, s82
v_mfma_f32_16x16x32_bf16 v[68:71], v[20:23], v[160:163], v[68:71]
s_waitcnt vmcnt(10)
s_waitcnt vmcnt(9)
s_barrier
ds_read_b64_tr_b16 v[20:21], v24 offset:24576
v_fma_f32 v127, v127, v32, s82
v_mul_f32 v120, v112, v120
v_mul_f32 v121, v113, v121
v_exp_f32 v120, v120
v_mul_f32 v122, v114, v122
v_exp_f32 v121, v121
s_waitcnt lgkmcnt(5)
v_mfma_f32_16x16x32_bf16 v[48:51], v[4:7], v[168:171], v[48:51]
ds_read_b64_tr_b16 v[22:23], v24 offset:25088
s_add_u32 s64, s63, 6
s_and_b32 s64, s64, 7
s_mul_i32 s64, s64, 458752
s_add_u32 s64, s64, s67
s_add_u32 m0, s57, 8192
s_add_u32 s59, s64, 0
buffer_load_dwordx4 v44, s[40:43], s59 offen lds
v_mfma_f32_16x16x32_bf16 v[56:59], v[4:7], v[172:175], v[56:59]
ds_read_b64_tr_b16 v[4:5], v25 offset:24576
ds_read_b64_tr_b16 v[128:129], v28 offset:16384
ds_read_b64_tr_b16 v[130:131], v28 offset:18432
v_mul_f32 v123, v115, v123
v_exp_f32 v122, v122
v_mul_f32 v124, v116, v124
v_mfma_f32_16x16x32_bf16 v[60:63], v[8:11], v[172:175], v[60:63]
ds_read_b64_tr_b16 v[6:7], v25 offset:25088
v_exp_f32 v123, v123
v_mul_f32 v125, v117, v125
s_add_u32 m0, s57, 12288
s_add_u32 s59, s64, 229376
buffer_load_dwordx4 v44, s[40:43], s59 offen lds
v_mfma_f32_16x16x32_bf16 v[52:55], v[8:11], v[168:171], v[52:55]
ds_read_b64_tr_b16 v[8:9], v26 offset:24576
)ASM"
R"ASM(
v_exp_f32 v124, v124
v_mul_f32 v126, v118, v126
v_exp_f32 v125, v125
s_waitcnt lgkmcnt(7)
v_mfma_f32_16x16x32_bf16 v[64:67], v[12:15], v[168:171], v[64:67]
ds_read_b64_tr_b16 v[10:11], v26 offset:25088
v_mul_f32 v127, v119, v127
v_exp_f32 v126, v126
v_exp_f32 v127, v127
v_mfma_f32_16x16x32_bf16 v[72:75], v[12:15], v[172:175], v[72:75]
ds_read_b64_tr_b16 v[12:13], v27 offset:24576
ds_read_b64_tr_b16 v[132:133], v28 offset:16392
ds_read_b64_tr_b16 v[134:135], v28 offset:18440
v_add_f32 v120, 1.0, v120
v_add_f32 v121, 1.0, v121
v_rcp_f32 v120, v120
v_mfma_f32_16x16x32_bf16 v[76:79], v[16:19], v[172:175], v[76:79]
ds_read_b64_tr_b16 v[14:15], v27 offset:25088
v_add_f32 v122, 1.0, v122
v_rcp_f32 v121, v121
v_add_f32 v123, 1.0, v123
s_add_u32 m0, s57, 49152
s_add_u32 s59, s64, 3670016
buffer_load_dwordx4 v44, s[40:43], s59 offen lds
v_mfma_f32_16x16x32_bf16 v[68:71], v[16:19], v[168:171], v[68:71]
ds_read_b64_tr_b16 v[16:17], v24 offset:28672
v_rcp_f32 v122, v122
v_add_f32 v124, 1.0, v124
v_rcp_f32 v123, v123
v_add_f32 v125, 1.0, v125
s_waitcnt lgkmcnt(7)
v_mfma_f32_16x16x32_bf16 v[48:51], v[20:23], v[176:179], v[48:51]
ds_read_b64_tr_b16 v[18:19], v24 offset:29184
v_rcp_f32 v124, v124
s_add_u32 m0, s57, 53248
s_add_u32 s59, s64, 3899392
buffer_load_dwordx4 v44, s[40:43], s59 offen lds
v_mfma_f32_16x16x32_bf16 v[56:59], v[20:23], v[180:183], v[56:59]
ds_read_b64_tr_b16 v[20:21], v25 offset:28672
v_add_f32 v126, 1.0, v126
v_rcp_f32 v125, v125
v_add_f32 v127, 1.0, v127
v_mfma_f32_16x16x32_bf16 v[60:63], v[4:7], v[180:183], v[60:63]
ds_read_b64_tr_b16 v[22:23], v25 offset:29184
v_rcp_f32 v126, v126
v_rcp_f32 v127, v127
v_mul_f32 v112, v112, v120
v_mfma_f32_16x16x32_bf16 v[52:55], v[4:7], v[176:179], v[52:55]
ds_read_b64_tr_b16 v[4:5], v26 offset:28672
v_mul_f32 v113, v113, v121
v_mul_f32 v114, v114, v122
v_mul_f32 v115, v115, v123
s_waitcnt lgkmcnt(5)
v_mfma_f32_16x16x32_bf16 v[64:67], v[8:11], v[176:179], v[64:67]
ds_read_b64_tr_b16 v[6:7], v26 offset:29184
v_mul_f32 v116, v116, v124
v_mul_f32 v117, v117, v125
v_mul_f32 v118, v118, v126
v_mfma_f32_16x16x32_bf16 v[72:75], v[8:11], v[180:183], v[72:75]
ds_read_b64_tr_b16 v[8:9], v27 offset:28672
v_mul_f32 v119, v119, v127
v_cvt_pk_bf16_f32 v112, v112, v113
v_cvt_pk_bf16_f32 v113, v114, v115
v_mfma_f32_16x16x32_bf16 v[76:79], v[12:15], v[180:183], v[76:79]
ds_read_b64_tr_b16 v[10:11], v27 offset:29184
v_cvt_pk_bf16_f32 v114, v116, v117
v_cvt_pk_bf16_f32 v115, v118, v119
s_add_u32 s74, s80, 32768
buffer_store_dwordx4 v[112:115], v31, s[48:51], s74 offen sc1
v_mfma_f32_16x16x32_bf16 v[68:71], v[12:15], v[176:179], v[68:71]
s_waitcnt vmcnt(10)
s_barrier
ds_read_b64_tr_b16 v[12:13], v24 offset:32768
s_add_u32 s64, s66, 7
s_and_b32 s64, s64, 7
s_mul_i32 s64, s64, 458752
s_waitcnt lgkmcnt(5)
v_mfma_f32_16x16x32_bf16 v[48:51], v[16:19], v[184:187], v[48:51]
ds_read_b64_tr_b16 v[14:15], v24 offset:33280
s_add_u32 s64, s64, s67
s_add_u32 m0, s57, 16384
s_add_u32 s59, s64, 0
buffer_load_dwordx4 v44, s[40:43], s59 offen lds
v_mfma_f32_16x16x32_bf16 v[56:59], v[16:19], v[188:191], v[56:59]
ds_read_b64_tr_b16 v[16:17], v25 offset:32768
ds_read_b64_tr_b16 v[136:137], v28 offset:32768
ds_read_b64_tr_b16 v[138:139], v28 offset:34816
v_mfma_f32_16x16x32_bf16 v[60:63], v[20:23], v[188:191], v[60:63]
ds_read_b64_tr_b16 v[18:19], v25 offset:33280
s_add_u32 m0, s57, 20480
s_add_u32 s59, s64, 229376
buffer_load_dwordx4 v44, s[40:43], s59 offen lds
v_mfma_f32_16x16x32_bf16 v[52:55], v[20:23], v[184:187], v[52:55]
ds_read_b64_tr_b16 v[20:21], v26 offset:32768
s_waitcnt lgkmcnt(7)
v_mfma_f32_16x16x32_bf16 v[64:67], v[4:7], v[184:187], v[64:67]
ds_read_b64_tr_b16 v[22:23], v26 offset:33280
v_mfma_f32_16x16x32_bf16 v[72:75], v[4:7], v[188:191], v[72:75]
ds_read_b64_tr_b16 v[4:5], v27 offset:32768
ds_read_b64_tr_b16 v[140:141], v28 offset:32776
ds_read_b64_tr_b16 v[142:143], v28 offset:34824
v_mfma_f32_16x16x32_bf16 v[76:79], v[8:11], v[188:191], v[76:79]
ds_read_b64_tr_b16 v[6:7], v27 offset:33280
s_add_u32 m0, s57, 57344
s_add_u32 s59, s64, 3670016
buffer_load_dwordx4 v44, s[40:43], s59 offen lds
v_mfma_f32_16x16x32_bf16 v[68:71], v[8:11], v[184:187], v[68:71]
ds_read_b64_tr_b16 v[8:9], v24 offset:36864
s_add_u32 m0, s57, 61440
s_waitcnt lgkmcnt(7)
v_mfma_f32_16x16x32_bf16 v[48:51], v[12:15], v[192:195], v[48:51]
ds_read_b64_tr_b16 v[10:11], v24 offset:37376
s_add_u32 s59, s64, 3899392
buffer_load_dwordx4 v44, s[40:43], s59 offen lds
v_mfma_f32_16x16x32_bf16 v[56:59], v[12:15], v[196:199], v[56:59]
ds_read_b64_tr_b16 v[12:13], v25 offset:36864
v_mfma_f32_16x16x32_bf16 v[60:63], v[16:19], v[196:199], v[60:63]
ds_read_b64_tr_b16 v[14:15], v25 offset:37376
v_mfma_f32_16x16x32_bf16 v[52:55], v[16:19], v[192:195], v[52:55]
ds_read_b64_tr_b16 v[16:17], v26 offset:36864
s_waitcnt lgkmcnt(5)
v_mfma_f32_16x16x32_bf16 v[64:67], v[20:23], v[192:195], v[64:67]
ds_read_b64_tr_b16 v[18:19], v26 offset:37376
v_mfma_f32_16x16x32_bf16 v[72:75], v[20:23], v[196:199], v[72:75]
ds_read_b64_tr_b16 v[20:21], v27 offset:36864
v_mfma_f32_16x16x32_bf16 v[76:79], v[4:7], v[196:199], v[76:79]
ds_read_b64_tr_b16 v[22:23], v27 offset:37376
v_mfma_f32_16x16x32_bf16 v[68:71], v[4:7], v[192:195], v[68:71]
s_waitcnt vmcnt(10)
s_barrier
ds_read_b64_tr_b16 v[4:5], v24 offset:0
s_waitcnt lgkmcnt(5)
v_mfma_f32_16x16x32_bf16 v[48:51], v[8:11], v[200:203], v[48:51]
ds_read_b64_tr_b16 v[6:7], v24 offset:512
v_mfma_f32_16x16x32_bf16 v[56:59], v[8:11], v[204:207], v[56:59]
ds_read_b64_tr_b16 v[8:9], v25 offset:0
ds_read_b64_tr_b16 v[144:145], v28 offset:0
v_mfma_f32_16x16x32_bf16 v[60:63], v[12:15], v[204:207], v[60:63]
ds_read_b64_tr_b16 v[10:11], v25 offset:512
ds_read_b64_tr_b16 v[146:147], v28 offset:2048
v_mfma_f32_16x16x32_bf16 v[52:55], v[12:15], v[200:203], v[52:55]
ds_read_b64_tr_b16 v[12:13], v26 offset:0
s_waitcnt lgkmcnt(7)
v_mfma_f32_16x16x32_bf16 v[64:67], v[16:19], v[200:203], v[64:67]
ds_read_b64_tr_b16 v[14:15], v26 offset:512
v_mfma_f32_16x16x32_bf16 v[72:75], v[16:19], v[204:207], v[72:75]
ds_read_b64_tr_b16 v[16:17], v27 offset:0
ds_read_b64_tr_b16 v[148:149], v28 offset:8
v_mfma_f32_16x16x32_bf16 v[76:79], v[20:23], v[204:207], v[76:79]
ds_read_b64_tr_b16 v[18:19], v27 offset:512
ds_read_b64_tr_b16 v[150:151], v28 offset:2056
v_mfma_f32_16x16x32_bf16 v[68:71], v[20:23], v[200:203], v[68:71]
ds_read_b64_tr_b16 v[20:21], v24 offset:4096
s_waitcnt lgkmcnt(8)
v_mfma_f32_16x16x32_bf16 v[48:51], v[4:7], v[208:211], v[48:51]
ds_read_b64_tr_b16 v[22:23], v24 offset:4608
v_mfma_f32_16x16x32_bf16 v[56:59], v[4:7], v[212:215], v[56:59]
ds_read_b64_tr_b16 v[4:5], v25 offset:4096
v_mfma_f32_16x16x32_bf16 v[60:63], v[8:11], v[212:215], v[60:63]
ds_read_b64_tr_b16 v[6:7], v25 offset:4608
v_mfma_f32_16x16x32_bf16 v[52:55], v[8:11], v[208:211], v[52:55]
ds_read_b64_tr_b16 v[8:9], v26 offset:4096
s_waitcnt lgkmcnt(6)
v_mfma_f32_16x16x32_bf16 v[64:67], v[12:15], v[208:211], v[64:67]
ds_read_b64_tr_b16 v[10:11], v26 offset:4608
v_mfma_f32_16x16x32_bf16 v[72:75], v[12:15], v[212:215], v[72:75]
ds_read_b64_tr_b16 v[12:13], v27 offset:4096
v_mfma_f32_16x16x32_bf16 v[76:79], v[16:19], v[212:215], v[76:79]
ds_read_b64_tr_b16 v[14:15], v27 offset:4608
v_mfma_f32_16x16x32_bf16 v[68:71], v[16:19], v[208:211], v[68:71]
s_waitcnt lgkmcnt(8)
s_waitcnt vmcnt(5)
s_waitcnt vmcnt(4)
s_barrier
ds_read_b64_tr_b16 v[16:17], v24 offset:8192
s_add_u32 m0, s58, 24576
s_waitcnt lgkmcnt(5)
v_mfma_f32_16x16x32_bf16 v[48:51], v[20:23], v[216:219], v[48:51]
ds_read_b64_tr_b16 v[18:19], v24 offset:8704
s_add_u32 s59, s60, 0
buffer_load_dwordx4 v31, s[48:51], s59 offen sc1 lds
v_mfma_f32_16x16x32_bf16 v[56:59], v[20:23], v[220:223], v[56:59]
ds_read_b64_tr_b16 v[20:21], v25 offset:8192
ds_read_b64_tr_b16 v[152:153], v28 offset:16384
v_mfma_f32_16x16x32_bf16 v[60:63], v[4:7], v[220:223], v[60:63]
ds_read_b64_tr_b16 v[22:23], v25 offset:8704
ds_read_b64_tr_b16 v[154:155], v28 offset:18432
buffer_load_dwordx4 v31, s[48:51], s59 offen offset:1024 sc1 lds
v_mfma_f32_16x16x32_bf16 v[52:55], v[4:7], v[216:219], v[52:55]
ds_read_b64_tr_b16 v[4:5], v26 offset:8192
s_waitcnt lgkmcnt(7)
v_mfma_f32_16x16x32_bf16 v[64:67], v[8:11], v[216:219], v[64:67]
ds_read_b64_tr_b16 v[6:7], v26 offset:8704
v_mfma_f32_16x16x32_bf16 v[72:75], v[8:11], v[220:223], v[72:75]
ds_read_b64_tr_b16 v[8:9], v27 offset:8192
ds_read_b64_tr_b16 v[156:157], v28 offset:16392
v_mfma_f32_16x16x32_bf16 v[76:79], v[12:15], v[220:223], v[76:79]
ds_read_b64_tr_b16 v[10:11], v27 offset:8704
ds_read_b64_tr_b16 v[158:159], v28 offset:18440
buffer_load_dwordx4 v31, s[48:51], s59 offen offset:2048 sc1 lds
)ASM"
R"ASM(
v_mfma_f32_16x16x32_bf16 v[68:71], v[12:15], v[216:219], v[68:71]
ds_read_b64_tr_b16 v[12:13], v24 offset:12288
s_waitcnt lgkmcnt(8)
v_mfma_f32_16x16x32_bf16 v[48:51], v[16:19], v[224:227], v[48:51]
ds_read_b64_tr_b16 v[14:15], v24 offset:12800
buffer_load_dwordx4 v31, s[48:51], s59 offen offset:3072 sc1 lds
v_mfma_f32_16x16x32_bf16 v[56:59], v[16:19], v[228:231], v[56:59]
ds_read_b64_tr_b16 v[16:17], v25 offset:12288
v_mfma_f32_16x16x32_bf16 v[60:63], v[20:23], v[228:231], v[60:63]
ds_read_b64_tr_b16 v[18:19], v25 offset:12800
v_mfma_f32_16x16x32_bf16 v[52:55], v[20:23], v[224:227], v[52:55]
ds_read_b64_tr_b16 v[20:21], v26 offset:12288
s_waitcnt lgkmcnt(6)
v_mfma_f32_16x16x32_bf16 v[64:67], v[4:7], v[224:227], v[64:67]
ds_read_b64_tr_b16 v[22:23], v26 offset:12800
v_mfma_f32_16x16x32_bf16 v[72:75], v[4:7], v[228:231], v[72:75]
ds_read_b64_tr_b16 v[4:5], v27 offset:12288
v_mfma_f32_16x16x32_bf16 v[76:79], v[8:11], v[228:231], v[76:79]
ds_read_b64_tr_b16 v[6:7], v27 offset:12800
v_mfma_f32_16x16x32_bf16 v[68:71], v[8:11], v[224:227], v[68:71]
s_waitcnt lgkmcnt(8)
s_waitcnt vmcnt(4)
s_barrier
ds_read_b64_tr_b16 v[8:9], v24 offset:16384
s_add_u32 m0, s58, 32768
s_waitcnt lgkmcnt(5)
v_mfma_f32_16x16x32_bf16 v[48:51], v[12:15], v[232:235], v[48:51]
ds_read_b64_tr_b16 v[10:11], v24 offset:16896
s_add_u32 s59, s60, 57344
buffer_load_dwordx4 v31, s[48:51], s59 offen sc1 lds
v_mfma_f32_16x16x32_bf16 v[56:59], v[12:15], v[236:239], v[56:59]
ds_read_b64_tr_b16 v[12:13], v25 offset:16384
ds_read_b64_tr_b16 v[160:161], v28 offset:32768
v_mfma_f32_16x16x32_bf16 v[60:63], v[16:19], v[236:239], v[60:63]
ds_read_b64_tr_b16 v[14:15], v25 offset:16896
ds_read_b64_tr_b16 v[162:163], v28 offset:34816
buffer_load_dwordx4 v31, s[48:51], s59 offen offset:1024 sc1 lds
v_mfma_f32_16x16x32_bf16 v[52:55], v[16:19], v[232:235], v[52:55]
ds_read_b64_tr_b16 v[16:17], v26 offset:16384
s_waitcnt lgkmcnt(7)
v_mfma_f32_16x16x32_bf16 v[64:67], v[20:23], v[232:235], v[64:67]
ds_read_b64_tr_b16 v[18:19], v26 offset:16896
v_mfma_f32_16x16x32_bf16 v[72:75], v[20:23], v[236:239], v[72:75]
ds_read_b64_tr_b16 v[20:21], v27 offset:16384
ds_read_b64_tr_b16 v[164:165], v28 offset:32776
v_mfma_f32_16x16x32_bf16 v[76:79], v[4:7], v[236:239], v[76:79]
ds_read_b64_tr_b16 v[22:23], v27 offset:16896
ds_read_b64_tr_b16 v[166:167], v28 offset:34824
buffer_load_dwordx4 v31, s[48:51], s59 offen offset:2048 sc1 lds
v_mfma_f32_16x16x32_bf16 v[68:71], v[4:7], v[232:235], v[68:71]
ds_read_b64_tr_b16 v[4:5], v24 offset:20480
s_waitcnt lgkmcnt(8)
v_mfma_f32_16x16x32_bf16 v[48:51], v[8:11], v[240:243], v[48:51]
ds_read_b64_tr_b16 v[6:7], v24 offset:20992
buffer_load_dwordx4 v31, s[48:51], s59 offen offset:3072 sc1 lds
v_mfma_f32_16x16x32_bf16 v[56:59], v[8:11], v[244:247], v[56:59]
ds_read_b64_tr_b16 v[8:9], v25 offset:20480
v_mfma_f32_16x16x32_bf16 v[60:63], v[12:15], v[244:247], v[60:63]
ds_read_b64_tr_b16 v[10:11], v25 offset:20992
v_mfma_f32_16x16x32_bf16 v[52:55], v[12:15], v[240:243], v[52:55]
ds_read_b64_tr_b16 v[12:13], v26 offset:20480
s_waitcnt lgkmcnt(6)
v_mfma_f32_16x16x32_bf16 v[64:67], v[16:19], v[240:243], v[64:67]
ds_read_b64_tr_b16 v[14:15], v26 offset:20992
v_mfma_f32_16x16x32_bf16 v[72:75], v[16:19], v[244:247], v[72:75]
ds_read_b64_tr_b16 v[16:17], v27 offset:20480
v_mfma_f32_16x16x32_bf16 v[76:79], v[20:23], v[244:247], v[76:79]
ds_read_b64_tr_b16 v[18:19], v27 offset:20992
v_mfma_f32_16x16x32_bf16 v[68:71], v[20:23], v[240:243], v[68:71]
s_waitcnt lgkmcnt(8)
s_waitcnt vmcnt(4)
s_barrier
v_add_u32 v24, 0, v31
ds_read_b128 v[20:23], v24 offset:24576
s_add_u32 m0, s58, 0
s_waitcnt lgkmcnt(5)
v_mfma_f32_16x16x32_bf16 v[48:51], v[4:7], v[248:251], v[48:51]
s_add_u32 s59, s60, 8192
buffer_load_dwordx4 v31, s[48:51], s59 offen sc1 lds
v_mfma_f32_16x16x32_bf16 v[56:59], v[4:7], v[252:255], v[56:59]
ds_read_b128 v[4:7], v24 offset:25600
ds_read_b64_tr_b16 v[168:169], v28 offset:0
v_mfma_f32_16x16x32_bf16 v[60:63], v[8:11], v[252:255], v[60:63]
ds_read_b64_tr_b16 v[170:171], v28 offset:2048
buffer_load_dwordx4 v31, s[48:51], s59 offen offset:1024 sc1 lds
v_mfma_f32_16x16x32_bf16 v[52:55], v[8:11], v[248:251], v[52:55]
ds_read_b128 v[8:11], v24 offset:26624
s_waitcnt lgkmcnt(5)
v_mfma_f32_16x16x32_bf16 v[64:67], v[12:15], v[248:251], v[64:67]
ds_write_b128 v30, v[56:59] offset:51200
v_mfma_f32_16x16x32_bf16 v[72:75], v[12:15], v[252:255], v[72:75]
ds_read_b128 v[12:15], v24 offset:27648
ds_read_b64_tr_b16 v[172:173], v28 offset:8
ds_read_b64_tr_b16 v[174:175], v28 offset:2056
v_mfma_f32_16x16x32_bf16 v[76:79], v[16:19], v[252:255], v[76:79]
ds_write_b128 v30, v[60:63] offset:52224
buffer_load_dwordx4 v31, s[48:51], s59 offen offset:2048 sc1 lds
v_mfma_f32_16x16x32_bf16 v[68:71], v[16:19], v[248:251], v[68:71]
ds_read_b128 v[16:19], v24 offset:28672
ds_write_b128 v30, v[72:75] offset:34816
s_waitcnt lgkmcnt(10)
v_mfma_f32_16x16x32_bf16 v[80:83], v[128:131], v[20:23], 0
buffer_load_dwordx4 v31, s[48:51], s59 offen offset:3072 sc1 lds
v_mfma_f32_16x16x32_bf16 v[84:87], v[132:135], v[20:23], 0
ds_read_b128 v[20:23], v24 offset:29696
v_mfma_f32_16x16x32_bf16 v[92:95], v[132:135], v[4:7], 0
ds_write_b128 v30, v[76:79] offset:35840
v_mfma_f32_16x16x32_bf16 v[88:91], v[128:131], v[4:7], 0
ds_read_b128 v[4:7], v24 offset:30720
s_waitcnt lgkmcnt(8)
v_mfma_f32_16x16x32_bf16 v[96:99], v[128:131], v[8:11], 0
v_mfma_f32_16x16x32_bf16 v[100:103], v[132:135], v[8:11], 0
ds_read_b128 v[8:11], v24 offset:31744
v_mfma_f32_16x16x32_bf16 v[108:111], v[132:135], v[12:15], 0
v_mfma_f32_16x16x32_bf16 v[104:107], v[128:131], v[12:15], 0
s_waitcnt lgkmcnt(7)
s_waitcnt vmcnt(4)
s_waitcnt lgkmcnt(6)
s_waitcnt lgkmcnt(2)
s_barrier
ds_read_b128 v[12:15], v24 offset:32768
ds_read_b128 v[56:59], v30 offset:49152
ds_read_b128 v[60:63], v30 offset:50176
ds_read_b128 v[72:75], v30 offset:32768
v_mfma_f32_16x16x32_bf16 v[112:115], v[128:131], v[16:19], 0
ds_read_b128 v[76:79], v30 offset:33792
s_add_u32 m0, s58, 8192
s_add_u32 s59, s60, 65536
buffer_load_dwordx4 v31, s[48:51], s59 offen sc1 lds
v_mfma_f32_16x16x32_bf16 v[116:119], v[132:135], v[16:19], 0
ds_read_b128 v[16:19], v24 offset:33792
ds_read_b64_tr_b16 v[176:177], v28 offset:16384
v_mfma_f32_16x16x32_bf16 v[124:127], v[132:135], v[20:23], 0
ds_read_b64_tr_b16 v[178:179], v28 offset:18432
buffer_load_dwordx4 v31, s[48:51], s59 offen offset:1024 sc1 lds
v_mfma_f32_16x16x32_bf16 v[120:123], v[128:131], v[20:23], 0
ds_read_b128 v[20:23], v24 offset:34816
s_waitcnt lgkmcnt(9)
v_mfma_f32_16x16x32_bf16 v[240:243], v[128:131], v[4:7], 0
v_mfma_f32_16x16x32_bf16 v[244:247], v[132:135], v[4:7], 0
ds_read_b128 v[4:7], v24 offset:35840
ds_read_b64_tr_b16 v[180:181], v28 offset:16392
v_mfma_f32_16x16x32_bf16 v[252:255], v[132:135], v[8:11], 0
ds_read_b64_tr_b16 v[182:183], v28 offset:18440
buffer_load_dwordx4 v31, s[48:51], s59 offen offset:2048 sc1 lds
v_mfma_f32_16x16x32_bf16 v[248:251], v[128:131], v[8:11], 0
ds_read_b128 v[8:11], v24 offset:36864
s_waitcnt lgkmcnt(11)
v_add_f32 v48, v48, v56
v_add_f32 v49, v49, v57
v_add_f32 v50, v50, v58
s_waitcnt lgkmcnt(7)
v_mfma_f32_16x16x32_bf16 v[80:83], v[136:139], v[12:15], v[80:83]
v_add_f32 v51, v51, v59
v_add_f32 v52, v52, v60
v_add_f32 v53, v53, v61
buffer_load_dwordx4 v31, s[48:51], s59 offen offset:3072 sc1 lds
v_mfma_f32_16x16x32_bf16 v[84:87], v[140:143], v[12:15], v[84:87]
ds_read_b128 v[12:15], v24 offset:37888
v_add_f32 v54, v54, v62
v_add_f32 v55, v55, v63
v_mul_f32 v56, v48, v48
v_mfma_f32_16x16x32_bf16 v[92:95], v[140:143], v[16:19], v[92:95]
v_mul_f32 v57, v49, v49
v_mul_f32 v58, v50, v50
v_mul_f32 v59, v51, v51
v_mfma_f32_16x16x32_bf16 v[88:91], v[136:139], v[16:19], v[88:91]
ds_read_b128 v[16:19], v24 offset:38912
v_mul_f32 v60, v52, v52
v_mul_f32 v61, v53, v53
s_waitcnt lgkmcnt(5)
v_mfma_f32_16x16x32_bf16 v[96:99], v[136:139], v[20:23], v[96:99]
v_mul_f32 v62, v54, v54
v_mul_f32 v63, v55, v55
v_fma_f32 v56, v56, v32, s82
v_mfma_f32_16x16x32_bf16 v[100:103], v[140:143], v[20:23], v[100:103]
ds_read_b128 v[20:23], v24 offset:39936
v_fma_f32 v57, v57, v32, s82
v_fma_f32 v58, v58, v32, s82
v_fma_f32 v59, v59, v32, s82
v_mfma_f32_16x16x32_bf16 v[108:111], v[140:143], v[4:7], v[108:111]
v_fma_f32 v60, v60, v32, s82
v_fma_f32 v61, v61, v32, s82
v_fma_f32 v62, v62, v32, s82
v_mfma_f32_16x16x32_bf16 v[104:107], v[136:139], v[4:7], v[104:107]
s_waitcnt lgkmcnt(4)
s_waitcnt vmcnt(4)
s_barrier
ds_read_b128 v[4:7], v24 offset:0
v_fma_f32 v63, v63, v32, s82
v_mul_f32 v56, v48, v56
v_mul_f32 v57, v49, v57
v_exp_f32 v56, v56
v_mul_f32 v58, v50, v58
v_exp_f32 v57, v57
s_add_u32 m0, s58, 16384
s_add_u32 s59, s60, 16384
buffer_load_dwordx4 v31, s[48:51], s59 offen sc1 lds
s_waitcnt lgkmcnt(3)
v_mfma_f32_16x16x32_bf16 v[112:115], v[136:139], v[8:11], v[112:115]
)ASM"
R"ASM(
v_bfe_u32 v1, v0, 4, 2
v_lshlrev_b32 v40, 4, v1
s_mov_b32 s52, s16
s_and_b32 s53, s17, 0xffff
s_mov_b32 s54, 2048
s_lshl_b32 s75, s56, 6
s_lshl_b32 s73, s78, 8
s_add_u32 s75, s75, s73
buffer_load_dwordx4 v[36:39], v40, s[52:55], s75 offen
v_mfma_f32_16x16x32_bf16 v[116:119], v[140:143], v[8:11], v[116:119]
ds_read_b128 v[8:11], v24 offset:1024
v_mul_f32 v59, v51, v59
v_exp_f32 v58, v58
v_mul_f32 v60, v52, v60
v_mfma_f32_16x16x32_bf16 v[124:127], v[140:143], v[12:15], v[124:127]
v_exp_f32 v59, v59
v_mul_f32 v61, v53, v61
buffer_load_dwordx4 v31, s[48:51], s59 offen offset:1024 sc1 lds
v_mfma_f32_16x16x32_bf16 v[120:123], v[136:139], v[12:15], v[120:123]
ds_read_b128 v[12:15], v24 offset:2048
v_exp_f32 v60, v60
v_mul_f32 v62, v54, v62
v_exp_f32 v61, v61
s_waitcnt lgkmcnt(3)
v_mfma_f32_16x16x32_bf16 v[240:243], v[136:139], v[16:19], v[240:243]
v_mul_f32 v63, v55, v63
v_exp_f32 v62, v62
v_exp_f32 v63, v63
v_mfma_f32_16x16x32_bf16 v[244:247], v[140:143], v[16:19], v[244:247]
ds_read_b128 v[16:19], v24 offset:3072
v_add_f32 v56, 1.0, v56
v_add_f32 v57, 1.0, v57
v_rcp_f32 v56, v56
v_mfma_f32_16x16x32_bf16 v[252:255], v[140:143], v[20:23], v[252:255]
v_add_f32 v58, 1.0, v58
v_rcp_f32 v57, v57
v_add_f32 v59, 1.0, v59
buffer_load_dwordx4 v31, s[48:51], s59 offen offset:2048 sc1 lds
v_mfma_f32_16x16x32_bf16 v[248:251], v[136:139], v[20:23], v[248:251]
ds_read_b128 v[20:23], v24 offset:4096
v_rcp_f32 v58, v58
v_add_f32 v60, 1.0, v60
v_rcp_f32 v59, v59
s_waitcnt lgkmcnt(3)
v_mfma_f32_16x16x32_bf16 v[80:83], v[144:147], v[4:7], v[80:83]
v_add_f32 v61, 1.0, v61
v_rcp_f32 v60, v60
buffer_load_dwordx4 v31, s[48:51], s59 offen offset:3072 sc1 lds
v_mfma_f32_16x16x32_bf16 v[84:87], v[148:151], v[4:7], v[84:87]
ds_read_b128 v[4:7], v24 offset:5120
v_add_f32 v62, 1.0, v62
v_rcp_f32 v61, v61
v_add_f32 v63, 1.0, v63
v_mfma_f32_16x16x32_bf16 v[92:95], v[148:151], v[8:11], v[92:95]
v_rcp_f32 v62, v62
v_rcp_f32 v63, v63
v_mul_f32 v48, v48, v56
v_mfma_f32_16x16x32_bf16 v[88:91], v[144:147], v[8:11], v[88:91]
ds_read_b128 v[8:11], v24 offset:6144
v_mul_f32 v49, v49, v57
v_mul_f32 v50, v50, v58
v_mul_f32 v51, v51, v59
s_waitcnt lgkmcnt(3)
v_mfma_f32_16x16x32_bf16 v[96:99], v[144:147], v[12:15], v[96:99]
v_mul_f32 v52, v52, v60
v_mul_f32 v53, v53, v61
v_mul_f32 v54, v54, v62
v_mfma_f32_16x16x32_bf16 v[100:103], v[148:151], v[12:15], v[100:103]
ds_read_b128 v[12:15], v24 offset:7168
v_mul_f32 v55, v55, v63
v_cvt_pk_bf16_f32 v48, v48, v49
v_cvt_pk_bf16_f32 v49, v50, v51
v_mfma_f32_16x16x32_bf16 v[108:111], v[148:151], v[16:19], v[108:111]
v_cvt_pk_bf16_f32 v50, v52, v53
v_cvt_pk_bf16_f32 v51, v54, v55
s_add_u32 s74, s80, 40960
buffer_store_dwordx4 v[48:51], v31, s[48:51], s74 offen sc1
v_mfma_f32_16x16x32_bf16 v[104:107], v[144:147], v[16:19], v[104:107]
s_waitcnt vmcnt(6)
s_barrier
ds_read_b128 v[16:19], v24 offset:8192
v_add_f32 v64, v64, v72
v_add_f32 v65, v65, v73
v_add_f32 v66, v66, v74
v_add_f32 v67, v67, v75
s_waitcnt lgkmcnt(3)
v_mfma_f32_16x16x32_bf16 v[112:115], v[144:147], v[20:23], v[112:115]
v_add_f32 v68, v68, v76
v_add_f32 v69, v69, v77
s_add_u32 m0, s58, 24576
s_add_u32 s59, s60, 73728
buffer_load_dwordx4 v31, s[48:51], s59 offen sc1 lds
v_mfma_f32_16x16x32_bf16 v[116:119], v[148:151], v[20:23], v[116:119]
ds_read_b128 v[20:23], v24 offset:9216
ds_read_b64_tr_b16 v[184:185], v28 offset:32768
ds_read_b64_tr_b16 v[186:187], v28 offset:34816
v_add_f32 v70, v70, v78
v_add_f32 v71, v71, v79
v_mfma_f32_16x16x32_bf16 v[124:127], v[148:151], v[4:7], v[124:127]
v_mul_f32 v72, v64, v64
v_mul_f32 v73, v65, v65
v_mul_f32 v74, v66, v66
v_mul_f32 v75, v67, v67
buffer_load_dwordx4 v31, s[48:51], s59 offen offset:1024 sc1 lds
v_mfma_f32_16x16x32_bf16 v[120:123], v[144:147], v[4:7], v[120:123]
ds_read_b128 v[4:7], v24 offset:10240
v_mul_f32 v76, v68, v68
v_mul_f32 v77, v69, v69
s_waitcnt lgkmcnt(5)
v_mfma_f32_16x16x32_bf16 v[240:243], v[144:147], v[8:11], v[240:243]
v_mul_f32 v78, v70, v70
v_mul_f32 v79, v71, v71
v_fma_f32 v72, v72, v32, s82
v_mfma_f32_16x16x32_bf16 v[244:247], v[148:151], v[8:11], v[244:247]
ds_read_b128 v[8:11], v24 offset:11264
ds_read_b64_tr_b16 v[188:189], v28 offset:32776
ds_read_b64_tr_b16 v[190:191], v28 offset:34824
v_fma_f32 v73, v73, v32, s82
v_fma_f32 v74, v74, v32, s82
v_mfma_f32_16x16x32_bf16 v[252:255], v[148:151], v[12:15], v[252:255]
v_fma_f32 v75, v75, v32, s82
v_fma_f32 v76, v76, v32, s82
v_fma_f32 v77, v77, v32, s82
v_fma_f32 v78, v78, v32, s82
buffer_load_dwordx4 v31, s[48:51], s59 offen offset:2048 sc1 lds
v_mfma_f32_16x16x32_bf16 v[248:251], v[144:147], v[12:15], v[248:251]
ds_read_b128 v[12:15], v24 offset:12288
v_fma_f32 v79, v79, v32, s82
v_mul_f32 v72, v64, v72
v_mul_f32 v73, v65, v73
s_waitcnt lgkmcnt(7)
v_mfma_f32_16x16x32_bf16 v[80:83], v[152:155], v[16:19], v[80:83]
v_exp_f32 v72, v72
v_mul_f32 v74, v66, v74
v_exp_f32 v73, v73
buffer_load_dwordx4 v31, s[48:51], s59 offen offset:3072 sc1 lds
v_mfma_f32_16x16x32_bf16 v[84:87], v[156:159], v[16:19], v[84:87]
ds_read_b128 v[16:19], v24 offset:13312
v_mul_f32 v75, v67, v75
v_exp_f32 v74, v74
v_mfma_f32_16x16x32_bf16 v[92:95], v[156:159], v[20:23], v[92:95]
v_mul_f32 v76, v68, v76
v_exp_f32 v75, v75
v_mul_f32 v77, v69, v77
v_mfma_f32_16x16x32_bf16 v[88:91], v[152:155], v[20:23], v[88:91]
ds_read_b128 v[20:23], v24 offset:14336
v_exp_f32 v76, v76
v_mul_f32 v78, v70, v78
v_exp_f32 v77, v77
s_waitcnt lgkmcnt(5)
v_mfma_f32_16x16x32_bf16 v[96:99], v[152:155], v[4:7], v[96:99]
v_mul_f32 v79, v71, v79
v_exp_f32 v78, v78
v_exp_f32 v79, v79
v_mfma_f32_16x16x32_bf16 v[100:103], v[156:159], v[4:7], v[100:103]
ds_read_b128 v[4:7], v24 offset:15360
v_add_f32 v72, 1.0, v72
v_add_f32 v73, 1.0, v73
v_rcp_f32 v72, v72
v_mfma_f32_16x16x32_bf16 v[108:111], v[156:159], v[8:11], v[108:111]
v_add_f32 v74, 1.0, v74
v_rcp_f32 v73, v73
v_add_f32 v75, 1.0, v75
v_mfma_f32_16x16x32_bf16 v[104:107], v[152:155], v[8:11], v[104:107]
s_waitcnt lgkmcnt(4)
s_waitcnt vmcnt(5)
s_barrier
ds_read_b128 v[8:11], v24 offset:16384
v_rcp_f32 v74, v74
v_add_f32 v76, 1.0, v76
v_rcp_f32 v75, v75
v_add_f32 v77, 1.0, v77
s_waitcnt lgkmcnt(3)
v_mfma_f32_16x16x32_bf16 v[112:115], v[152:155], v[12:15], v[112:115]
v_rcp_f32 v76, v76
s_add_u32 m0, s58, 32768
s_add_u32 s59, s60, 24576
buffer_load_dwordx4 v31, s[48:51], s59 offen sc1 lds
v_mfma_f32_16x16x32_bf16 v[116:119], v[156:159], v[12:15], v[116:119]
ds_read_b128 v[12:15], v24 offset:17408
v_add_f32 v78, 1.0, v78
v_rcp_f32 v77, v77
v_add_f32 v79, 1.0, v79
v_mfma_f32_16x16x32_bf16 v[124:127], v[156:159], v[16:19], v[124:127]
v_rcp_f32 v78, v78
v_rcp_f32 v79, v79
v_mul_f32 v64, v64, v72
buffer_load_dwordx4 v31, s[48:51], s59 offen offset:1024 sc1 lds
v_mfma_f32_16x16x32_bf16 v[120:123], v[152:155], v[16:19], v[120:123]
ds_read_b128 v[16:19], v24 offset:18432
v_mul_f32 v65, v65, v73
v_mul_f32 v66, v66, v74
v_mul_f32 v67, v67, v75
s_waitcnt lgkmcnt(3)
v_mfma_f32_16x16x32_bf16 v[240:243], v[152:155], v[20:23], v[240:243]
v_mul_f32 v68, v68, v76
v_mul_f32 v69, v69, v77
v_mul_f32 v70, v70, v78
v_mfma_f32_16x16x32_bf16 v[244:247], v[156:159], v[20:23], v[244:247]
ds_read_b128 v[20:23], v24 offset:19456
)ASM"
R"ASM(
v_mul_f32 v71, v71, v79
v_cvt_pk_bf16_f32 v64, v64, v65
v_cvt_pk_bf16_f32 v65, v66, v67
v_mfma_f32_16x16x32_bf16 v[252:255], v[156:159], v[4:7], v[252:255]
v_cvt_pk_bf16_f32 v66, v68, v69
v_cvt_pk_bf16_f32 v67, v70, v71
buffer_load_dwordx4 v31, s[48:51], s59 offen offset:2048 sc1 lds
v_mfma_f32_16x16x32_bf16 v[248:251], v[152:155], v[4:7], v[248:251]
ds_read_b128 v[4:7], v24 offset:20480
s_waitcnt lgkmcnt(3)
v_mfma_f32_16x16x32_bf16 v[80:83], v[160:163], v[8:11], v[80:83]
buffer_load_dwordx4 v31, s[48:51], s59 offen offset:3072 sc1 lds
v_mfma_f32_16x16x32_bf16 v[84:87], v[164:167], v[8:11], v[84:87]
ds_read_b128 v[8:11], v24 offset:21504
v_mfma_f32_16x16x32_bf16 v[92:95], v[164:167], v[12:15], v[92:95]
v_mfma_f32_16x16x32_bf16 v[88:91], v[160:163], v[12:15], v[88:91]
ds_read_b128 v[12:15], v24 offset:22528
s_add_u32 s74, s80, 49152
s_waitcnt lgkmcnt(3)
v_mfma_f32_16x16x32_bf16 v[96:99], v[160:163], v[16:19], v[96:99]
buffer_store_dwordx4 v[64:67], v31, s[48:51], s74 offen sc1
v_mfma_f32_16x16x32_bf16 v[100:103], v[164:167], v[16:19], v[100:103]
ds_read_b128 v[16:19], v24 offset:23552
v_mfma_f32_16x16x32_bf16 v[108:111], v[164:167], v[20:23], v[108:111]
v_mfma_f32_16x16x32_bf16 v[104:107], v[160:163], v[20:23], v[104:107]
s_waitcnt vmcnt(5)
s_barrier
ds_read_b128 v[20:23], v24 offset:24576
s_add_u32 m0, s58, 0
s_waitcnt lgkmcnt(3)
v_mfma_f32_16x16x32_bf16 v[112:115], v[160:163], v[4:7], v[112:115]
s_add_u32 s59, s60, 81920
buffer_load_dwordx4 v31, s[48:51], s59 offen sc1 lds
v_mfma_f32_16x16x32_bf16 v[116:119], v[164:167], v[4:7], v[116:119]
ds_read_b128 v[4:7], v24 offset:25600
ds_read_b64_tr_b16 v[192:193], v28 offset:0
v_mfma_f32_16x16x32_bf16 v[124:127], v[164:167], v[8:11], v[124:127]
ds_read_b64_tr_b16 v[194:195], v28 offset:2048
buffer_load_dwordx4 v31, s[48:51], s59 offen offset:1024 sc1 lds
v_mfma_f32_16x16x32_bf16 v[120:123], v[160:163], v[8:11], v[120:123]
ds_read_b128 v[8:11], v24 offset:26624
s_waitcnt lgkmcnt(5)
v_mfma_f32_16x16x32_bf16 v[240:243], v[160:163], v[12:15], v[240:243]
v_mfma_f32_16x16x32_bf16 v[244:247], v[164:167], v[12:15], v[244:247]
ds_read_b128 v[12:15], v24 offset:27648
ds_read_b64_tr_b16 v[196:197], v28 offset:8
v_mfma_f32_16x16x32_bf16 v[252:255], v[164:167], v[16:19], v[252:255]
ds_read_b64_tr_b16 v[198:199], v28 offset:2056
buffer_load_dwordx4 v31, s[48:51], s59 offen offset:2048 sc1 lds
v_mfma_f32_16x16x32_bf16 v[248:251], v[160:163], v[16:19], v[248:251]
ds_read_b128 v[16:19], v24 offset:28672
s_waitcnt lgkmcnt(7)
v_mfma_f32_16x16x32_bf16 v[80:83], v[168:171], v[20:23], v[80:83]
buffer_load_dwordx4 v31, s[48:51], s59 offen offset:3072 sc1 lds
v_mfma_f32_16x16x32_bf16 v[84:87], v[172:175], v[20:23], v[84:87]
ds_read_b128 v[20:23], v24 offset:29696
v_mfma_f32_16x16x32_bf16 v[92:95], v[172:175], v[4:7], v[92:95]
v_mfma_f32_16x16x32_bf16 v[88:91], v[168:171], v[4:7], v[88:91]
ds_read_b128 v[4:7], v24 offset:30720
s_waitcnt lgkmcnt(5)
v_mfma_f32_16x16x32_bf16 v[96:99], v[168:171], v[8:11], v[96:99]
v_mfma_f32_16x16x32_bf16 v[100:103], v[172:175], v[8:11], v[100:103]
ds_read_b128 v[8:11], v24 offset:31744
v_mfma_f32_16x16x32_bf16 v[108:111], v[172:175], v[12:15], v[108:111]
v_mfma_f32_16x16x32_bf16 v[104:107], v[168:171], v[12:15], v[104:107]
s_waitcnt lgkmcnt(4)
s_waitcnt vmcnt(5)
s_barrier
ds_read_b128 v[12:15], v24 offset:32768
s_add_u32 m0, s58, 8192
s_waitcnt lgkmcnt(3)
v_mfma_f32_16x16x32_bf16 v[112:115], v[168:171], v[16:19], v[112:115]
s_add_u32 s59, s60, 32768
buffer_load_dwordx4 v31, s[48:51], s59 offen sc1 lds
v_mfma_f32_16x16x32_bf16 v[116:119], v[172:175], v[16:19], v[116:119]
ds_read_b128 v[16:19], v24 offset:33792
ds_read_b64_tr_b16 v[200:201], v28 offset:16384
v_mfma_f32_16x16x32_bf16 v[124:127], v[172:175], v[20:23], v[124:127]
ds_read_b64_tr_b16 v[202:203], v28 offset:18432
buffer_load_dwordx4 v31, s[48:51], s59 offen offset:1024 sc1 lds
v_mfma_f32_16x16x32_bf16 v[120:123], v[168:171], v[20:23], v[120:123]
ds_read_b128 v[20:23], v24 offset:34816
s_waitcnt lgkmcnt(5)
v_mfma_f32_16x16x32_bf16 v[240:243], v[168:171], v[4:7], v[240:243]
v_mfma_f32_16x16x32_bf16 v[244:247], v[172:175], v[4:7], v[244:247]
ds_read_b128 v[4:7], v24 offset:35840
ds_read_b64_tr_b16 v[204:205], v28 offset:16392
v_mfma_f32_16x16x32_bf16 v[252:255], v[172:175], v[8:11], v[252:255]
ds_read_b64_tr_b16 v[206:207], v28 offset:18440
buffer_load_dwordx4 v31, s[48:51], s59 offen offset:2048 sc1 lds
v_mfma_f32_16x16x32_bf16 v[248:251], v[168:171], v[8:11], v[248:251]
ds_read_b128 v[8:11], v24 offset:36864
s_waitcnt lgkmcnt(7)
v_mfma_f32_16x16x32_bf16 v[80:83], v[176:179], v[12:15], v[80:83]
buffer_load_dwordx4 v31, s[48:51], s59 offen offset:3072 sc1 lds
v_mfma_f32_16x16x32_bf16 v[84:87], v[180:183], v[12:15], v[84:87]
ds_read_b128 v[12:15], v24 offset:37888
v_mfma_f32_16x16x32_bf16 v[92:95], v[180:183], v[16:19], v[92:95]
v_mfma_f32_16x16x32_bf16 v[88:91], v[176:179], v[16:19], v[88:91]
ds_read_b128 v[16:19], v24 offset:38912
s_waitcnt lgkmcnt(5)
v_mfma_f32_16x16x32_bf16 v[96:99], v[176:179], v[20:23], v[96:99]
v_mfma_f32_16x16x32_bf16 v[100:103], v[180:183], v[20:23], v[100:103]
ds_read_b128 v[20:23], v24 offset:39936
v_mfma_f32_16x16x32_bf16 v[108:111], v[180:183], v[4:7], v[108:111]
v_mfma_f32_16x16x32_bf16 v[104:107], v[176:179], v[4:7], v[104:107]
s_waitcnt lgkmcnt(4)
s_waitcnt vmcnt(4)
s_barrier
ds_read_b128 v[4:7], v24 offset:0
s_add_u32 m0, s58, 16384
s_waitcnt lgkmcnt(3)
v_mfma_f32_16x16x32_bf16 v[112:115], v[176:179], v[8:11], v[112:115]
s_add_u32 s59, s60, 90112
buffer_load_dwordx4 v31, s[48:51], s59 offen sc1 lds
v_mfma_f32_16x16x32_bf16 v[116:119], v[180:183], v[8:11], v[116:119]
ds_read_b128 v[8:11], v24 offset:1024
v_mfma_f32_16x16x32_bf16 v[124:127], v[180:183], v[12:15], v[124:127]
buffer_load_dwordx4 v31, s[48:51], s59 offen offset:1024 sc1 lds
v_mfma_f32_16x16x32_bf16 v[120:123], v[176:179], v[12:15], v[120:123]
ds_read_b128 v[12:15], v24 offset:2048
s_waitcnt lgkmcnt(3)
v_mfma_f32_16x16x32_bf16 v[240:243], v[176:179], v[16:19], v[240:243]
v_mfma_f32_16x16x32_bf16 v[244:247], v[180:183], v[16:19], v[244:247]
ds_read_b128 v[16:19], v24 offset:3072
v_mfma_f32_16x16x32_bf16 v[252:255], v[180:183], v[20:23], v[252:255]
buffer_load_dwordx4 v31, s[48:51], s59 offen offset:2048 sc1 lds
v_mfma_f32_16x16x32_bf16 v[248:251], v[176:179], v[20:23], v[248:251]
ds_read_b128 v[20:23], v24 offset:4096
s_waitcnt lgkmcnt(3)
v_mfma_f32_16x16x32_bf16 v[80:83], v[184:187], v[4:7], v[80:83]
buffer_load_dwordx4 v31, s[48:51], s59 offen offset:3072 sc1 lds
v_mfma_f32_16x16x32_bf16 v[84:87], v[188:191], v[4:7], v[84:87]
ds_read_b128 v[4:7], v24 offset:5120
v_mfma_f32_16x16x32_bf16 v[92:95], v[188:191], v[8:11], v[92:95]
v_mfma_f32_16x16x32_bf16 v[88:91], v[184:187], v[8:11], v[88:91]
ds_read_b128 v[8:11], v24 offset:6144
s_waitcnt lgkmcnt(3)
v_mfma_f32_16x16x32_bf16 v[96:99], v[184:187], v[12:15], v[96:99]
v_mfma_f32_16x16x32_bf16 v[100:103], v[188:191], v[12:15], v[100:103]
ds_read_b128 v[12:15], v24 offset:7168
v_mfma_f32_16x16x32_bf16 v[108:111], v[188:191], v[16:19], v[108:111]
v_mfma_f32_16x16x32_bf16 v[104:107], v[184:187], v[16:19], v[104:107]
s_waitcnt vmcnt(4)
s_barrier
ds_read_b128 v[16:19], v24 offset:8192
s_add_u32 m0, s58, 24576
s_waitcnt lgkmcnt(3)
v_mfma_f32_16x16x32_bf16 v[112:115], v[184:187], v[20:23], v[112:115]
s_add_u32 s59, s61, 0
buffer_load_dwordx4 v31, s[48:51], s59 offen sc1 lds
v_mfma_f32_16x16x32_bf16 v[116:119], v[188:191], v[20:23], v[116:119]
ds_read_b128 v[20:23], v24 offset:9216
ds_read_b64_tr_b16 v[208:209], v28 offset:32768
v_mfma_f32_16x16x32_bf16 v[124:127], v[188:191], v[4:7], v[124:127]
ds_read_b64_tr_b16 v[210:211], v28 offset:34816
buffer_load_dwordx4 v31, s[48:51], s59 offen offset:1024 sc1 lds
v_mfma_f32_16x16x32_bf16 v[120:123], v[184:187], v[4:7], v[120:123]
ds_read_b128 v[4:7], v24 offset:10240
s_waitcnt lgkmcnt(5)
v_mfma_f32_16x16x32_bf16 v[240:243], v[184:187], v[8:11], v[240:243]
v_mfma_f32_16x16x32_bf16 v[244:247], v[188:191], v[8:11], v[244:247]
ds_read_b128 v[8:11], v24 offset:11264
ds_read_b64_tr_b16 v[212:213], v28 offset:32776
v_mfma_f32_16x16x32_bf16 v[252:255], v[188:191], v[12:15], v[252:255]
ds_read_b64_tr_b16 v[214:215], v28 offset:34824
buffer_load_dwordx4 v31, s[48:51], s59 offen offset:2048 sc1 lds
v_mfma_f32_16x16x32_bf16 v[248:251], v[184:187], v[12:15], v[248:251]
ds_read_b128 v[12:15], v24 offset:12288
s_waitcnt lgkmcnt(7)
v_mfma_f32_16x16x32_bf16 v[80:83], v[192:195], v[16:19], v[80:83]
buffer_load_dwordx4 v31, s[48:51], s59 offen offset:3072 sc1 lds
v_mfma_f32_16x16x32_bf16 v[84:87], v[196:199], v[16:19], v[84:87]
ds_read_b128 v[16:19], v24 offset:13312
v_mfma_f32_16x16x32_bf16 v[92:95], v[196:199], v[20:23], v[92:95]
v_mfma_f32_16x16x32_bf16 v[88:91], v[192:195], v[20:23], v[88:91]
ds_read_b128 v[20:23], v24 offset:14336
s_waitcnt lgkmcnt(5)
v_mfma_f32_16x16x32_bf16 v[96:99], v[192:195], v[4:7], v[96:99]
v_mfma_f32_16x16x32_bf16 v[100:103], v[196:199], v[4:7], v[100:103]
ds_read_b128 v[4:7], v24 offset:15360
v_mfma_f32_16x16x32_bf16 v[108:111], v[196:199], v[8:11], v[108:111]
v_mfma_f32_16x16x32_bf16 v[104:107], v[192:195], v[8:11], v[104:107]
s_waitcnt lgkmcnt(4)
s_waitcnt vmcnt(4)
s_barrier
ds_read_b128 v[8:11], v24 offset:16384
s_add_u32 m0, s58, 32768
s_waitcnt lgkmcnt(3)
v_mfma_f32_16x16x32_bf16 v[112:115], v[192:195], v[12:15], v[112:115]
s_add_u32 s59, s61, 8192
buffer_load_dwordx4 v31, s[48:51], s59 offen sc1 lds
v_mfma_f32_16x16x32_bf16 v[116:119], v[196:199], v[12:15], v[116:119]
ds_read_b128 v[12:15], v24 offset:17408
v_mfma_f32_16x16x32_bf16 v[124:127], v[196:199], v[16:19], v[124:127]
buffer_load_dwordx4 v31, s[48:51], s59 offen offset:1024 sc1 lds
v_mfma_f32_16x16x32_bf16 v[120:123], v[192:195], v[16:19], v[120:123]
ds_read_b128 v[16:19], v24 offset:18432
s_waitcnt lgkmcnt(3)
v_mfma_f32_16x16x32_bf16 v[240:243], v[192:195], v[20:23], v[240:243]
)ASM"
R"ASM(
v_mfma_f32_16x16x32_bf16 v[244:247], v[196:199], v[20:23], v[244:247]
ds_read_b128 v[20:23], v24 offset:19456
v_mfma_f32_16x16x32_bf16 v[252:255], v[196:199], v[4:7], v[252:255]
buffer_load_dwordx4 v31, s[48:51], s59 offen offset:2048 sc1 lds
v_mfma_f32_16x16x32_bf16 v[248:251], v[192:195], v[4:7], v[248:251]
ds_read_b128 v[4:7], v24 offset:20480
s_waitcnt lgkmcnt(3)
v_mfma_f32_16x16x32_bf16 v[80:83], v[200:203], v[8:11], v[80:83]
buffer_load_dwordx4 v31, s[48:51], s59 offen offset:3072 sc1 lds
v_mfma_f32_16x16x32_bf16 v[84:87], v[204:207], v[8:11], v[84:87]
ds_read_b128 v[8:11], v24 offset:21504
v_mfma_f32_16x16x32_bf16 v[92:95], v[204:207], v[12:15], v[92:95]
v_mfma_f32_16x16x32_bf16 v[88:91], v[200:203], v[12:15], v[88:91]
ds_read_b128 v[12:15], v24 offset:22528
s_waitcnt lgkmcnt(3)
v_mfma_f32_16x16x32_bf16 v[96:99], v[200:203], v[16:19], v[96:99]
v_mfma_f32_16x16x32_bf16 v[100:103], v[204:207], v[16:19], v[100:103]
ds_read_b128 v[16:19], v24 offset:23552
v_mfma_f32_16x16x32_bf16 v[108:111], v[204:207], v[20:23], v[108:111]
v_mfma_f32_16x16x32_bf16 v[104:107], v[200:203], v[20:23], v[104:107]
s_waitcnt vmcnt(4)
s_barrier
ds_read_b128 v[20:23], v24 offset:24576
s_add_u32 m0, s58, 0
s_waitcnt lgkmcnt(3)
v_mfma_f32_16x16x32_bf16 v[112:115], v[200:203], v[4:7], v[112:115]
s_add_u32 s59, s60, 40960
buffer_load_dwordx4 v31, s[48:51], s59 offen sc1 lds
v_mfma_f32_16x16x32_bf16 v[116:119], v[204:207], v[4:7], v[116:119]
ds_read_b128 v[4:7], v24 offset:25600
ds_read_b64_tr_b16 v[216:217], v28 offset:0
v_mfma_f32_16x16x32_bf16 v[124:127], v[204:207], v[8:11], v[124:127]
ds_read_b64_tr_b16 v[218:219], v28 offset:2048
buffer_load_dwordx4 v31, s[48:51], s59 offen offset:1024 sc1 lds
v_mfma_f32_16x16x32_bf16 v[120:123], v[200:203], v[8:11], v[120:123]
ds_read_b128 v[8:11], v24 offset:26624
s_waitcnt lgkmcnt(5)
v_mfma_f32_16x16x32_bf16 v[240:243], v[200:203], v[12:15], v[240:243]
v_mfma_f32_16x16x32_bf16 v[244:247], v[204:207], v[12:15], v[244:247]
ds_read_b128 v[12:15], v24 offset:27648
ds_read_b64_tr_b16 v[220:221], v28 offset:8
v_mfma_f32_16x16x32_bf16 v[252:255], v[204:207], v[16:19], v[252:255]
ds_read_b64_tr_b16 v[222:223], v28 offset:2056
buffer_load_dwordx4 v31, s[48:51], s59 offen offset:2048 sc1 lds
v_mfma_f32_16x16x32_bf16 v[248:251], v[200:203], v[16:19], v[248:251]
ds_read_b128 v[16:19], v24 offset:28672
s_waitcnt lgkmcnt(7)
v_mfma_f32_16x16x32_bf16 v[48:51], v[128:131], v[20:23], 0
buffer_load_dwordx4 v31, s[48:51], s59 offen offset:3072 sc1 lds
v_mfma_f32_16x16x32_bf16 v[52:55], v[132:135], v[20:23], 0
ds_read_b128 v[20:23], v24 offset:29696
v_mfma_f32_16x16x32_bf16 v[60:63], v[132:135], v[4:7], 0
v_mfma_f32_16x16x32_bf16 v[56:59], v[128:131], v[4:7], 0
ds_read_b128 v[4:7], v24 offset:30720
s_waitcnt lgkmcnt(5)
v_mfma_f32_16x16x32_bf16 v[64:67], v[128:131], v[8:11], 0
v_mfma_f32_16x16x32_bf16 v[68:71], v[132:135], v[8:11], 0
ds_read_b128 v[8:11], v24 offset:31744
v_mfma_f32_16x16x32_bf16 v[76:79], v[132:135], v[12:15], 0
v_mfma_f32_16x16x32_bf16 v[72:75], v[128:131], v[12:15], 0
s_waitcnt lgkmcnt(4)
s_waitcnt vmcnt(4)
s_barrier
ds_read_b128 v[12:15], v24 offset:32768
s_add_u32 m0, s58, 8192
s_waitcnt lgkmcnt(3)
v_mfma_f32_16x16x32_bf16 v[48:51], v[136:139], v[16:19], v[48:51]
s_add_u32 s59, s60, 98304
buffer_load_dwordx4 v31, s[48:51], s59 offen sc1 lds
v_mfma_f32_16x16x32_bf16 v[52:55], v[140:143], v[16:19], v[52:55]
ds_read_b128 v[16:19], v24 offset:33792
ds_read_b64_tr_b16 v[224:225], v28 offset:16384
v_mfma_f32_16x16x32_bf16 v[60:63], v[140:143], v[20:23], v[60:63]
ds_read_b64_tr_b16 v[226:227], v28 offset:18432
buffer_load_dwordx4 v31, s[48:51], s59 offen offset:1024 sc1 lds
v_mfma_f32_16x16x32_bf16 v[56:59], v[136:139], v[20:23], v[56:59]
ds_read_b128 v[20:23], v24 offset:34816
s_waitcnt lgkmcnt(5)
v_mfma_f32_16x16x32_bf16 v[64:67], v[136:139], v[4:7], v[64:67]
v_mfma_f32_16x16x32_bf16 v[68:71], v[140:143], v[4:7], v[68:71]
ds_read_b128 v[4:7], v24 offset:35840
ds_read_b64_tr_b16 v[228:229], v28 offset:16392
v_mfma_f32_16x16x32_bf16 v[76:79], v[140:143], v[8:11], v[76:79]
ds_read_b64_tr_b16 v[230:231], v28 offset:18440
buffer_load_dwordx4 v31, s[48:51], s59 offen offset:2048 sc1 lds
v_mfma_f32_16x16x32_bf16 v[72:75], v[136:139], v[8:11], v[72:75]
ds_read_b128 v[8:11], v24 offset:36864
s_waitcnt lgkmcnt(7)
v_mfma_f32_16x16x32_bf16 v[48:51], v[144:147], v[12:15], v[48:51]
buffer_load_dwordx4 v31, s[48:51], s59 offen offset:3072 sc1 lds
v_mfma_f32_16x16x32_bf16 v[52:55], v[148:151], v[12:15], v[52:55]
ds_read_b128 v[12:15], v24 offset:37888
v_mfma_f32_16x16x32_bf16 v[60:63], v[148:151], v[16:19], v[60:63]
v_mfma_f32_16x16x32_bf16 v[56:59], v[144:147], v[16:19], v[56:59]
ds_read_b128 v[16:19], v24 offset:38912
s_waitcnt lgkmcnt(5)
v_mfma_f32_16x16x32_bf16 v[64:67], v[144:147], v[20:23], v[64:67]
v_mfma_f32_16x16x32_bf16 v[68:71], v[148:151], v[20:23], v[68:71]
ds_read_b128 v[20:23], v24 offset:39936
v_mfma_f32_16x16x32_bf16 v[76:79], v[148:151], v[4:7], v[76:79]
v_mfma_f32_16x16x32_bf16 v[72:75], v[144:147], v[4:7], v[72:75]
s_waitcnt lgkmcnt(4)
s_waitcnt vmcnt(4)
s_barrier
ds_read_b128 v[4:7], v24 offset:0
s_add_u32 m0, s58, 16384
s_waitcnt lgkmcnt(3)
v_mfma_f32_16x16x32_bf16 v[48:51], v[152:155], v[8:11], v[48:51]
s_add_u32 s59, s60, 49152
buffer_load_dwordx4 v31, s[48:51], s59 offen sc1 lds
v_mfma_f32_16x16x32_bf16 v[52:55], v[156:159], v[8:11], v[52:55]
ds_read_b128 v[8:11], v24 offset:1024
v_mfma_f32_16x16x32_bf16 v[60:63], v[156:159], v[12:15], v[60:63]
buffer_load_dwordx4 v31, s[48:51], s59 offen offset:1024 sc1 lds
v_mfma_f32_16x16x32_bf16 v[56:59], v[152:155], v[12:15], v[56:59]
ds_read_b128 v[12:15], v24 offset:2048
s_waitcnt lgkmcnt(3)
v_mfma_f32_16x16x32_bf16 v[64:67], v[152:155], v[16:19], v[64:67]
v_mfma_f32_16x16x32_bf16 v[68:71], v[156:159], v[16:19], v[68:71]
ds_read_b128 v[16:19], v24 offset:3072
v_mfma_f32_16x16x32_bf16 v[76:79], v[156:159], v[20:23], v[76:79]
buffer_load_dwordx4 v31, s[48:51], s59 offen offset:2048 sc1 lds
v_mfma_f32_16x16x32_bf16 v[72:75], v[152:155], v[20:23], v[72:75]
ds_read_b128 v[20:23], v24 offset:4096
s_waitcnt lgkmcnt(3)
v_mfma_f32_16x16x32_bf16 v[80:83], v[208:211], v[4:7], v[80:83]
buffer_load_dwordx4 v31, s[48:51], s59 offen offset:3072 sc1 lds
v_mfma_f32_16x16x32_bf16 v[84:87], v[212:215], v[4:7], v[84:87]
ds_read_b128 v[4:7], v24 offset:5120
v_mfma_f32_16x16x32_bf16 v[92:95], v[212:215], v[8:11], v[92:95]
v_mfma_f32_16x16x32_bf16 v[88:91], v[208:211], v[8:11], v[88:91]
ds_read_b128 v[8:11], v24 offset:6144
s_waitcnt lgkmcnt(3)
v_mfma_f32_16x16x32_bf16 v[96:99], v[208:211], v[12:15], v[96:99]
v_mfma_f32_16x16x32_bf16 v[100:103], v[212:215], v[12:15], v[100:103]
ds_read_b128 v[12:15], v24 offset:7168
v_mfma_f32_16x16x32_bf16 v[108:111], v[212:215], v[16:19], v[108:111]
v_mfma_f32_16x16x32_bf16 v[104:107], v[208:211], v[16:19], v[104:107]
s_waitcnt vmcnt(4)
s_barrier
ds_read_b128 v[16:19], v24 offset:8192
s_add_u32 m0, s58, 24576
s_waitcnt lgkmcnt(3)
v_mfma_f32_16x16x32_bf16 v[112:115], v[208:211], v[20:23], v[112:115]
s_add_u32 s59, s60, 106496
buffer_load_dwordx4 v31, s[48:51], s59 offen sc1 lds
v_mfma_f32_16x16x32_bf16 v[116:119], v[212:215], v[20:23], v[116:119]
ds_read_b128 v[20:23], v24 offset:9216
ds_read_b64_tr_b16 v[232:233], v28 offset:32768
v_mfma_f32_16x16x32_bf16 v[124:127], v[212:215], v[4:7], v[124:127]
ds_read_b64_tr_b16 v[234:235], v28 offset:34816
buffer_load_dwordx4 v31, s[48:51], s59 offen offset:1024 sc1 lds
v_mfma_f32_16x16x32_bf16 v[120:123], v[208:211], v[4:7], v[120:123]
ds_read_b128 v[4:7], v24 offset:10240
s_waitcnt lgkmcnt(5)
v_mfma_f32_16x16x32_bf16 v[240:243], v[208:211], v[8:11], v[240:243]
v_mfma_f32_16x16x32_bf16 v[244:247], v[212:215], v[8:11], v[244:247]
ds_read_b128 v[8:11], v24 offset:11264
ds_read_b64_tr_b16 v[236:237], v28 offset:32776
v_mfma_f32_16x16x32_bf16 v[252:255], v[212:215], v[12:15], v[252:255]
ds_read_b64_tr_b16 v[238:239], v28 offset:34824
buffer_load_dwordx4 v31, s[48:51], s59 offen offset:2048 sc1 lds
v_mfma_f32_16x16x32_bf16 v[248:251], v[208:211], v[12:15], v[248:251]
ds_read_b128 v[12:15], v24 offset:12288
s_waitcnt lgkmcnt(7)
v_mfma_f32_16x16x32_bf16 v[80:83], v[216:219], v[16:19], v[80:83]
buffer_load_dwordx4 v31, s[48:51], s59 offen offset:3072 sc1 lds
v_mfma_f32_16x16x32_bf16 v[84:87], v[220:223], v[16:19], v[84:87]
ds_read_b128 v[16:19], v24 offset:13312
v_mfma_f32_16x16x32_bf16 v[92:95], v[220:223], v[20:23], v[92:95]
v_mfma_f32_16x16x32_bf16 v[88:91], v[216:219], v[20:23], v[88:91]
ds_read_b128 v[20:23], v24 offset:14336
s_waitcnt lgkmcnt(5)
v_mfma_f32_16x16x32_bf16 v[96:99], v[216:219], v[4:7], v[96:99]
v_mfma_f32_16x16x32_bf16 v[100:103], v[220:223], v[4:7], v[100:103]
ds_read_b128 v[4:7], v24 offset:15360
v_mfma_f32_16x16x32_bf16 v[108:111], v[220:223], v[8:11], v[108:111]
v_mfma_f32_16x16x32_bf16 v[104:107], v[216:219], v[8:11], v[104:107]
s_waitcnt lgkmcnt(4)
s_waitcnt vmcnt(4)
s_barrier
ds_read_b128 v[8:11], v24 offset:16384
s_add_u32 m0, s58, 32768
s_waitcnt lgkmcnt(3)
v_mfma_f32_16x16x32_bf16 v[112:115], v[216:219], v[12:15], v[112:115]
s_add_u32 s59, s61, 16384
buffer_load_dwordx4 v31, s[48:51], s59 offen sc1 lds
v_mfma_f32_16x16x32_bf16 v[116:119], v[220:223], v[12:15], v[116:119]
ds_read_b128 v[12:15], v24 offset:17408
v_mfma_f32_16x16x32_bf16 v[124:127], v[220:223], v[16:19], v[124:127]
buffer_load_dwordx4 v31, s[48:51], s59 offen offset:1024 sc1 lds
v_mfma_f32_16x16x32_bf16 v[120:123], v[216:219], v[16:19], v[120:123]
ds_read_b128 v[16:19], v24 offset:18432
s_waitcnt lgkmcnt(3)
v_mfma_f32_16x16x32_bf16 v[240:243], v[216:219], v[20:23], v[240:243]
v_mfma_f32_16x16x32_bf16 v[244:247], v[220:223], v[20:23], v[244:247]
ds_read_b128 v[20:23], v24 offset:19456
v_mfma_f32_16x16x32_bf16 v[252:255], v[220:223], v[4:7], v[252:255]
buffer_load_dwordx4 v31, s[48:51], s59 offen offset:2048 sc1 lds
v_mfma_f32_16x16x32_bf16 v[248:251], v[216:219], v[4:7], v[248:251]
)ASM"
R"ASM(
ds_read_b128 v[4:7], v24 offset:20480
s_waitcnt lgkmcnt(3)
v_mfma_f32_16x16x32_bf16 v[80:83], v[224:227], v[8:11], v[80:83]
buffer_load_dwordx4 v31, s[48:51], s59 offen offset:3072 sc1 lds
v_mfma_f32_16x16x32_bf16 v[84:87], v[228:231], v[8:11], v[84:87]
ds_read_b128 v[8:11], v24 offset:21504
v_mfma_f32_16x16x32_bf16 v[92:95], v[228:231], v[12:15], v[92:95]
v_mfma_f32_16x16x32_bf16 v[88:91], v[224:227], v[12:15], v[88:91]
ds_read_b128 v[12:15], v24 offset:22528
s_waitcnt lgkmcnt(3)
v_mfma_f32_16x16x32_bf16 v[96:99], v[224:227], v[16:19], v[96:99]
v_mfma_f32_16x16x32_bf16 v[100:103], v[228:231], v[16:19], v[100:103]
ds_read_b128 v[16:19], v24 offset:23552
v_mfma_f32_16x16x32_bf16 v[108:111], v[228:231], v[20:23], v[108:111]
v_mfma_f32_16x16x32_bf16 v[104:107], v[224:227], v[20:23], v[104:107]
s_waitcnt vmcnt(4)
s_barrier
ds_read_b128 v[20:23], v24 offset:24576
s_add_u32 m0, s58, 0
s_waitcnt lgkmcnt(3)
v_mfma_f32_16x16x32_bf16 v[112:115], v[224:227], v[4:7], v[112:115]
s_add_u32 s59, s61, 24576
buffer_load_dwordx4 v31, s[48:51], s59 offen sc1 lds
v_mfma_f32_16x16x32_bf16 v[116:119], v[228:231], v[4:7], v[116:119]
ds_read_b128 v[4:7], v24 offset:25600
v_mfma_f32_16x16x32_bf16 v[124:127], v[228:231], v[8:11], v[124:127]
buffer_load_dwordx4 v31, s[48:51], s59 offen offset:1024 sc1 lds
v_mfma_f32_16x16x32_bf16 v[120:123], v[224:227], v[8:11], v[120:123]
ds_read_b128 v[8:11], v24 offset:26624
s_waitcnt lgkmcnt(3)
v_mfma_f32_16x16x32_bf16 v[240:243], v[224:227], v[12:15], v[240:243]
v_mfma_f32_16x16x32_bf16 v[244:247], v[228:231], v[12:15], v[244:247]
ds_read_b128 v[12:15], v24 offset:27648
v_mfma_f32_16x16x32_bf16 v[252:255], v[228:231], v[16:19], v[252:255]
buffer_load_dwordx4 v31, s[48:51], s59 offen offset:2048 sc1 lds
v_mfma_f32_16x16x32_bf16 v[248:251], v[224:227], v[16:19], v[248:251]
ds_read_b128 v[16:19], v24 offset:28672
s_waitcnt lgkmcnt(3)
v_mfma_f32_16x16x32_bf16 v[80:83], v[232:235], v[20:23], v[80:83]
buffer_load_dwordx4 v31, s[48:51], s59 offen offset:3072 sc1 lds
v_mfma_f32_16x16x32_bf16 v[84:87], v[236:239], v[20:23], v[84:87]
ds_read_b128 v[20:23], v24 offset:29696
v_mfma_f32_16x16x32_bf16 v[92:95], v[236:239], v[4:7], v[92:95]
v_mfma_f32_16x16x32_bf16 v[88:91], v[232:235], v[4:7], v[88:91]
ds_read_b128 v[4:7], v24 offset:30720
s_waitcnt lgkmcnt(3)
v_mfma_f32_16x16x32_bf16 v[96:99], v[232:235], v[8:11], v[96:99]
v_mfma_f32_16x16x32_bf16 v[100:103], v[236:239], v[8:11], v[100:103]
ds_read_b128 v[8:11], v24 offset:31744
s_nop 2
v_cvt_pk_bf16_f32 v88, v88, v89
v_cvt_pk_bf16_f32 v89, v90, v91
v_mfma_f32_16x16x32_bf16 v[108:111], v[236:239], v[12:15], v[108:111]
v_cvt_pk_bf16_f32 v90, v92, v93
v_cvt_pk_bf16_f32 v91, v94, v95
ds_write_b128 v30, v[88:91] offset:34816
v_mfma_f32_16x16x32_bf16 v[104:107], v[232:235], v[12:15], v[104:107]
s_waitcnt vmcnt(4)
s_waitcnt lgkmcnt(0)
s_barrier
ds_read_b128 v[12:15], v24 offset:32768
ds_read_b128 v[92:95], v30 offset:32768
s_add_u32 m0, s58, 8192
v_mfma_f32_16x16x32_bf16 v[112:115], v[232:235], v[16:19], v[112:115]
s_add_u32 s59, s61, 32768
buffer_load_dwordx4 v31, s[48:51], s59 offen sc1 lds
v_mfma_f32_16x16x32_bf16 v[116:119], v[236:239], v[16:19], v[116:119]
ds_read_b128 v[16:19], v24 offset:33792
v_cvt_pk_bf16_f32 v104, v104, v105
v_cvt_pk_bf16_f32 v105, v106, v107
v_cvt_pk_bf16_f32 v106, v108, v109
v_mfma_f32_16x16x32_bf16 v[124:127], v[236:239], v[20:23], v[124:127]
v_cvt_pk_bf16_f32 v107, v110, v111
ds_write_b128 v30, v[104:107] offset:51200
buffer_load_dwordx4 v31, s[48:51], s59 offen offset:1024 sc1 lds
v_mfma_f32_16x16x32_bf16 v[120:123], v[232:235], v[20:23], v[120:123]
ds_read_b128 v[20:23], v24 offset:34816
v_mfma_f32_16x16x32_bf16 v[240:243], v[232:235], v[4:7], v[240:243]
v_mfma_f32_16x16x32_bf16 v[244:247], v[236:239], v[4:7], v[244:247]
ds_read_b128 v[4:7], v24 offset:35840
s_nop 3
v_cvt_pk_bf16_f32 v120, v120, v121
v_cvt_pk_bf16_f32 v121, v122, v123
v_cvt_pk_bf16_f32 v122, v124, v125
v_mfma_f32_16x16x32_bf16 v[252:255], v[236:239], v[8:11], v[252:255]
v_cvt_pk_bf16_f32 v123, v126, v127
ds_write_b128 v30, v[120:123] offset:2048
buffer_load_dwordx4 v31, s[48:51], s59 offen offset:2048 sc1 lds
v_mfma_f32_16x16x32_bf16 v[248:251], v[232:235], v[8:11], v[248:251]
ds_read_b128 v[8:11], v24 offset:36864
s_waitcnt lgkmcnt(6)
v_lshlrev_b32 v88, 16, v92
v_and_b32 v92, 0xffff0000, v92
v_lshlrev_b32 v89, 16, v93
s_waitcnt lgkmcnt(5)
v_mfma_f32_16x16x32_bf16 v[48:51], v[160:163], v[12:15], v[48:51]
v_and_b32 v93, 0xffff0000, v93
v_lshlrev_b32 v90, 16, v94
buffer_load_dwordx4 v31, s[48:51], s59 offen offset:3072 sc1 lds
v_mfma_f32_16x16x32_bf16 v[52:55], v[164:167], v[12:15], v[52:55]
ds_read_b128 v[12:15], v24 offset:37888
v_and_b32 v94, 0xffff0000, v94
v_lshlrev_b32 v91, 16, v95
v_and_b32 v95, 0xffff0000, v95
v_add_f32 v80, v80, v88
v_add_f32 v81, v81, v92
v_mfma_f32_16x16x32_bf16 v[60:63], v[164:167], v[16:19], v[60:63]
v_cvt_pk_bf16_f32 v248, v248, v249
v_cvt_pk_bf16_f32 v249, v250, v251
v_cvt_pk_bf16_f32 v250, v252, v253
v_cvt_pk_bf16_f32 v251, v254, v255
ds_write_b128 v30, v[248:251] offset:18432
v_mfma_f32_16x16x32_bf16 v[56:59], v[160:163], v[16:19], v[56:59]
ds_read_b128 v[16:19], v24 offset:38912
v_add_f32 v82, v82, v89
v_add_f32 v83, v83, v93
s_waitcnt lgkmcnt(5)
v_mfma_f32_16x16x32_bf16 v[64:67], v[160:163], v[20:23], v[64:67]
v_add_f32 v84, v84, v90
v_add_f32 v85, v85, v94
v_add_f32 v86, v86, v91
v_mfma_f32_16x16x32_bf16 v[68:71], v[164:167], v[20:23], v[68:71]
ds_read_b128 v[20:23], v24 offset:39936
v_add_f32 v87, v87, v95
v_cvt_pk_bf16_f32 v80, v80, v81
v_cvt_pk_bf16_f32 v81, v82, v83
v_mfma_f32_16x16x32_bf16 v[76:79], v[164:167], v[4:7], v[76:79]
v_cvt_pk_bf16_f32 v82, v84, v85
v_cvt_pk_bf16_f32 v83, v86, v87
buffer_store_dwordx4 v[80:83], v31, s[48:51], s85 offen offset:0 sc1
v_mfma_f32_16x16x32_bf16 v[72:75], v[160:163], v[4:7], v[72:75]
s_waitcnt vmcnt(5)
s_waitcnt lgkmcnt(5)
s_waitcnt lgkmcnt(2)
s_barrier
ds_read_b128 v[4:7], v24 offset:0
ds_read_b128 v[108:111], v30 offset:49152
ds_read_b128 v[124:127], v30 offset:0
ds_read_b128 v[252:255], v30 offset:16384
v_mfma_f32_16x16x32_bf16 v[48:51], v[168:171], v[8:11], v[48:51]
s_add_u32 m0, s58, 16384
s_add_u32 s59, s61, 40960
buffer_load_dwordx4 v31, s[48:51], s59 offen sc1 lds
v_mfma_f32_16x16x32_bf16 v[52:55], v[172:175], v[8:11], v[52:55]
ds_read_b128 v[8:11], v24 offset:1024
v_mfma_f32_16x16x32_bf16 v[60:63], v[172:175], v[12:15], v[60:63]
buffer_load_dwordx4 v31, s[48:51], s59 offen offset:1024 sc1 lds
v_mfma_f32_16x16x32_bf16 v[56:59], v[168:171], v[12:15], v[56:59]
ds_read_b128 v[12:15], v24 offset:2048
s_waitcnt lgkmcnt(6)
v_mfma_f32_16x16x32_bf16 v[64:67], v[168:171], v[16:19], v[64:67]
v_mfma_f32_16x16x32_bf16 v[68:71], v[172:175], v[16:19], v[68:71]
ds_read_b128 v[16:19], v24 offset:3072
v_mfma_f32_16x16x32_bf16 v[76:79], v[172:175], v[20:23], v[76:79]
buffer_load_dwordx4 v31, s[48:51], s59 offen offset:2048 sc1 lds
v_mfma_f32_16x16x32_bf16 v[72:75], v[168:171], v[20:23], v[72:75]
ds_read_b128 v[20:23], v24 offset:4096
s_waitcnt lgkmcnt(6)
v_lshlrev_b32 v104, 16, v108
v_and_b32 v108, 0xffff0000, v108
v_lshlrev_b32 v105, 16, v109
s_waitcnt lgkmcnt(3)
v_mfma_f32_16x16x32_bf16 v[48:51], v[176:179], v[4:7], v[48:51]
v_and_b32 v109, 0xffff0000, v109
v_lshlrev_b32 v106, 16, v110
buffer_load_dwordx4 v31, s[48:51], s59 offen offset:3072 sc1 lds
v_mfma_f32_16x16x32_bf16 v[52:55], v[180:183], v[4:7], v[52:55]
ds_read_b128 v[4:7], v24 offset:5120
v_and_b32 v110, 0xffff0000, v110
v_lshlrev_b32 v107, 16, v111
v_mfma_f32_16x16x32_bf16 v[60:63], v[180:183], v[8:11], v[60:63]
v_and_b32 v111, 0xffff0000, v111
v_add_f32 v96, v96, v104
v_add_f32 v97, v97, v108
v_mfma_f32_16x16x32_bf16 v[56:59], v[176:179], v[8:11], v[56:59]
ds_read_b128 v[8:11], v24 offset:6144
v_add_f32 v98, v98, v105
v_add_f32 v99, v99, v109
v_add_f32 v100, v100, v106
v_add_f32 v101, v101, v110
v_add_f32 v102, v102, v107
s_waitcnt lgkmcnt(3)
v_mfma_f32_16x16x32_bf16 v[64:67], v[176:179], v[12:15], v[64:67]
v_lshlrev_b32 v120, 16, v124
v_and_b32 v124, 0xffff0000, v124
v_lshlrev_b32 v121, 16, v125
v_and_b32 v125, 0xffff0000, v125
v_lshlrev_b32 v122, 16, v126
v_mfma_f32_16x16x32_bf16 v[68:71], v[180:183], v[12:15], v[68:71]
ds_read_b128 v[12:15], v24 offset:7168
v_add_f32 v103, v103, v111
v_cvt_pk_bf16_f32 v96, v96, v97
v_cvt_pk_bf16_f32 v97, v98, v99
v_cvt_pk_bf16_f32 v98, v100, v101
v_cvt_pk_bf16_f32 v99, v102, v103
v_mfma_f32_16x16x32_bf16 v[76:79], v[180:183], v[16:19], v[76:79]
buffer_store_dwordx4 v[96:99], v31, s[48:51], s85 offen offset:1024 sc1
v_and_b32 v126, 0xffff0000, v126
v_lshlrev_b32 v123, 16, v127
v_and_b32 v127, 0xffff0000, v127
)ASM"
R"ASM(
v_add_f32 v112, v112, v120
v_add_f32 v113, v113, v124
v_mfma_f32_16x16x32_bf16 v[72:75], v[176:179], v[16:19], v[72:75]
s_waitcnt vmcnt(6)
s_barrier
ds_read_b128 v[16:19], v24 offset:8192
v_add_f32 v114, v114, v121
v_add_f32 v115, v115, v125
v_add_f32 v116, v116, v122
v_add_f32 v117, v117, v126
v_add_f32 v118, v118, v123
v_lshlrev_b32 v248, 16, v252
s_waitcnt lgkmcnt(3)
v_mfma_f32_16x16x32_bf16 v[48:51], v[184:187], v[20:23], v[48:51]
v_and_b32 v252, 0xffff0000, v252
v_lshlrev_b32 v249, 16, v253
v_and_b32 v253, 0xffff0000, v253
v_lshlrev_b32 v250, 16, v254
s_add_u32 m0, s58, 24576
s_add_u32 s59, s61, 49152
buffer_load_dwordx4 v31, s[48:51], s59 offen sc1 lds
v_mfma_f32_16x16x32_bf16 v[52:55], v[188:191], v[20:23], v[52:55]
ds_read_b128 v[20:23], v24 offset:9216
v_add_f32 v119, v119, v127
v_cvt_pk_bf16_f32 v112, v112, v113
v_cvt_pk_bf16_f32 v113, v114, v115
v_cvt_pk_bf16_f32 v114, v116, v117
v_cvt_pk_bf16_f32 v115, v118, v119
v_mfma_f32_16x16x32_bf16 v[60:63], v[188:191], v[4:7], v[60:63]
v_and_b32 v254, 0xffff0000, v254
v_lshlrev_b32 v251, 16, v255
v_and_b32 v255, 0xffff0000, v255
v_add_f32 v240, v240, v248
v_add_f32 v241, v241, v252
buffer_load_dwordx4 v31, s[48:51], s59 offen offset:1024 sc1 lds
v_mfma_f32_16x16x32_bf16 v[56:59], v[184:187], v[4:7], v[56:59]
ds_read_b128 v[4:7], v24 offset:10240
v_add_f32 v242, v242, v249
v_add_f32 v243, v243, v253
s_waitcnt lgkmcnt(3)
v_mfma_f32_16x16x32_bf16 v[64:67], v[184:187], v[8:11], v[64:67]
v_add_f32 v244, v244, v250
v_add_f32 v245, v245, v254
v_add_f32 v246, v246, v251
v_mfma_f32_16x16x32_bf16 v[68:71], v[188:191], v[8:11], v[68:71]
ds_read_b128 v[8:11], v24 offset:11264
v_add_f32 v247, v247, v255
v_cvt_pk_bf16_f32 v240, v240, v241
v_cvt_pk_bf16_f32 v241, v242, v243
v_mfma_f32_16x16x32_bf16 v[76:79], v[188:191], v[12:15], v[76:79]
v_cvt_pk_bf16_f32 v242, v244, v245
v_cvt_pk_bf16_f32 v243, v246, v247
buffer_load_dwordx4 v31, s[48:51], s59 offen offset:2048 sc1 lds
v_mfma_f32_16x16x32_bf16 v[72:75], v[184:187], v[12:15], v[72:75]
ds_read_b128 v[12:15], v24 offset:12288
s_waitcnt lgkmcnt(3)
v_mfma_f32_16x16x32_bf16 v[48:51], v[192:195], v[16:19], v[48:51]
buffer_load_dwordx4 v31, s[48:51], s59 offen offset:3072 sc1 lds
v_mfma_f32_16x16x32_bf16 v[52:55], v[196:199], v[16:19], v[52:55]
ds_read_b128 v[16:19], v24 offset:13312
v_mfma_f32_16x16x32_bf16 v[60:63], v[196:199], v[20:23], v[60:63]
v_mfma_f32_16x16x32_bf16 v[56:59], v[192:195], v[20:23], v[56:59]
ds_read_b128 v[20:23], v24 offset:14336
buffer_store_dwordx4 v[112:115], v31, s[48:51], s86 offen offset:0 sc1
s_waitcnt lgkmcnt(3)
v_mfma_f32_16x16x32_bf16 v[64:67], v[192:195], v[4:7], v[64:67]
buffer_store_dwordx4 v[240:243], v31, s[48:51], s86 offen offset:1024 sc1
v_mfma_f32_16x16x32_bf16 v[68:71], v[196:199], v[4:7], v[68:71]
ds_read_b128 v[4:7], v24 offset:15360
v_lshlrev_b32 v112, 16, v36
v_mfma_f32_16x16x32_bf16 v[76:79], v[196:199], v[8:11], v[76:79]
v_and_b32 v113, 0xffff0000, v36
v_lshlrev_b32 v114, 16, v37
v_mfma_f32_16x16x32_bf16 v[72:75], v[192:195], v[8:11], v[72:75]
s_waitcnt vmcnt(7)
s_barrier
ds_read_b128 v[8:11], v24 offset:16384
s_add_u32 m0, s58, 32768
s_add_u32 s59, s62, 0
buffer_load_dwordx4 v31, s[48:51], s59 offen sc1 lds
s_waitcnt lgkmcnt(3)
v_mfma_f32_16x16x32_bf16 v[48:51], v[200:203], v[12:15], v[48:51]
v_and_b32 v115, 0xffff0000, v37
v_lshlrev_b32 v116, 16, v38
v_and_b32 v117, 0xffff0000, v38
v_mfma_f32_16x16x32_bf16 v[52:55], v[204:207], v[12:15], v[52:55]
ds_read_b128 v[12:15], v24 offset:17408
buffer_load_dwordx4 v31, s[48:51], s59 offen offset:1024 sc1 lds
v_mfma_f32_16x16x32_bf16 v[60:63], v[204:207], v[16:19], v[60:63]
v_lshlrev_b32 v118, 16, v39
v_and_b32 v119, 0xffff0000, v39
v_mfma_f32_16x16x32_bf16 v[56:59], v[200:203], v[16:19], v[56:59]
ds_read_b128 v[16:19], v24 offset:18432
v_mov_b32 v120, v112
s_waitcnt lgkmcnt(3)
v_mfma_f32_16x16x32_bf16 v[64:67], v[200:203], v[20:23], v[64:67]
v_mov_b32 v121, v113
v_mov_b32 v122, v114
v_mfma_f32_16x16x32_bf16 v[68:71], v[204:207], v[20:23], v[68:71]
ds_read_b128 v[20:23], v24 offset:19456
buffer_load_dwordx4 v31, s[48:51], s59 offen offset:2048 sc1 lds
v_mov_b32 v123, v115
v_mfma_f32_16x16x32_bf16 v[76:79], v[204:207], v[4:7], v[76:79]
v_mov_b32 v124, v116
v_mov_b32 v125, v117
v_mfma_f32_16x16x32_bf16 v[72:75], v[200:203], v[4:7], v[72:75]
ds_read_b128 v[4:7], v24 offset:20480
buffer_load_dwordx4 v31, s[48:51], s59 offen offset:3072 sc1 lds
s_waitcnt lgkmcnt(3)
v_mfma_f32_16x16x32_bf16 v[48:51], v[208:211], v[8:11], v[48:51]
v_mov_b32 v126, v118
v_mov_b32 v127, v119
v_mfma_f32_16x16x32_bf16 v[52:55], v[212:215], v[8:11], v[52:55]
ds_read_b128 v[8:11], v24 offset:21504
v_mfma_f32_16x16x32_bf16 v[60:63], v[212:215], v[12:15], v[60:63]
v_mfma_f32_16x16x32_bf16 v[56:59], v[208:211], v[12:15], v[56:59]
ds_read_b128 v[12:15], v24 offset:22528
s_waitcnt lgkmcnt(3)
v_mfma_f32_16x16x32_bf16 v[64:67], v[208:211], v[16:19], v[64:67]
v_mfma_f32_16x16x32_bf16 v[68:71], v[212:215], v[16:19], v[68:71]
ds_read_b128 v[16:19], v24 offset:23552
v_mfma_f32_16x16x32_bf16 v[76:79], v[212:215], v[20:23], v[76:79]
v_mfma_f32_16x16x32_bf16 v[72:75], v[208:211], v[20:23], v[72:75]
s_waitcnt vmcnt(6)
s_barrier
ds_read_b128 v[20:23], v24 offset:24576
s_add_u32 m0, s58, 0
s_waitcnt lgkmcnt(3)
v_mfma_f32_16x16x32_bf16 v[48:51], v[216:219], v[4:7], v[48:51]
s_add_u32 s59, s62, 8192
buffer_load_dwordx4 v31, s[48:51], s59 offen sc1 lds
v_mfma_f32_16x16x32_bf16 v[52:55], v[220:223], v[4:7], v[52:55]
ds_read_b128 v[4:7], v24 offset:25600
v_mfma_f32_16x16x32_bf16 v[60:63], v[220:223], v[8:11], v[60:63]
buffer_load_dwordx4 v31, s[48:51], s59 offen offset:1024 sc1 lds
v_mfma_f32_16x16x32_bf16 v[56:59], v[216:219], v[8:11], v[56:59]
ds_read_b128 v[8:11], v24 offset:26624
s_waitcnt lgkmcnt(3)
v_mfma_f32_16x16x32_bf16 v[64:67], v[216:219], v[12:15], v[64:67]
v_mfma_f32_16x16x32_bf16 v[68:71], v[220:223], v[12:15], v[68:71]
ds_read_b128 v[12:15], v24 offset:27648
v_mfma_f32_16x16x32_bf16 v[76:79], v[220:223], v[16:19], v[76:79]
buffer_load_dwordx4 v31, s[48:51], s59 offen offset:2048 sc1 lds
v_mfma_f32_16x16x32_bf16 v[72:75], v[216:219], v[16:19], v[72:75]
ds_read_b128 v[16:19], v24 offset:28672
s_waitcnt lgkmcnt(3)
v_mfma_f32_16x16x32_bf16 v[48:51], v[224:227], v[20:23], v[48:51]
buffer_load_dwordx4 v31, s[48:51], s59 offen offset:3072 sc1 lds
v_mfma_f32_16x16x32_bf16 v[52:55], v[228:231], v[20:23], v[52:55]
ds_read_b128 v[20:23], v24 offset:29696
v_mfma_f32_16x16x32_bf16 v[60:63], v[228:231], v[4:7], v[60:63]
v_mfma_f32_16x16x32_bf16 v[56:59], v[224:227], v[4:7], v[56:59]
ds_read_b128 v[4:7], v24 offset:30720
s_waitcnt lgkmcnt(3)
v_mfma_f32_16x16x32_bf16 v[64:67], v[224:227], v[8:11], v[64:67]
v_mfma_f32_16x16x32_bf16 v[68:71], v[228:231], v[8:11], v[68:71]
ds_read_b128 v[8:11], v24 offset:31744
v_mfma_f32_16x16x32_bf16 v[76:79], v[228:231], v[12:15], v[76:79]
v_mfma_f32_16x16x32_bf16 v[72:75], v[224:227], v[12:15], v[72:75]
s_waitcnt vmcnt(4)
s_barrier
ds_read_b128 v[12:15], v24 offset:32768
s_add_u32 m0, s58, 8192
s_waitcnt lgkmcnt(3)
v_mfma_f32_16x16x32_bf16 v[48:51], v[232:235], v[16:19], v[48:51]
s_add_u32 s59, s62, 16384
buffer_load_dwordx4 v31, s[48:51], s59 offen sc1 lds
v_mfma_f32_16x16x32_bf16 v[52:55], v[236:239], v[16:19], v[52:55]
ds_read_b128 v[16:19], v24 offset:33792
v_mfma_f32_16x16x32_bf16 v[60:63], v[236:239], v[20:23], v[60:63]
buffer_load_dwordx4 v31, s[48:51], s59 offen offset:1024 sc1 lds
v_mfma_f32_16x16x32_bf16 v[56:59], v[232:235], v[20:23], v[56:59]
ds_read_b128 v[20:23], v24 offset:34816
s_waitcnt lgkmcnt(3)
v_mfma_f32_16x16x32_bf16 v[64:67], v[232:235], v[4:7], v[64:67]
v_mfma_f32_16x16x32_bf16 v[68:71], v[236:239], v[4:7], v[68:71]
ds_read_b128 v[4:7], v24 offset:35840
s_nop 2
v_cvt_pk_bf16_f32 v56, v56, v57
v_cvt_pk_bf16_f32 v57, v58, v59
v_cvt_pk_bf16_f32 v58, v60, v61
v_mfma_f32_16x16x32_bf16 v[76:79], v[236:239], v[8:11], v[76:79]
v_cvt_pk_bf16_f32 v59, v62, v63
ds_write_b128 v30, v[56:59] offset:34816
buffer_load_dwordx4 v31, s[48:51], s59 offen offset:2048 sc1 lds
v_mfma_f32_16x16x32_bf16 v[72:75], v[232:235], v[8:11], v[72:75]
ds_read_b128 v[8:11], v24 offset:36864
s_waitcnt lgkmcnt(4)
v_mfma_f32_16x16x32_bf16 v[80:83], v[128:131], v[12:15], 0
buffer_load_dwordx4 v31, s[48:51], s59 offen offset:3072 sc1 lds
v_mfma_f32_16x16x32_bf16 v[84:87], v[132:135], v[12:15], 0
ds_read_b128 v[12:15], v24 offset:37888
s_nop 1
v_cvt_pk_bf16_f32 v72, v72, v73
v_cvt_pk_bf16_f32 v73, v74, v75
v_mfma_f32_16x16x32_bf16 v[92:95], v[132:135], v[16:19], 0
v_cvt_pk_bf16_f32 v74, v76, v77
v_cvt_pk_bf16_f32 v75, v78, v79
ds_write_b128 v30, v[72:75] offset:51200
v_mfma_f32_16x16x32_bf16 v[88:91], v[128:131], v[16:19], 0
)ASM"
R"ASM(
ds_read_b128 v[16:19], v24 offset:38912
s_waitcnt lgkmcnt(5)
v_mfma_f32_16x16x32_bf16 v[96:99], v[128:131], v[20:23], 0
v_mfma_f32_16x16x32_bf16 v[100:103], v[132:135], v[20:23], 0
ds_read_b128 v[20:23], v24 offset:39936
v_mfma_f32_16x16x32_bf16 v[108:111], v[132:135], v[4:7], 0
v_mfma_f32_16x16x32_bf16 v[104:107], v[128:131], v[4:7], 0
s_waitcnt vmcnt(4)
s_waitcnt lgkmcnt(5)
s_waitcnt lgkmcnt(2)
s_barrier
ds_read_b128 v[4:7], v24 offset:0
ds_read_b128 v[60:63], v30 offset:32768
ds_read_b128 v[76:79], v30 offset:49152
v_mfma_f32_16x16x32_bf16 v[80:83], v[136:139], v[8:11], v[80:83]
s_add_u32 m0, s58, 16384
s_add_u32 s59, s62, 24576
buffer_load_dwordx4 v31, s[48:51], s59 offen sc1 lds
v_mfma_f32_16x16x32_bf16 v[84:87], v[140:143], v[8:11], v[84:87]
ds_read_b128 v[8:11], v24 offset:1024
v_mfma_f32_16x16x32_bf16 v[92:95], v[140:143], v[12:15], v[92:95]
buffer_load_dwordx4 v31, s[48:51], s59 offen offset:1024 sc1 lds
v_mfma_f32_16x16x32_bf16 v[88:91], v[136:139], v[12:15], v[88:91]
ds_read_b128 v[12:15], v24 offset:2048
s_waitcnt lgkmcnt(3)
v_lshlrev_b32 v56, 16, v60
v_and_b32 v60, 0xffff0000, v60
v_lshlrev_b32 v57, 16, v61
v_and_b32 v61, 0xffff0000, v61
v_lshlrev_b32 v58, 16, v62
v_mfma_f32_16x16x32_bf16 v[96:99], v[136:139], v[16:19], v[96:99]
v_and_b32 v62, 0xffff0000, v62
v_lshlrev_b32 v59, 16, v63
v_and_b32 v63, 0xffff0000, v63
v_add_f32 v48, v48, v56
v_add_f32 v49, v49, v60
v_mfma_f32_16x16x32_bf16 v[100:103], v[140:143], v[16:19], v[100:103]
ds_read_b128 v[16:19], v24 offset:3072
v_add_f32 v50, v50, v57
v_add_f32 v51, v51, v61
v_add_f32 v52, v52, v58
v_add_f32 v53, v53, v62
v_add_f32 v54, v54, v59
v_add_f32 v55, v55, v63
v_cvt_pk_bf16_f32 v48, v48, v49
v_cvt_pk_bf16_f32 v49, v50, v51
v_cvt_pk_bf16_f32 v50, v52, v53
v_cvt_pk_bf16_f32 v51, v54, v55
buffer_store_dwordx4 v[48:51], v31, s[48:51], s87 offen offset:0 sc1
v_mfma_f32_16x16x32_bf16 v[108:111], v[140:143], v[20:23], v[108:111]
s_waitcnt lgkmcnt(3)
v_lshlrev_b32 v72, 16, v76
v_and_b32 v76, 0xffff0000, v76
v_lshlrev_b32 v73, 16, v77
v_and_b32 v77, 0xffff0000, v77
v_lshlrev_b32 v74, 16, v78
v_and_b32 v78, 0xffff0000, v78
v_lshlrev_b32 v75, 16, v79
v_and_b32 v79, 0xffff0000, v79
v_add_f32 v64, v64, v72
v_add_f32 v65, v65, v76
buffer_load_dwordx4 v31, s[48:51], s59 offen offset:2048 sc1 lds
v_mfma_f32_16x16x32_bf16 v[104:107], v[136:139], v[20:23], v[104:107]
ds_read_b128 v[20:23], v24 offset:4096
v_add_f32 v66, v66, v73
v_add_f32 v67, v67, v77
v_add_f32 v68, v68, v74
v_add_f32 v69, v69, v78
v_add_f32 v70, v70, v75
v_add_f32 v71, v71, v79
s_waitcnt lgkmcnt(3)
v_mfma_f32_16x16x32_bf16 v[80:83], v[144:147], v[4:7], v[80:83]
v_cvt_pk_bf16_f32 v64, v64, v65
v_cvt_pk_bf16_f32 v65, v66, v67
v_cvt_pk_bf16_f32 v66, v68, v69
v_cvt_pk_bf16_f32 v67, v70, v71
buffer_store_dwordx4 v[64:67], v31, s[48:51], s87 offen offset:1024 sc1
buffer_load_dwordx4 v31, s[48:51], s59 offen offset:3072 sc1 lds
v_mfma_f32_16x16x32_bf16 v[84:87], v[148:151], v[4:7], v[84:87]
ds_read_b128 v[4:7], v24 offset:5120
v_mfma_f32_16x16x32_bf16 v[92:95], v[148:151], v[8:11], v[92:95]
v_mfma_f32_16x16x32_bf16 v[88:91], v[144:147], v[8:11], v[88:91]
ds_read_b128 v[8:11], v24 offset:6144
s_waitcnt lgkmcnt(3)
v_mfma_f32_16x16x32_bf16 v[96:99], v[144:147], v[12:15], v[96:99]
v_mfma_f32_16x16x32_bf16 v[100:103], v[148:151], v[12:15], v[100:103]
ds_read_b128 v[12:15], v24 offset:7168
v_mfma_f32_16x16x32_bf16 v[108:111], v[148:151], v[16:19], v[108:111]
v_mfma_f32_16x16x32_bf16 v[104:107], v[144:147], v[16:19], v[104:107]
s_waitcnt vmcnt(6)
s_barrier
ds_read_b128 v[16:19], v24 offset:8192
s_add_u32 m0, s58, 24576
s_waitcnt lgkmcnt(3)
v_mfma_f32_16x16x32_bf16 v[80:83], v[152:155], v[20:23], v[80:83]
s_add_u32 s59, s62, 32768
buffer_load_dwordx4 v31, s[48:51], s59 offen sc1 lds
v_mfma_f32_16x16x32_bf16 v[84:87], v[156:159], v[20:23], v[84:87]
ds_read_b128 v[20:23], v24 offset:9216
v_mfma_f32_16x16x32_bf16 v[92:95], v[156:159], v[4:7], v[92:95]
buffer_load_dwordx4 v31, s[48:51], s59 offen offset:1024 sc1 lds
v_mfma_f32_16x16x32_bf16 v[88:91], v[152:155], v[4:7], v[88:91]
ds_read_b128 v[4:7], v24 offset:10240
s_waitcnt lgkmcnt(3)
v_mfma_f32_16x16x32_bf16 v[96:99], v[152:155], v[8:11], v[96:99]
v_mfma_f32_16x16x32_bf16 v[100:103], v[156:159], v[8:11], v[100:103]
ds_read_b128 v[8:11], v24 offset:11264
v_mfma_f32_16x16x32_bf16 v[108:111], v[156:159], v[12:15], v[108:111]
buffer_load_dwordx4 v31, s[48:51], s59 offen offset:2048 sc1 lds
v_mfma_f32_16x16x32_bf16 v[104:107], v[152:155], v[12:15], v[104:107]
ds_read_b128 v[12:15], v24 offset:12288
s_waitcnt lgkmcnt(3)
v_mfma_f32_16x16x32_bf16 v[80:83], v[160:163], v[16:19], v[80:83]
buffer_load_dwordx4 v31, s[48:51], s59 offen offset:3072 sc1 lds
v_mfma_f32_16x16x32_bf16 v[84:87], v[164:167], v[16:19], v[84:87]
ds_read_b128 v[16:19], v24 offset:13312
v_mfma_f32_16x16x32_bf16 v[92:95], v[164:167], v[20:23], v[92:95]
v_mfma_f32_16x16x32_bf16 v[88:91], v[160:163], v[20:23], v[88:91]
ds_read_b128 v[20:23], v24 offset:14336
s_waitcnt lgkmcnt(3)
v_mfma_f32_16x16x32_bf16 v[96:99], v[160:163], v[4:7], v[96:99]
v_mfma_f32_16x16x32_bf16 v[100:103], v[164:167], v[4:7], v[100:103]
ds_read_b128 v[4:7], v24 offset:15360
v_mfma_f32_16x16x32_bf16 v[108:111], v[164:167], v[8:11], v[108:111]
v_mfma_f32_16x16x32_bf16 v[104:107], v[160:163], v[8:11], v[104:107]
s_waitcnt vmcnt(4)
s_barrier
ds_read_b128 v[8:11], v24 offset:16384
s_add_u32 m0, s58, 32768
s_add_u32 s59, s62, 40960
buffer_load_dwordx4 v31, s[48:51], s59 offen sc1 lds
s_waitcnt lgkmcnt(3)
v_mfma_f32_16x16x32_bf16 v[80:83], v[168:171], v[12:15], v[80:83]
s_mov_b64 exec, 1
buffer_load_dwordx2 v[26:27], off, s[48:51], s89 offset:768 sc1
s_mov_b64 exec, -1
s_mov_b64 exec, 1
buffer_load_dwordx2 v[28:29], off, s[48:51], s90 offset:768 sc1
s_mov_b64 exec, -1
v_mfma_f32_16x16x32_bf16 v[84:87], v[172:175], v[12:15], v[84:87]
ds_read_b128 v[12:15], v24 offset:17408
v_mfma_f32_16x16x32_bf16 v[92:95], v[172:175], v[16:19], v[92:95]
buffer_load_dwordx4 v31, s[48:51], s59 offen offset:1024 sc1 lds
v_mfma_f32_16x16x32_bf16 v[88:91], v[168:171], v[16:19], v[88:91]
ds_read_b128 v[16:19], v24 offset:18432
s_waitcnt lgkmcnt(3)
v_mfma_f32_16x16x32_bf16 v[96:99], v[168:171], v[20:23], v[96:99]
v_mfma_f32_16x16x32_bf16 v[100:103], v[172:175], v[20:23], v[100:103]
ds_read_b128 v[20:23], v24 offset:19456
v_mfma_f32_16x16x32_bf16 v[108:111], v[172:175], v[4:7], v[108:111]
buffer_load_dwordx4 v31, s[48:51], s59 offen offset:2048 sc1 lds
v_mfma_f32_16x16x32_bf16 v[104:107], v[168:171], v[4:7], v[104:107]
ds_read_b128 v[4:7], v24 offset:20480
s_waitcnt lgkmcnt(3)
v_mfma_f32_16x16x32_bf16 v[80:83], v[176:179], v[8:11], v[80:83]
buffer_load_dwordx4 v31, s[48:51], s59 offen offset:3072 sc1 lds
v_mfma_f32_16x16x32_bf16 v[84:87], v[180:183], v[8:11], v[84:87]
ds_read_b128 v[8:11], v24 offset:21504
v_mfma_f32_16x16x32_bf16 v[92:95], v[180:183], v[12:15], v[92:95]
v_mfma_f32_16x16x32_bf16 v[88:91], v[176:179], v[12:15], v[88:91]
ds_read_b128 v[12:15], v24 offset:22528
s_mov_b32 s52, s18
s_and_b32 s53, s19, 0xffff
s_mov_b32 s54, 4194304
s_mov_b32 s55, 0x20000
s_lshl_b32 s42, s77, 8
s_waitcnt lgkmcnt(3)
v_mfma_f32_16x16x32_bf16 v[96:99], v[176:179], v[16:19], v[96:99]
s_lshl_b32 s43, s76, 6
s_add_u32 s42, s42, s43
s_add_u32 s42, s42, 0
s_lshl_b32 s42, s42, 11
s_lshl_b32 s43, s78, 8
v_mfma_f32_16x16x32_bf16 v[100:103], v[180:183], v[16:19], v[100:103]
ds_read_b128 v[16:19], v24 offset:23552
s_add_u32 s42, s42, s43
s_lshl_b32 s43, s56, 6
s_add_u32 s42, s42, s43
s_add_u32 s43, s42, 65536
v_mfma_f32_16x16x32_bf16 v[108:111], v[180:183], v[20:23], v[108:111]
v_bfe_u32 v1, v0, 0, 4
v_lshlrev_b32 v25, 11, v1
v_bfe_u32 v1, v0, 4, 2
v_lshl_add_u32 v25, v1, 4, v25
v_mfma_f32_16x16x32_bf16 v[104:107], v[176:179], v[20:23], v[104:107]
s_waitcnt vmcnt(6)
s_barrier
ds_read_b128 v[20:23], v24 offset:24576
s_add_u32 m0, s58, 0
s_waitcnt lgkmcnt(3)
v_mfma_f32_16x16x32_bf16 v[80:83], v[184:187], v[4:7], v[80:83]
s_add_u32 s59, s62, 49152
buffer_load_dwordx4 v31, s[48:51], s59 offen sc1 lds
v_mfma_f32_16x16x32_bf16 v[84:87], v[188:191], v[4:7], v[84:87]
ds_read_b128 v[4:7], v24 offset:25600
v_mfma_f32_16x16x32_bf16 v[92:95], v[188:191], v[8:11], v[92:95]
buffer_load_dwordx4 v31, s[48:51], s59 offen offset:1024 sc1 lds
v_mfma_f32_16x16x32_bf16 v[88:91], v[184:187], v[8:11], v[88:91]
ds_read_b128 v[8:11], v24 offset:26624
s_waitcnt lgkmcnt(3)
)ASM"
R"ASM(
v_mfma_f32_16x16x32_bf16 v[96:99], v[184:187], v[12:15], v[96:99]
v_mfma_f32_16x16x32_bf16 v[100:103], v[188:191], v[12:15], v[100:103]
ds_read_b128 v[12:15], v24 offset:27648
v_mfma_f32_16x16x32_bf16 v[108:111], v[188:191], v[16:19], v[108:111]
buffer_load_dwordx4 v31, s[48:51], s59 offen offset:2048 sc1 lds
v_mfma_f32_16x16x32_bf16 v[104:107], v[184:187], v[16:19], v[104:107]
ds_read_b128 v[16:19], v24 offset:28672
buffer_load_dwordx4 v31, s[48:51], s59 offen offset:3072 sc1 lds
.LchkoL2:
s_waitcnt vmcnt(8)
v_cmp_eq_u64 vcc, s[96:97], v[26:27]
s_mov_b32 s38, 0x0c0c0c0c
s_bitcmp1_b32 vcc_lo, 0
s_cselect_b32 s38, 0x01000c0c, s38
s_cselect_b32 s39, 0xffff0000, 0
.LokoL2:
s_add_u32 s74, s84, 16384
buffer_load_dwordx4 v[240:243], v31, s[48:51], s74 offen offset:0 sc1
s_waitcnt lgkmcnt(3)
v_mfma_f32_16x16x32_bf16 v[80:83], v[192:195], v[20:23], v[80:83]
buffer_load_dwordx4 v[244:247], v31, s[48:51], s74 offen offset:1024 sc1
.LchkoL3:
s_waitcnt vmcnt(9)
v_cmp_eq_u64 vcc, s[98:99], v[28:29]
s_mov_b32 s40, 0x0c0c0c0c
s_bitcmp1_b32 vcc_lo, 0
s_cselect_b32 s40, s38, s40
s_cselect_b32 s41, s39, 0
.LokoL3:
s_add_u32 s74, s84, 32768
buffer_load_dwordx4 v[248:251], v31, s[48:51], s74 offen offset:0 sc1
buffer_load_dwordx4 v[252:255], v31, s[48:51], s74 offen offset:1024 sc1
v_mfma_f32_16x16x32_bf16 v[84:87], v[196:199], v[20:23], v[84:87]
ds_read_b128 v[20:23], v24 offset:29696
v_mfma_f32_16x16x32_bf16 v[92:95], v[196:199], v[4:7], v[92:95]
v_mfma_f32_16x16x32_bf16 v[88:91], v[192:195], v[4:7], v[88:91]
ds_read_b128 v[4:7], v24 offset:30720
s_waitcnt lgkmcnt(3)
v_mfma_f32_16x16x32_bf16 v[96:99], v[192:195], v[8:11], v[96:99]
v_mfma_f32_16x16x32_bf16 v[100:103], v[196:199], v[8:11], v[100:103]
ds_read_b128 v[8:11], v24 offset:31744
v_mfma_f32_16x16x32_bf16 v[108:111], v[196:199], v[12:15], v[108:111]
v_mfma_f32_16x16x32_bf16 v[104:107], v[192:195], v[12:15], v[104:107]
s_waitcnt vmcnt(8)
s_barrier
ds_read_b128 v[12:15], v24 offset:32768
s_waitcnt lgkmcnt(3)
v_mfma_f32_16x16x32_bf16 v[80:83], v[200:203], v[16:19], v[80:83]
s_mov_b64 exec, 1
buffer_load_dwordx2 v[34:35], off, s[48:51], s88 offset:896 sc1
s_mov_b64 exec, -1
v_mfma_f32_16x16x32_bf16 v[84:87], v[204:207], v[16:19], v[84:87]
ds_read_b128 v[16:19], v24 offset:33792
v_mfma_f32_16x16x32_bf16 v[92:95], v[204:207], v[20:23], v[92:95]
v_mfma_f32_16x16x32_bf16 v[88:91], v[200:203], v[20:23], v[88:91]
ds_read_b128 v[20:23], v24 offset:34816
s_waitcnt lgkmcnt(3)
v_mfma_f32_16x16x32_bf16 v[96:99], v[200:203], v[4:7], v[96:99]
v_mfma_f32_16x16x32_bf16 v[100:103], v[204:207], v[4:7], v[100:103]
ds_read_b128 v[4:7], v24 offset:35840
v_mfma_f32_16x16x32_bf16 v[108:111], v[204:207], v[8:11], v[108:111]
v_mfma_f32_16x16x32_bf16 v[104:107], v[200:203], v[8:11], v[104:107]
ds_read_b128 v[8:11], v24 offset:36864
s_waitcnt lgkmcnt(3)
v_mfma_f32_16x16x32_bf16 v[80:83], v[208:211], v[12:15], v[80:83]
v_mfma_f32_16x16x32_bf16 v[84:87], v[212:215], v[12:15], v[84:87]
ds_read_b128 v[12:15], v24 offset:37888
v_mfma_f32_16x16x32_bf16 v[92:95], v[212:215], v[16:19], v[92:95]
v_mfma_f32_16x16x32_bf16 v[88:91], v[208:211], v[16:19], v[88:91]
ds_read_b128 v[16:19], v24 offset:38912
s_waitcnt lgkmcnt(3)
v_mfma_f32_16x16x32_bf16 v[96:99], v[208:211], v[20:23], v[96:99]
v_mfma_f32_16x16x32_bf16 v[100:103], v[212:215], v[20:23], v[100:103]
ds_read_b128 v[20:23], v24 offset:39936
v_mfma_f32_16x16x32_bf16 v[108:111], v[212:215], v[4:7], v[108:111]
v_mfma_f32_16x16x32_bf16 v[104:107], v[208:211], v[4:7], v[104:107]
s_waitcnt vmcnt(5)
s_barrier
ds_read_b128 v[4:7], v24 offset:1024
s_waitcnt vmcnt(4)
v_perm_b32 v1, v240, v240, s38
v_and_b32 v240, s39, v240
v_add_f32 v112, v112, v1
v_add_f32 v113, v113, v240
s_waitcnt lgkmcnt(3)
v_mfma_f32_16x16x32_bf16 v[80:83], v[216:219], v[8:11], v[80:83]
v_perm_b32 v1, v241, v241, s38
v_and_b32 v241, s39, v241
v_add_f32 v114, v114, v1
v_add_f32 v115, v115, v241
v_mfma_f32_16x16x32_bf16 v[84:87], v[220:223], v[8:11], v[84:87]
ds_read_b128 v[8:11], v24 offset:5120
v_perm_b32 v1, v242, v242, s38
v_and_b32 v242, s39, v242
v_add_f32 v116, v116, v1
v_add_f32 v117, v117, v242
v_mfma_f32_16x16x32_bf16 v[92:95], v[220:223], v[12:15], v[92:95]
v_perm_b32 v1, v243, v243, s38
v_and_b32 v243, s39, v243
v_add_f32 v118, v118, v1
v_add_f32 v119, v119, v243
v_mfma_f32_16x16x32_bf16 v[88:91], v[216:219], v[12:15], v[88:91]
ds_read_b128 v[12:15], v24 offset:3072
s_waitcnt vmcnt(3)
v_perm_b32 v1, v244, v244, s38
v_and_b32 v244, s39, v244
v_add_f32 v120, v120, v1
v_add_f32 v121, v121, v244
s_waitcnt lgkmcnt(3)
v_mfma_f32_16x16x32_bf16 v[96:99], v[216:219], v[16:19], v[96:99]
v_perm_b32 v1, v245, v245, s38
v_and_b32 v245, s39, v245
v_add_f32 v122, v122, v1
v_add_f32 v123, v123, v245
v_mfma_f32_16x16x32_bf16 v[100:103], v[220:223], v[16:19], v[100:103]
ds_read_b128 v[16:19], v24 offset:7168
v_perm_b32 v1, v246, v246, s38
v_and_b32 v246, s39, v246
v_add_f32 v124, v124, v1
v_add_f32 v125, v125, v246
v_mfma_f32_16x16x32_bf16 v[108:111], v[220:223], v[20:23], v[108:111]
v_perm_b32 v1, v247, v247, s38
v_and_b32 v247, s39, v247
v_add_f32 v126, v126, v1
v_add_f32 v127, v127, v247
v_mfma_f32_16x16x32_bf16 v[104:107], v[216:219], v[20:23], v[104:107]
ds_read_b128 v[20:23], v24 offset:0
s_waitcnt vmcnt(2)
v_perm_b32 v1, v248, v248, s40
v_and_b32 v248, s41, v248
v_add_f32 v112, v112, v1
v_add_f32 v113, v113, v248
v_perm_b32 v1, v249, v249, s40
v_and_b32 v249, s41, v249
v_add_f32 v114, v114, v1
v_add_f32 v115, v115, v249
s_waitcnt lgkmcnt(3)
v_mfma_f32_16x16x32_bf16 v[88:91], v[224:227], v[4:7], v[88:91]
.LchkoL1:
s_waitcnt vmcnt(0)
v_cmp_eq_u64 vcc, s[94:95], v[34:35]
s_mov_b32 s36, 0x0c0c0c0c
s_bitcmp1_b32 vcc_lo, 0
s_cselect_b32 s36, s40, s36
s_cselect_b32 s37, s41, 0
.LokoL1:
s_add_u32 s74, s84, 0
buffer_load_dwordx4 v[36:39], v31, s[48:51], s74 offen offset:0 sc1
buffer_load_dwordx4 v[40:43], v31, s[48:51], s74 offen offset:1024 sc1
v_mfma_f32_16x16x32_bf16 v[92:95], v[228:231], v[4:7], v[92:95]
ds_read_b128 v[4:7], v24 offset:4096
v_perm_b32 v1, v250, v250, s40
v_and_b32 v250, s41, v250
v_add_f32 v116, v116, v1
v_add_f32 v117, v117, v250
v_mfma_f32_16x16x32_bf16 v[92:95], v[236:239], v[8:11], v[92:95]
v_perm_b32 v1, v251, v251, s40
v_and_b32 v251, s41, v251
v_add_f32 v118, v118, v1
v_add_f32 v119, v119, v251
v_mfma_f32_16x16x32_bf16 v[88:91], v[232:235], v[8:11], v[88:91]
ds_read_b128 v[8:11], v24 offset:2048
v_perm_b32 v1, v252, v252, s40
v_and_b32 v252, s41, v252
v_add_f32 v120, v120, v1
v_add_f32 v121, v121, v252
s_waitcnt lgkmcnt(3)
v_mfma_f32_16x16x32_bf16 v[104:107], v[224:227], v[12:15], v[104:107]
v_perm_b32 v1, v253, v253, s40
v_and_b32 v253, s41, v253
v_add_f32 v122, v122, v1
v_add_f32 v123, v123, v253
v_mfma_f32_16x16x32_bf16 v[108:111], v[228:231], v[12:15], v[108:111]
ds_read_b128 v[12:15], v24 offset:6144
ds_write_b128 v30, v[88:91] offset:34816
v_perm_b32 v1, v254, v254, s40
v_and_b32 v254, s41, v254
v_add_f32 v124, v124, v1
v_mfma_f32_16x16x32_bf16 v[108:111], v[236:239], v[16:19], v[108:111]
v_add_f32 v125, v125, v254
v_perm_b32 v1, v255, v255, s40
v_and_b32 v255, s41, v255
v_add_f32 v126, v126, v1
v_add_f32 v127, v127, v255
v_mfma_f32_16x16x32_bf16 v[104:107], v[232:235], v[16:19], v[104:107]
s_waitcnt lgkmcnt(3)
v_mfma_f32_16x16x32_bf16 v[80:83], v[224:227], v[20:23], v[80:83]
ds_write_b128 v30, v[92:95] offset:35840
v_mfma_f32_16x16x32_bf16 v[84:87], v[228:231], v[20:23], v[84:87]
v_mfma_f32_16x16x32_bf16 v[84:87], v[236:239], v[4:7], v[84:87]
s_nop 2
ds_write_b128 v30, v[104:107] offset:51200
v_mfma_f32_16x16x32_bf16 v[80:83], v[232:235], v[4:7], v[80:83]
s_waitcnt lgkmcnt(3)
v_mfma_f32_16x16x32_bf16 v[96:99], v[224:227], v[8:11], v[96:99]
ds_write_b128 v30, v[108:111] offset:52224
v_mfma_f32_16x16x32_bf16 v[100:103], v[228:231], v[8:11], v[100:103]
v_mfma_f32_16x16x32_bf16 v[100:103], v[236:239], v[12:15], v[100:103]
v_mfma_f32_16x16x32_bf16 v[96:99], v[232:235], v[12:15], v[96:99]
s_waitcnt lgkmcnt(0)
)ASM"
R"ASM(
s_barrier
ds_read_b128 v[88:91], v30 offset:32768
ds_read_b128 v[92:95], v30 offset:33792
ds_read_b128 v[104:107], v30 offset:49152
ds_read_b128 v[108:111], v30 offset:50176
s_waitcnt lgkmcnt(3)
v_add_f32 v80, v80, v88
v_add_f32 v81, v81, v89
v_add_f32 v82, v82, v90
v_add_f32 v83, v83, v91
s_waitcnt lgkmcnt(2)
v_add_f32 v84, v84, v92
v_add_f32 v85, v85, v93
v_add_f32 v86, v86, v94
v_add_f32 v87, v87, v95
s_waitcnt lgkmcnt(1)
v_add_f32 v96, v96, v104
v_add_f32 v97, v97, v105
v_add_f32 v98, v98, v106
v_add_f32 v99, v99, v107
s_waitcnt lgkmcnt(0)
v_add_f32 v100, v100, v108
v_add_f32 v101, v101, v109
v_add_f32 v102, v102, v110
v_add_f32 v103, v103, v111
s_waitcnt vmcnt(1)
v_perm_b32 v1, v36, v36, s36
v_and_b32 v36, s37, v36
v_add_f32 v112, v112, v1
v_add_f32 v113, v113, v36
v_perm_b32 v1, v37, v37, s36
v_and_b32 v37, s37, v37
v_add_f32 v114, v114, v1
v_add_f32 v115, v115, v37
v_perm_b32 v1, v38, v38, s36
v_and_b32 v38, s37, v38
v_add_f32 v116, v116, v1
v_add_f32 v117, v117, v38
v_perm_b32 v1, v39, v39, s36
v_and_b32 v39, s37, v39
v_add_f32 v118, v118, v1
v_add_f32 v119, v119, v39
s_waitcnt vmcnt(0)
v_perm_b32 v1, v40, v40, s36
v_and_b32 v40, s37, v40
v_add_f32 v120, v120, v1
v_add_f32 v121, v121, v40
v_perm_b32 v1, v41, v41, s36
v_and_b32 v41, s37, v41
v_add_f32 v122, v122, v1
v_add_f32 v123, v123, v41
v_perm_b32 v1, v42, v42, s36
v_and_b32 v42, s37, v42
v_add_f32 v124, v124, v1
v_add_f32 v125, v125, v42
v_perm_b32 v1, v43, v43, s36
v_and_b32 v43, s37, v43
v_add_f32 v126, v126, v1
v_add_f32 v127, v127, v43
s_cmp_eq_u32 s39, 0
s_cbranch_scc1 .LslowL2
.LbackL2:
s_cmp_eq_u32 s41, 0
s_cbranch_scc1 .LslowL3
.LbackL3:
s_cmp_eq_u32 s37, 0
s_cbranch_scc1 .LslowL1
.LbackL1:
v_add_f32 v80, v80, v112
v_add_f32 v81, v81, v113
v_add_f32 v82, v82, v114
v_add_f32 v83, v83, v115
v_add_f32 v84, v84, v116
v_add_f32 v85, v85, v117
v_add_f32 v86, v86, v118
v_add_f32 v87, v87, v119
v_cvt_pk_bf16_f32 v80, v80, v81
v_cvt_pk_bf16_f32 v81, v82, v83
v_cvt_pk_bf16_f32 v82, v84, v85
v_cvt_pk_bf16_f32 v83, v86, v87
buffer_store_dwordx4 v[80:83], v25, s[52:55], s42 offen sc1
v_add_f32 v96, v96, v120
v_add_f32 v97, v97, v121
v_add_f32 v98, v98, v122
v_add_f32 v99, v99, v123
v_add_f32 v100, v100, v124
v_add_f32 v101, v101, v125
v_add_f32 v102, v102, v126
v_add_f32 v103, v103, v127
v_cvt_pk_bf16_f32 v96, v96, v97
v_cvt_pk_bf16_f32 v97, v98, v99
v_cvt_pk_bf16_f32 v98, v100, v101
v_cvt_pk_bf16_f32 v99, v102, v103
buffer_store_dwordx4 v[96:99], v25, s[52:55], s43 offen sc1
s_barrier
s_endpgm
.LslowL2:
buffer_load_dwordx2 v[26:27], off, s[48:51], s89 offset:768 sc1
s_waitcnt vmcnt(0)
v_cmp_eq_u64 vcc, s[96:97], v[26:27]
s_cmp_eq_u64 vcc, exec
s_cbranch_scc1 .LsgL2
s_sub_u32 s83, s83, 1
s_cbranch_scc0 .LslowL2
s_mov_b32 s83, 0
.LsgL2:
s_mov_b32 s38, 0x01000c0c
s_mov_b32 s39, 0xffff0000
s_add_u32 s74, s84, 16384
buffer_load_dwordx4 v[240:243], v31, s[48:51], s74 offen offset:0 sc1
buffer_load_dwordx4 v[244:247], v31, s[48:51], s74 offen offset:1024 sc1
s_waitcnt vmcnt(0)
v_perm_b32 v1, v240, v240, s38
v_and_b32 v240, s39, v240
v_add_f32 v112, v112, v1
v_add_f32 v113, v113, v240
v_perm_b32 v1, v241, v241, s38
v_and_b32 v241, s39, v241
v_add_f32 v114, v114, v1
v_add_f32 v115, v115, v241
v_perm_b32 v1, v242, v242, s38
v_and_b32 v242, s39, v242
v_add_f32 v116, v116, v1
v_add_f32 v117, v117, v242
v_perm_b32 v1, v243, v243, s38
v_and_b32 v243, s39, v243
v_add_f32 v118, v118, v1
v_add_f32 v119, v119, v243
v_perm_b32 v1, v244, v244, s38
v_and_b32 v244, s39, v244
v_add_f32 v120, v120, v1
v_add_f32 v121, v121, v244
v_perm_b32 v1, v245, v245, s38
v_and_b32 v245, s39, v245
v_add_f32 v122, v122, v1
v_add_f32 v123, v123, v245
v_perm_b32 v1, v246, v246, s38
v_and_b32 v246, s39, v246
v_add_f32 v124, v124, v1
v_add_f32 v125, v125, v246
v_perm_b32 v1, v247, v247, s38
v_and_b32 v247, s39, v247
v_add_f32 v126, v126, v1
v_add_f32 v127, v127, v247
s_branch .LbackL2
.LslowL3:
buffer_load_dwordx2 v[28:29], off, s[48:51], s90 offset:768 sc1
s_waitcnt vmcnt(0)
v_cmp_eq_u64 vcc, s[98:99], v[28:29]
s_cmp_eq_u64 vcc, exec
s_cbranch_scc1 .LsgL3
s_sub_u32 s83, s83, 1
s_cbranch_scc0 .LslowL3
s_mov_b32 s83, 0
.LsgL3:
s_mov_b32 s40, 0x01000c0c
s_mov_b32 s41, 0xffff0000
s_add_u32 s74, s84, 32768
buffer_load_dwordx4 v[248:251], v31, s[48:51], s74 offen offset:0 sc1
buffer_load_dwordx4 v[252:255], v31, s[48:51], s74 offen offset:1024 sc1
s_waitcnt vmcnt(0)
v_perm_b32 v1, v248, v248, s40
v_and_b32 v248, s41, v248
v_add_f32 v112, v112, v1
v_add_f32 v113, v113, v248
v_perm_b32 v1, v249, v249, s40
v_and_b32 v249, s41, v249
v_add_f32 v114, v114, v1
v_add_f32 v115, v115, v249
v_perm_b32 v1, v250, v250, s40
v_and_b32 v250, s41, v250
v_add_f32 v116, v116, v1
v_add_f32 v117, v117, v250
v_perm_b32 v1, v251, v251, s40
v_and_b32 v251, s41, v251
v_add_f32 v118, v118, v1
v_add_f32 v119, v119, v251
v_perm_b32 v1, v252, v252, s40
v_and_b32 v252, s41, v252
v_add_f32 v120, v120, v1
v_add_f32 v121, v121, v252
v_perm_b32 v1, v253, v253, s40
v_and_b32 v253, s41, v253
v_add_f32 v122, v122, v1
v_add_f32 v123, v123, v253
v_perm_b32 v1, v254, v254, s40
v_and_b32 v254, s41, v254
v_add_f32 v124, v124, v1
v_add_f32 v125, v125, v254
v_perm_b32 v1, v255, v255, s40
v_and_b32 v255, s41, v255
v_add_f32 v126, v126, v1
v_add_f32 v127, v127, v255
s_branch .LbackL3
.LslowL1:
buffer_load_dwordx2 v[34:35], off, s[48:51], s88 offset:896 sc1
s_waitcnt vmcnt(0)
v_cmp_eq_u64 vcc, s[94:95], v[34:35]
s_cmp_eq_u64 vcc, exec
s_cbranch_scc1 .LsgL1
)ASM"
R"ASM(
s_sub_u32 s83, s83, 1
s_cbranch_scc0 .LslowL1
s_mov_b32 s83, 0
.LsgL1:
s_mov_b32 s36, 0x01000c0c
s_mov_b32 s37, 0xffff0000
s_add_u32 s74, s84, 0
buffer_load_dwordx4 v[36:39], v31, s[48:51], s74 offen offset:0 sc1
buffer_load_dwordx4 v[40:43], v31, s[48:51], s74 offen offset:1024 sc1
s_waitcnt vmcnt(0)
v_perm_b32 v1, v36, v36, s36
v_and_b32 v36, s37, v36
v_add_f32 v112, v112, v1
v_add_f32 v113, v113, v36
v_perm_b32 v1, v37, v37, s36
v_and_b32 v37, s37, v37
v_add_f32 v114, v114, v1
v_add_f32 v115, v115, v37
v_perm_b32 v1, v38, v38, s36
v_and_b32 v38, s37, v38
v_add_f32 v116, v116, v1
v_add_f32 v117, v117, v38
v_perm_b32 v1, v39, v39, s36
v_and_b32 v39, s37, v39
v_add_f32 v118, v118, v1
v_add_f32 v119, v119, v39
v_perm_b32 v1, v40, v40, s36
v_and_b32 v40, s37, v40
v_add_f32 v120, v120, v1
v_add_f32 v121, v121, v40
v_perm_b32 v1, v41, v41, s36
v_and_b32 v41, s37, v41
v_add_f32 v122, v122, v1
v_add_f32 v123, v123, v41
v_perm_b32 v1, v42, v42, s36
v_and_b32 v42, s37, v42
v_add_f32 v124, v124, v1
v_add_f32 v125, v125, v42
v_perm_b32 v1, v43, v43, s36
v_and_b32 v43, s37, v43
v_add_f32 v126, v126, v1
v_add_f32 v127, v127, v43
s_branch .LbackL1
.LroleH:
s_sub_u32 s56, s56, 4
s_lshl_b32 s57, s56, 10
s_andn2_b32 s72, s79, 0x38
s_lshr_b32 s73, s76, 1
s_xor_b32 s74, s73, 1
s_lshl_b32 s75, s74, 2
s_add_u32 s75, s75, s56
s_lshl_b32 s75, s75, 3
s_add_u32 s75, s75, s72
s_lshl_b32 s66, s75, 13
s_lshl_b32 s75, s73, 2
s_add_u32 s75, s75, s56
s_lshl_b32 s75, s75, 3
s_add_u32 s75, s75, s72
s_lshl_b32 s67, s75, 13
s_waitcnt lgkmcnt(0)
s_load_dwordx2 s[68:69], s[20:21], s66
s_load_dwordx2 s[70:71], s[20:21], s67
s_lshl_b32 s81, s79, 13
s_load_dwordx2 s[92:93], s[20:21], s81
s_xor_b32 s88, s81, 8192
s_load_dwordx2 s[94:95], s[20:21], s88
s_xor_b32 s89, s81, 16384
s_load_dwordx2 s[96:97], s[20:21], s89
s_xor_b32 s90, s81, 24576
s_load_dwordx2 s[98:99], s[20:21], s90
s_mov_b32 s52, s12
s_and_b32 s53, s13, 0xffff
s_mov_b32 s54, 7168
s_mov_b32 s55, 0x20000
s_mov_b32 s36, s16
s_and_b32 s37, s17, 0xffff
s_mov_b32 s38, 2048
s_mov_b32 s39, 0x20000
v_lshlrev_b32 v34, 3, v0
s_and_b32 s75, s78, 3
s_lshl_b32 s61, s76, 2
s_add_u32 s75, s75, s61
s_mul_i32 s61, s75, 448
s_lshl_b32 s62, s78, 8
buffer_load_dword v45, v34, s[52:55], s61 offen
buffer_load_dword v45, v34, s[36:39], s62 offen
v_lshlrev_b32 v31, 4, v0
v_bfe_u32 v1, v0, 4, 2
v_lshlrev_b32 v24, 10, v1
v_bfe_u32 v1, v0, 2, 2
v_lshl_add_u32 v24, v1, 7, v24
v_bfe_u32 v1, v0, 3, 2
v_lshl_add_u32 v24, v1, 5, v24
v_bfe_u32 v1, v0, 0, 2
v_lshl_add_u32 v24, v1, 3, v24
v_add_u32 v24, 40960, v24
v_xor_b32 v25, 32, v24
v_xor_b32 v26, 64, v24
v_xor_b32 v27, 96, v24
v_bfe_u32 v1, v0, 1, 3
v_lshrrev_b32 v28, 4, v0
v_xor_b32 v1, v1, v28
v_and_b32 v28, 15, v0
v_lshlrev_b32 v28, 7, v28
v_lshl_add_u32 v28, v1, 4, v28
s_lshl_b32 s72, s56, 12
s_add_u32 s73, s72, 81920
v_add_u32 v28, s73, v28
v_xor_b32 v29, 64, v28
s_add_u32 s72, s72, 98304
v_add_u32 v30, s72, v31
s_mov_b32 s48, s20
s_and_b32 s49, s21, 0xffff
s_mov_b32 s50, 33554432
s_mov_b32 s51, 0x20000
s_lshl_b32 s80, s77, 2
s_add_u32 s80, s80, s76
s_mul_i32 s80, s80, 458752
s_mul_i32 s72, s78, 57344
s_add_u32 s80, s80, s72
s_lshl_b32 s72, s56, 11
s_add_u32 s80, s80, s72
s_add_u32 s80, s80, 2098176
s_lshl_b32 s72, s56, 12
s_add_u32 s72, s72, 16779264
s_mul_i32 s84, s79, 49152
s_add_u32 s84, s84, s72
s_and_b32 s75, s76, 1
s_xor_b32 s75, s75, 2
s_xor_b32 s73, s79, s75
s_mul_i32 s73, s73, 49152
s_sub_u32 s75, s75, 1
s_lshl_b32 s75, s75, 14
s_add_u32 s73, s73, s75
s_add_u32 s85, s73, s72
s_and_b32 s75, s76, 1
s_xor_b32 s75, s75, 3
s_xor_b32 s73, s79, s75
s_mul_i32 s73, s73, 49152
s_sub_u32 s75, s75, 1
s_lshl_b32 s75, s75, 14
s_add_u32 s73, s73, s75
s_add_u32 s86, s73, s72
s_mov_b32 s75, 1
s_xor_b32 s73, s79, s75
s_mul_i32 s73, s73, 49152
s_sub_u32 s75, s75, 1
s_lshl_b32 s75, s75, 14
s_add_u32 s73, s73, s75
s_add_u32 s87, s73, s72
s_mul_i32 s72, s76, 1835008
s_lshl_b32 s73, s78, 8
s_add_u32 s72, s72, s73
s_add_u32 s44, s14, s72
s_addc_u32 s45, s15, 0
s_and_b32 s45, s45, 0xffff
s_sub_u32 s46, 7340032, s72
s_mov_b32 s47, 0x20000
s_lshr_b32 s72, s77, 2
s_mul_i32 s72, s72, 448
s_and_b32 s73, s77, 3
s_lshl_b32 s73, s73, 3
s_add_u32 s72, s72, s73
s_lshl_b32 s72, s72, 11
v_bfe_u32 v1, v0, 1, 3
v_lshlrev_b32 v43, 11, v1
v_bfe_u32 v1, v0, 0, 1
v_lshl_add_u32 v43, v1, 7, v43
v_add_u32 v43, s72, v43
s_mov_b32 s40, s10
s_and_b32 s41, s11, 0xffff
s_mov_b32 s42, 7340032
s_mov_b32 s43, 0x20000
s_lshl_b32 s72, s77, 1
s_lshr_b32 s73, s78, 2
s_add_u32 s72, s72, s73
s_lshl_b32 s72, s72, 2
s_add_u32 s72, s72, s56
s_lshl_b32 s72, s72, 4
v_and_b32 v42, 15, v0
v_add_u32 v42, s72, v42
s_movk_i32 s74, 7168
v_mul_u32_u24 v42, s74, v42
s_and_b32 s72, s78, 3
s_lshl_b32 s73, s76, 2
s_add_u32 s72, s72, s73
s_mul_i32 s72, s72, 448
s_and_b32 s73, s78, 1
s_lshl_b32 s65, s73, 6
s_add_u32 s65, s65, s72
s_mov_b32 s60, s65
s_add_u32 s65, s65, 256
v_lshrrev_b32 v40, 4, v0
v_lshlrev_b32 v40, 3, v40
s_mul_i32 s91, s73, 384
s_sub_u32 s91, s72, s91
s_add_u32 s91, s91, 384
s_waitcnt lgkmcnt(0)
s_barrier
ds_read_b128 v[128:131], v28 offset:16384
)ASM"
R"ASM(
ds_read_b128 v[132:135], v28 offset:18432
ds_read_b64_tr_b16 v[4:5], v24 offset:0
ds_read_b64_tr_b16 v[6:7], v24 offset:512
buffer_load_dwordx2 v[36:37], v40, s[52:55], s60 offen offset:0
buffer_load_dwordx2 v[38:39], v40, s[52:55], s60 offen offset:32
ds_read_b64_tr_b16 v[8:9], v25 offset:0
ds_read_b64_tr_b16 v[10:11], v25 offset:512
ds_read_b64_tr_b16 v[12:13], v26 offset:0
ds_read_b64_tr_b16 v[14:15], v26 offset:512
ds_read_b128 v[136:139], v29 offset:16384
ds_read_b64_tr_b16 v[16:17], v27 offset:0
ds_read_b64_tr_b16 v[18:19], v27 offset:512
ds_read_b128 v[140:143], v29 offset:18432
ds_read_b64_tr_b16 v[20:21], v24 offset:4096
s_waitcnt lgkmcnt(7)
v_mfma_f32_16x16x32_bf16 v[48:51], v[4:7], v[128:131], 0
ds_read_b64_tr_b16 v[22:23], v24 offset:4608
s_mov_b32 s82, 0xc0135761
v_mfma_f32_16x16x32_bf16 v[56:59], v[4:7], v[132:135], 0
ds_read_b64_tr_b16 v[4:5], v25 offset:4096
v_mfma_f32_16x16x32_bf16 v[60:63], v[8:11], v[132:135], 0
ds_read_b64_tr_b16 v[6:7], v25 offset:4608
v_mov_b32 v32, 0xbdd2d3e8
v_mfma_f32_16x16x32_bf16 v[52:55], v[8:11], v[128:131], 0
ds_read_b64_tr_b16 v[8:9], v26 offset:4096
s_waitcnt lgkmcnt(6)
v_mfma_f32_16x16x32_bf16 v[64:67], v[12:15], v[128:131], 0
ds_read_b64_tr_b16 v[10:11], v26 offset:4608
s_mov_b32 s83, 200000
v_mfma_f32_16x16x32_bf16 v[72:75], v[12:15], v[132:135], 0
ds_read_b64_tr_b16 v[12:13], v27 offset:4096
v_mfma_f32_16x16x32_bf16 v[76:79], v[16:19], v[132:135], 0
ds_read_b64_tr_b16 v[14:15], v27 offset:4608
s_mov_b32 s72, 0
v_mfma_f32_16x16x32_bf16 v[68:71], v[16:19], v[128:131], 0
s_waitcnt lgkmcnt(8)
s_barrier
ds_read_b64_tr_b16 v[16:17], v24 offset:8192
ds_read_b128 v[144:147], v28 offset:32768
s_waitcnt lgkmcnt(6)
v_mfma_f32_16x16x32_bf16 v[80:83], v[20:23], v[128:131], 0
ds_read_b64_tr_b16 v[18:19], v24 offset:8704
s_add_u32 s72, s72, 0
v_mfma_f32_16x16x32_bf16 v[88:91], v[20:23], v[132:135], 0
ds_read_b64_tr_b16 v[20:21], v25 offset:8192
ds_read_b128 v[148:151], v28 offset:34816
v_mfma_f32_16x16x32_bf16 v[92:95], v[4:7], v[132:135], 0
ds_read_b64_tr_b16 v[22:23], v25 offset:8704
s_add_u32 s81, s81, s72
v_mfma_f32_16x16x32_bf16 v[84:87], v[4:7], v[128:131], 0
ds_read_b64_tr_b16 v[4:5], v26 offset:8192
ds_read_b128 v[152:155], v29 offset:32768
s_waitcnt lgkmcnt(8)
v_mfma_f32_16x16x32_bf16 v[96:99], v[8:11], v[128:131], 0
ds_read_b64_tr_b16 v[6:7], v26 offset:8704
s_add_u32 s88, s88, s72
v_mfma_f32_16x16x32_bf16 v[104:107], v[8:11], v[132:135], 0
ds_read_b64_tr_b16 v[8:9], v27 offset:8192
ds_read_b128 v[156:159], v29 offset:34816
v_mfma_f32_16x16x32_bf16 v[108:111], v[12:15], v[132:135], 0
ds_read_b64_tr_b16 v[10:11], v27 offset:8704
s_add_u32 s89, s89, s72
v_mfma_f32_16x16x32_bf16 v[100:103], v[12:15], v[128:131], 0
ds_read_b64_tr_b16 v[12:13], v24 offset:12288
s_waitcnt lgkmcnt(7)
v_mfma_f32_16x16x32_bf16 v[48:51], v[16:19], v[136:139], v[48:51]
ds_read_b64_tr_b16 v[14:15], v24 offset:12800
s_add_u32 s90, s90, s72
v_mfma_f32_16x16x32_bf16 v[56:59], v[16:19], v[140:143], v[56:59]
ds_read_b64_tr_b16 v[16:17], v25 offset:12288
s_add_u32 s92, s92, 0x7f4a7c15
v_mfma_f32_16x16x32_bf16 v[60:63], v[20:23], v[140:143], v[60:63]
ds_read_b64_tr_b16 v[18:19], v25 offset:12800
s_addc_u32 s93, s93, 0x9e3779b9
v_mfma_f32_16x16x32_bf16 v[52:55], v[20:23], v[136:139], v[52:55]
ds_read_b64_tr_b16 v[20:21], v26 offset:12288
s_waitcnt lgkmcnt(5)
v_mfma_f32_16x16x32_bf16 v[64:67], v[4:7], v[136:139], v[64:67]
ds_read_b64_tr_b16 v[22:23], v26 offset:12800
s_mul_i32 s92, s92, 0x85ebca6b
v_mfma_f32_16x16x32_bf16 v[72:75], v[4:7], v[140:143], v[72:75]
ds_read_b64_tr_b16 v[4:5], v27 offset:12288
v_mfma_f32_16x16x32_bf16 v[76:79], v[8:11], v[140:143], v[76:79]
ds_read_b64_tr_b16 v[6:7], v27 offset:12800
s_lshr_b32 s75, s92, 13
v_mfma_f32_16x16x32_bf16 v[68:71], v[8:11], v[136:139], v[68:71]
s_barrier
ds_read_b64_tr_b16 v[8:9], v24 offset:16384
s_waitcnt lgkmcnt(5)
v_mfma_f32_16x16x32_bf16 v[80:83], v[12:15], v[136:139], v[80:83]
ds_read_b64_tr_b16 v[10:11], v24 offset:16896
s_xor_b32 s92, s92, s75
v_mfma_f32_16x16x32_bf16 v[88:91], v[12:15], v[140:143], v[88:91]
ds_read_b64_tr_b16 v[12:13], v25 offset:16384
v_mfma_f32_16x16x32_bf16 v[92:95], v[16:19], v[140:143], v[92:95]
ds_read_b64_tr_b16 v[14:15], v25 offset:16896
s_xor_b32 s93, s93, s92
v_mfma_f32_16x16x32_bf16 v[84:87], v[16:19], v[136:139], v[84:87]
ds_read_b64_tr_b16 v[16:17], v26 offset:16384
s_waitcnt lgkmcnt(5)
v_mfma_f32_16x16x32_bf16 v[96:99], v[20:23], v[136:139], v[96:99]
ds_read_b64_tr_b16 v[18:19], v26 offset:16896
s_mul_i32 s93, s93, 0xc2b2ae35
v_mfma_f32_16x16x32_bf16 v[104:107], v[20:23], v[140:143], v[104:107]
ds_read_b64_tr_b16 v[20:21], v27 offset:16384
v_mfma_f32_16x16x32_bf16 v[108:111], v[4:7], v[140:143], v[108:111]
ds_read_b64_tr_b16 v[22:23], v27 offset:16896
s_lshr_b32 s75, s93, 16
v_mfma_f32_16x16x32_bf16 v[100:103], v[4:7], v[136:139], v[100:103]
ds_read_b64_tr_b16 v[4:5], v24 offset:20480
s_waitcnt lgkmcnt(5)
v_mfma_f32_16x16x32_bf16 v[48:51], v[8:11], v[144:147], v[48:51]
ds_read_b64_tr_b16 v[6:7], v24 offset:20992
s_xor_b32 s93, s93, s75
v_mfma_f32_16x16x32_bf16 v[56:59], v[8:11], v[148:151], v[56:59]
ds_read_b64_tr_b16 v[8:9], v25 offset:20480
v_mfma_f32_16x16x32_bf16 v[60:63], v[12:15], v[148:151], v[60:63]
ds_read_b64_tr_b16 v[10:11], v25 offset:20992
s_xor_b32 s92, s92, s93
v_mfma_f32_16x16x32_bf16 v[52:55], v[12:15], v[144:147], v[52:55]
ds_read_b64_tr_b16 v[12:13], v26 offset:20480
s_waitcnt lgkmcnt(5)
v_mfma_f32_16x16x32_bf16 v[64:67], v[16:19], v[144:147], v[64:67]
ds_read_b64_tr_b16 v[14:15], v26 offset:20992
s_mul_i32 s92, s92, 0x27d4eb2f
v_mfma_f32_16x16x32_bf16 v[72:75], v[16:19], v[148:151], v[72:75]
ds_read_b64_tr_b16 v[16:17], v27 offset:20480
v_mfma_f32_16x16x32_bf16 v[76:79], v[20:23], v[148:151], v[76:79]
ds_read_b64_tr_b16 v[18:19], v27 offset:20992
s_lshr_b32 s75, s92, 15
v_mfma_f32_16x16x32_bf16 v[68:71], v[20:23], v[144:147], v[68:71]
s_barrier
ds_read_b64_tr_b16 v[20:21], v24 offset:24576
ds_read_b128 v[160:163], v28 offset:0
s_waitcnt vmcnt(1)
v_lshlrev_b32 v41, 16, v36
v_and_b32 v36, 0xffff0000, v36
v_add_f32 v48, v48, v41
v_add_f32 v49, v49, v36
v_add_f32 v56, v56, v41
v_add_f32 v57, v57, v36
s_waitcnt lgkmcnt(6)
v_mfma_f32_16x16x32_bf16 v[80:83], v[4:7], v[144:147], v[80:83]
ds_read_b64_tr_b16 v[22:23], v24 offset:25088
v_lshlrev_b32 v41, 16, v37
v_and_b32 v37, 0xffff0000, v37
v_add_f32 v50, v50, v41
v_add_f32 v51, v51, v37
v_add_f32 v58, v58, v41
v_add_f32 v59, v59, v37
s_xor_b32 s92, s92, s75
v_mfma_f32_16x16x32_bf16 v[88:91], v[4:7], v[148:151], v[88:91]
ds_read_b64_tr_b16 v[4:5], v25 offset:24576
ds_read_b128 v[164:167], v28 offset:2048
s_waitcnt vmcnt(0)
v_lshlrev_b32 v41, 16, v38
v_and_b32 v38, 0xffff0000, v38
v_add_f32 v52, v52, v41
v_add_f32 v53, v53, v38
v_add_f32 v60, v60, v41
v_add_f32 v61, v61, v38
v_mfma_f32_16x16x32_bf16 v[92:95], v[8:11], v[148:151], v[92:95]
ds_read_b64_tr_b16 v[6:7], v25 offset:25088
v_lshlrev_b32 v41, 16, v39
v_and_b32 v39, 0xffff0000, v39
v_add_f32 v54, v54, v41
v_add_f32 v55, v55, v39
v_add_f32 v62, v62, v41
v_add_f32 v63, v63, v39
s_add_u32 s94, s94, 0x7f4a7c15
s_addc_u32 s95, s95, 0x9e3779b9
v_mfma_f32_16x16x32_bf16 v[84:87], v[8:11], v[144:147], v[84:87]
ds_read_b64_tr_b16 v[8:9], v26 offset:24576
ds_read_b128 v[168:171], v29 offset:0
s_waitcnt lgkmcnt(8)
v_mfma_f32_16x16x32_bf16 v[96:99], v[12:15], v[144:147], v[96:99]
ds_read_b64_tr_b16 v[10:11], v26 offset:25088
s_mul_i32 s94, s94, 0x85ebca6b
v_mfma_f32_16x16x32_bf16 v[104:107], v[12:15], v[148:151], v[104:107]
ds_read_b64_tr_b16 v[12:13], v27 offset:24576
ds_read_b128 v[172:175], v29 offset:2048
v_mfma_f32_16x16x32_bf16 v[108:111], v[16:19], v[148:151], v[108:111]
ds_read_b64_tr_b16 v[14:15], v27 offset:25088
s_lshr_b32 s75, s94, 13
v_mfma_f32_16x16x32_bf16 v[100:103], v[16:19], v[144:147], v[100:103]
ds_read_b64_tr_b16 v[16:17], v24 offset:28672
buffer_load_dwordx2 v[36:37], v40, s[52:55], s60 offen offset:64
s_waitcnt lgkmcnt(7)
v_mfma_f32_16x16x32_bf16 v[48:51], v[20:23], v[152:155], v[48:51]
ds_read_b64_tr_b16 v[18:19], v24 offset:29184
buffer_load_dwordx2 v[38:39], v40, s[52:55], s60 offen offset:96
s_xor_b32 s94, s94, s75
v_mfma_f32_16x16x32_bf16 v[56:59], v[20:23], v[156:159], v[56:59]
ds_read_b64_tr_b16 v[20:21], v25 offset:28672
v_mfma_f32_16x16x32_bf16 v[60:63], v[4:7], v[156:159], v[60:63]
ds_read_b64_tr_b16 v[22:23], v25 offset:29184
s_xor_b32 s95, s95, s94
v_mfma_f32_16x16x32_bf16 v[52:55], v[4:7], v[152:155], v[52:55]
ds_read_b64_tr_b16 v[4:5], v26 offset:28672
s_waitcnt lgkmcnt(5)
)ASM"
R"ASM(
v_mfma_f32_16x16x32_bf16 v[64:67], v[8:11], v[152:155], v[64:67]
ds_read_b64_tr_b16 v[6:7], v26 offset:29184
s_mul_i32 s95, s95, 0xc2b2ae35
v_mfma_f32_16x16x32_bf16 v[72:75], v[8:11], v[156:159], v[72:75]
ds_read_b64_tr_b16 v[8:9], v27 offset:28672
v_mfma_f32_16x16x32_bf16 v[76:79], v[12:15], v[156:159], v[76:79]
ds_read_b64_tr_b16 v[10:11], v27 offset:29184
s_lshr_b32 s75, s95, 16
v_mfma_f32_16x16x32_bf16 v[68:71], v[12:15], v[152:155], v[68:71]
s_barrier
ds_read_b64_tr_b16 v[12:13], v24 offset:32768
s_waitcnt lgkmcnt(5)
v_mfma_f32_16x16x32_bf16 v[80:83], v[16:19], v[152:155], v[80:83]
ds_read_b64_tr_b16 v[14:15], v24 offset:33280
s_xor_b32 s95, s95, s75
v_mfma_f32_16x16x32_bf16 v[88:91], v[16:19], v[156:159], v[88:91]
ds_read_b64_tr_b16 v[16:17], v25 offset:32768
v_mfma_f32_16x16x32_bf16 v[92:95], v[20:23], v[156:159], v[92:95]
ds_read_b64_tr_b16 v[18:19], v25 offset:33280
s_xor_b32 s94, s94, s95
v_mfma_f32_16x16x32_bf16 v[84:87], v[20:23], v[152:155], v[84:87]
ds_read_b64_tr_b16 v[20:21], v26 offset:32768
s_waitcnt lgkmcnt(5)
v_mfma_f32_16x16x32_bf16 v[96:99], v[4:7], v[152:155], v[96:99]
ds_read_b64_tr_b16 v[22:23], v26 offset:33280
v_mfma_f32_16x16x32_bf16 v[104:107], v[4:7], v[156:159], v[104:107]
ds_read_b64_tr_b16 v[4:5], v27 offset:32768
v_mfma_f32_16x16x32_bf16 v[108:111], v[8:11], v[156:159], v[108:111]
ds_read_b64_tr_b16 v[6:7], v27 offset:33280
s_mul_i32 s94, s94, 0x27d4eb2f
v_mfma_f32_16x16x32_bf16 v[100:103], v[8:11], v[152:155], v[100:103]
ds_read_b64_tr_b16 v[8:9], v24 offset:36864
s_waitcnt lgkmcnt(5)
v_mfma_f32_16x16x32_bf16 v[48:51], v[12:15], v[160:163], v[48:51]
ds_read_b64_tr_b16 v[10:11], v24 offset:37376
s_lshr_b32 s75, s94, 15
v_mfma_f32_16x16x32_bf16 v[56:59], v[12:15], v[164:167], v[56:59]
ds_read_b64_tr_b16 v[12:13], v25 offset:36864
v_mfma_f32_16x16x32_bf16 v[60:63], v[16:19], v[164:167], v[60:63]
ds_read_b64_tr_b16 v[14:15], v25 offset:37376
s_xor_b32 s94, s94, s75
v_mfma_f32_16x16x32_bf16 v[52:55], v[16:19], v[160:163], v[52:55]
ds_read_b64_tr_b16 v[16:17], v26 offset:36864
s_add_u32 s96, s96, 0x7f4a7c15
s_waitcnt lgkmcnt(5)
v_mfma_f32_16x16x32_bf16 v[64:67], v[20:23], v[160:163], v[64:67]
ds_read_b64_tr_b16 v[18:19], v26 offset:37376
s_addc_u32 s97, s97, 0x9e3779b9
v_mfma_f32_16x16x32_bf16 v[72:75], v[20:23], v[164:167], v[72:75]
ds_read_b64_tr_b16 v[20:21], v27 offset:36864
v_mfma_f32_16x16x32_bf16 v[76:79], v[4:7], v[164:167], v[76:79]
ds_read_b64_tr_b16 v[22:23], v27 offset:37376
s_mul_i32 s96, s96, 0x85ebca6b
v_mfma_f32_16x16x32_bf16 v[68:71], v[4:7], v[160:163], v[68:71]
s_barrier
ds_read_b64_tr_b16 v[4:5], v24 offset:0
ds_read_b128 v[176:179], v28 offset:32768
s_waitcnt lgkmcnt(6)
v_mfma_f32_16x16x32_bf16 v[80:83], v[8:11], v[160:163], v[80:83]
ds_read_b64_tr_b16 v[6:7], v24 offset:512
s_lshr_b32 s75, s96, 13
v_mfma_f32_16x16x32_bf16 v[88:91], v[8:11], v[164:167], v[88:91]
ds_read_b64_tr_b16 v[8:9], v25 offset:0
ds_read_b128 v[180:183], v28 offset:34816
v_mfma_f32_16x16x32_bf16 v[92:95], v[12:15], v[164:167], v[92:95]
ds_read_b64_tr_b16 v[10:11], v25 offset:512
s_xor_b32 s96, s96, s75
v_mfma_f32_16x16x32_bf16 v[84:87], v[12:15], v[160:163], v[84:87]
ds_read_b64_tr_b16 v[12:13], v26 offset:0
ds_read_b128 v[184:187], v29 offset:32768
s_waitcnt lgkmcnt(8)
v_mfma_f32_16x16x32_bf16 v[96:99], v[16:19], v[160:163], v[96:99]
ds_read_b64_tr_b16 v[14:15], v26 offset:512
s_xor_b32 s97, s97, s96
v_mfma_f32_16x16x32_bf16 v[104:107], v[16:19], v[164:167], v[104:107]
ds_read_b64_tr_b16 v[16:17], v27 offset:0
ds_read_b128 v[188:191], v29 offset:34816
v_mfma_f32_16x16x32_bf16 v[108:111], v[20:23], v[164:167], v[108:111]
ds_read_b64_tr_b16 v[18:19], v27 offset:512
s_mul_i32 s97, s97, 0xc2b2ae35
v_mfma_f32_16x16x32_bf16 v[100:103], v[20:23], v[160:163], v[100:103]
ds_read_b64_tr_b16 v[20:21], v24 offset:4096
s_lshr_b32 s75, s97, 16
s_mov_b32 s58, 1430663696
s_bitcmp1_b32 s56, 0
s_cselect_b32 s58, 3720854168, s58
s_lshr_b32 s64, s56, 1
s_lshl_b32 s64, s64, 2
s_waitcnt lgkmcnt(7)
v_mfma_f32_16x16x32_bf16 v[48:51], v[4:7], v[168:171], v[48:51]
ds_read_b64_tr_b16 v[22:23], v24 offset:4608
s_add_u32 s64, s64, 0
s_lshr_b32 s58, s58, s64
s_and_b32 s58, s58, 15
s_lshl_b32 s58, s58, 16
s_mov_b64 exec, 0xffff
buffer_load_dword v45, v43, s[44:47], s58 offen
s_mov_b64 exec, -1
v_mfma_f32_16x16x32_bf16 v[56:59], v[4:7], v[172:175], v[56:59]
ds_read_b64_tr_b16 v[4:5], v25 offset:4096
v_mfma_f32_16x16x32_bf16 v[60:63], v[8:11], v[172:175], v[60:63]
ds_read_b64_tr_b16 v[6:7], v25 offset:4608
s_xor_b32 s97, s97, s75
v_mfma_f32_16x16x32_bf16 v[52:55], v[8:11], v[168:171], v[52:55]
ds_read_b64_tr_b16 v[8:9], v26 offset:4096
s_waitcnt lgkmcnt(5)
v_mfma_f32_16x16x32_bf16 v[64:67], v[12:15], v[168:171], v[64:67]
ds_read_b64_tr_b16 v[10:11], v26 offset:4608
s_xor_b32 s96, s96, s97
v_mfma_f32_16x16x32_bf16 v[72:75], v[12:15], v[172:175], v[72:75]
ds_read_b64_tr_b16 v[12:13], v27 offset:4096
v_mfma_f32_16x16x32_bf16 v[76:79], v[16:19], v[172:175], v[76:79]
ds_read_b64_tr_b16 v[14:15], v27 offset:4608
s_mul_i32 s96, s96, 0x27d4eb2f
v_mfma_f32_16x16x32_bf16 v[68:71], v[16:19], v[168:171], v[68:71]
s_barrier
ds_read_b64_tr_b16 v[16:17], v24 offset:8192
s_waitcnt lgkmcnt(5)
v_mfma_f32_16x16x32_bf16 v[80:83], v[20:23], v[168:171], v[80:83]
ds_read_b64_tr_b16 v[18:19], v24 offset:8704
s_lshr_b32 s75, s96, 15
v_mfma_f32_16x16x32_bf16 v[88:91], v[20:23], v[172:175], v[88:91]
ds_read_b64_tr_b16 v[20:21], v25 offset:8192
v_mfma_f32_16x16x32_bf16 v[92:95], v[4:7], v[172:175], v[92:95]
ds_read_b64_tr_b16 v[22:23], v25 offset:8704
s_xor_b32 s96, s96, s75
v_mfma_f32_16x16x32_bf16 v[84:87], v[4:7], v[168:171], v[84:87]
ds_read_b64_tr_b16 v[4:5], v26 offset:8192
s_waitcnt vmcnt(2)
v_lshlrev_b32 v41, 16, v36
v_and_b32 v36, 0xffff0000, v36
v_add_f32 v64, v64, v41
v_add_f32 v65, v65, v36
v_add_f32 v72, v72, v41
v_add_f32 v73, v73, v36
v_lshlrev_b32 v41, 16, v37
s_waitcnt lgkmcnt(5)
v_mfma_f32_16x16x32_bf16 v[96:99], v[8:11], v[168:171], v[96:99]
ds_read_b64_tr_b16 v[6:7], v26 offset:8704
v_and_b32 v37, 0xffff0000, v37
v_add_f32 v66, v66, v41
v_add_f32 v67, v67, v37
v_add_f32 v74, v74, v41
v_add_f32 v75, v75, v37
s_add_u32 s98, s98, 0x7f4a7c15
s_addc_u32 s99, s99, 0x9e3779b9
v_mfma_f32_16x16x32_bf16 v[104:107], v[8:11], v[172:175], v[104:107]
ds_read_b64_tr_b16 v[8:9], v27 offset:8192
s_waitcnt vmcnt(1)
v_lshlrev_b32 v41, 16, v38
v_and_b32 v38, 0xffff0000, v38
v_add_f32 v68, v68, v41
v_add_f32 v69, v69, v38
v_add_f32 v76, v76, v41
v_add_f32 v77, v77, v38
v_mfma_f32_16x16x32_bf16 v[108:111], v[12:15], v[172:175], v[108:111]
ds_read_b64_tr_b16 v[10:11], v27 offset:8704
v_lshlrev_b32 v41, 16, v39
v_and_b32 v39, 0xffff0000, v39
v_add_f32 v70, v70, v41
v_add_f32 v71, v71, v39
v_add_f32 v78, v78, v41
v_add_f32 v79, v79, v39
s_mul_i32 s98, s98, 0x85ebca6b
v_mfma_f32_16x16x32_bf16 v[100:103], v[12:15], v[168:171], v[100:103]
ds_read_b64_tr_b16 v[12:13], v24 offset:12288
s_waitcnt lgkmcnt(5)
v_mfma_f32_16x16x32_bf16 v[48:51], v[16:19], v[176:179], v[48:51]
ds_read_b64_tr_b16 v[14:15], v24 offset:12800
s_lshr_b32 s75, s98, 13
v_mfma_f32_16x16x32_bf16 v[56:59], v[16:19], v[180:183], v[56:59]
ds_read_b64_tr_b16 v[16:17], v25 offset:12288
v_mfma_f32_16x16x32_bf16 v[60:63], v[20:23], v[180:183], v[60:63]
ds_read_b64_tr_b16 v[18:19], v25 offset:12800
s_xor_b32 s98, s98, s75
v_mfma_f32_16x16x32_bf16 v[52:55], v[20:23], v[176:179], v[52:55]
ds_read_b64_tr_b16 v[20:21], v26 offset:12288
buffer_load_dwordx2 v[36:37], v40, s[52:55], s60 offen offset:128
s_waitcnt lgkmcnt(5)
v_mfma_f32_16x16x32_bf16 v[64:67], v[4:7], v[176:179], v[64:67]
ds_read_b64_tr_b16 v[22:23], v26 offset:12800
buffer_load_dwordx2 v[38:39], v40, s[52:55], s60 offen offset:160
s_xor_b32 s99, s99, s98
v_mfma_f32_16x16x32_bf16 v[72:75], v[4:7], v[180:183], v[72:75]
ds_read_b64_tr_b16 v[4:5], v27 offset:12288
v_mfma_f32_16x16x32_bf16 v[76:79], v[8:11], v[180:183], v[76:79]
ds_read_b64_tr_b16 v[6:7], v27 offset:12800
s_mul_i32 s99, s99, 0xc2b2ae35
v_mfma_f32_16x16x32_bf16 v[68:71], v[8:11], v[176:179], v[68:71]
s_barrier
ds_read_b64_tr_b16 v[8:9], v24 offset:16384
ds_read_b128 v[192:195], v28 offset:0
s_waitcnt lgkmcnt(6)
v_mfma_f32_16x16x32_bf16 v[80:83], v[12:15], v[176:179], v[80:83]
ds_read_b64_tr_b16 v[10:11], v24 offset:16896
s_lshr_b32 s75, s99, 16
v_mfma_f32_16x16x32_bf16 v[88:91], v[12:15], v[180:183], v[88:91]
ds_read_b64_tr_b16 v[12:13], v25 offset:16384
ds_read_b128 v[196:199], v28 offset:2048
v_mfma_f32_16x16x32_bf16 v[92:95], v[16:19], v[180:183], v[92:95]
)ASM"
R"ASM(
ds_read_b64_tr_b16 v[14:15], v25 offset:16896
s_xor_b32 s99, s99, s75
v_mfma_f32_16x16x32_bf16 v[84:87], v[16:19], v[176:179], v[84:87]
ds_read_b64_tr_b16 v[16:17], v26 offset:16384
ds_read_b128 v[200:203], v29 offset:0
s_waitcnt lgkmcnt(8)
v_mfma_f32_16x16x32_bf16 v[96:99], v[20:23], v[176:179], v[96:99]
ds_read_b64_tr_b16 v[18:19], v26 offset:16896
s_xor_b32 s98, s98, s99
v_mfma_f32_16x16x32_bf16 v[104:107], v[20:23], v[180:183], v[104:107]
ds_read_b64_tr_b16 v[20:21], v27 offset:16384
ds_read_b128 v[204:207], v29 offset:2048
v_mfma_f32_16x16x32_bf16 v[108:111], v[4:7], v[180:183], v[108:111]
ds_read_b64_tr_b16 v[22:23], v27 offset:16896
s_mul_i32 s98, s98, 0x27d4eb2f
v_mfma_f32_16x16x32_bf16 v[100:103], v[4:7], v[176:179], v[100:103]
ds_read_b64_tr_b16 v[4:5], v24 offset:20480
s_waitcnt lgkmcnt(7)
v_mfma_f32_16x16x32_bf16 v[48:51], v[8:11], v[184:187], v[48:51]
ds_read_b64_tr_b16 v[6:7], v24 offset:20992
s_lshr_b32 s75, s98, 15
v_mfma_f32_16x16x32_bf16 v[56:59], v[8:11], v[188:191], v[56:59]
ds_read_b64_tr_b16 v[8:9], v25 offset:20480
v_mfma_f32_16x16x32_bf16 v[60:63], v[12:15], v[188:191], v[60:63]
ds_read_b64_tr_b16 v[10:11], v25 offset:20992
s_xor_b32 s98, s98, s75
v_mfma_f32_16x16x32_bf16 v[52:55], v[12:15], v[184:187], v[52:55]
ds_read_b64_tr_b16 v[12:13], v26 offset:20480
s_waitcnt lgkmcnt(5)
v_mfma_f32_16x16x32_bf16 v[64:67], v[16:19], v[184:187], v[64:67]
ds_read_b64_tr_b16 v[14:15], v26 offset:20992
v_mov_b32 v2, s92
v_mfma_f32_16x16x32_bf16 v[72:75], v[16:19], v[188:191], v[72:75]
ds_read_b64_tr_b16 v[16:17], v27 offset:20480
v_mfma_f32_16x16x32_bf16 v[76:79], v[20:23], v[188:191], v[76:79]
ds_read_b64_tr_b16 v[18:19], v27 offset:20992
v_mov_b32 v3, s93
v_mfma_f32_16x16x32_bf16 v[68:71], v[20:23], v[184:187], v[68:71]
s_barrier
ds_read_b64_tr_b16 v[20:21], v24 offset:24576
s_lshr_b32 s79, s56, 1
s_mov_b32 s58, 1430663696
s_bitcmp1_b32 s56, 0
s_cselect_b32 s58, 3720854168, s58
s_lshr_b32 s64, s56, 1
s_lshl_b32 s64, s64, 2
s_waitcnt lgkmcnt(5)
v_mfma_f32_16x16x32_bf16 v[80:83], v[4:7], v[184:187], v[80:83]
ds_read_b64_tr_b16 v[22:23], v24 offset:25088
s_add_u32 s64, s64, 8
s_lshr_b32 s58, s58, s64
s_and_b32 s58, s58, 15
s_lshl_b32 s58, s58, 16
s_mov_b64 exec, 0xffff
buffer_load_dword v45, v43, s[44:47], s58 offen
s_mov_b64 exec, -1
v_mfma_f32_16x16x32_bf16 v[88:91], v[4:7], v[188:191], v[88:91]
ds_read_b64_tr_b16 v[4:5], v25 offset:24576
v_mfma_f32_16x16x32_bf16 v[92:95], v[8:11], v[188:191], v[92:95]
ds_read_b64_tr_b16 v[6:7], v25 offset:25088
v_mfma_f32_16x16x32_bf16 v[84:87], v[8:11], v[184:187], v[84:87]
ds_read_b64_tr_b16 v[8:9], v26 offset:24576
s_waitcnt lgkmcnt(5)
v_mfma_f32_16x16x32_bf16 v[96:99], v[12:15], v[184:187], v[96:99]
ds_read_b64_tr_b16 v[10:11], v26 offset:25088
s_lshl_b32 s79, s79, 12
v_mfma_f32_16x16x32_bf16 v[104:107], v[12:15], v[188:191], v[104:107]
ds_read_b64_tr_b16 v[12:13], v27 offset:24576
v_mfma_f32_16x16x32_bf16 v[108:111], v[16:19], v[188:191], v[108:111]
ds_read_b64_tr_b16 v[14:15], v27 offset:25088
s_and_b32 s72, s56, 1
v_mfma_f32_16x16x32_bf16 v[100:103], v[16:19], v[184:187], v[100:103]
ds_read_b64_tr_b16 v[16:17], v24 offset:28672
s_waitcnt lgkmcnt(5)
v_mfma_f32_16x16x32_bf16 v[48:51], v[20:23], v[192:195], v[48:51]
ds_read_b64_tr_b16 v[18:19], v24 offset:29184
s_lshl_b32 s72, s72, 6
v_mfma_f32_16x16x32_bf16 v[56:59], v[20:23], v[196:199], v[56:59]
ds_read_b64_tr_b16 v[20:21], v25 offset:28672
v_mfma_f32_16x16x32_bf16 v[60:63], v[4:7], v[196:199], v[60:63]
ds_read_b64_tr_b16 v[22:23], v25 offset:29184
s_add_u32 s79, s79, s72
v_mfma_f32_16x16x32_bf16 v[52:55], v[4:7], v[192:195], v[52:55]
ds_read_b64_tr_b16 v[4:5], v26 offset:28672
s_waitcnt lgkmcnt(5)
v_mfma_f32_16x16x32_bf16 v[64:67], v[8:11], v[192:195], v[64:67]
ds_read_b64_tr_b16 v[6:7], v26 offset:29184
s_add_u32 s79, s79, 90112
v_mfma_f32_16x16x32_bf16 v[72:75], v[8:11], v[196:199], v[72:75]
ds_read_b64_tr_b16 v[8:9], v27 offset:28672
s_add_u32 s68, s68, 0x7f4a7c15
v_mfma_f32_16x16x32_bf16 v[76:79], v[12:15], v[196:199], v[76:79]
ds_read_b64_tr_b16 v[10:11], v27 offset:29184
s_addc_u32 s69, s69, 0x9e3779b9
v_mfma_f32_16x16x32_bf16 v[68:71], v[12:15], v[192:195], v[68:71]
s_barrier
ds_read_b64_tr_b16 v[12:13], v24 offset:32768
ds_read_b128 v[208:211], v28 offset:32768
s_waitcnt lgkmcnt(6)
v_mfma_f32_16x16x32_bf16 v[80:83], v[16:19], v[192:195], v[80:83]
ds_read_b64_tr_b16 v[14:15], v24 offset:33280
s_mul_i32 s68, s68, 0x85ebca6b
v_mfma_f32_16x16x32_bf16 v[88:91], v[16:19], v[196:199], v[88:91]
ds_read_b64_tr_b16 v[16:17], v25 offset:32768
ds_read_b128 v[212:215], v28 offset:34816
v_mfma_f32_16x16x32_bf16 v[92:95], v[20:23], v[196:199], v[92:95]
ds_read_b64_tr_b16 v[18:19], v25 offset:33280
s_lshr_b32 s75, s68, 13
v_mfma_f32_16x16x32_bf16 v[84:87], v[20:23], v[192:195], v[84:87]
ds_read_b64_tr_b16 v[20:21], v26 offset:32768
ds_read_b128 v[216:219], v29 offset:32768
s_waitcnt lgkmcnt(8)
v_mfma_f32_16x16x32_bf16 v[96:99], v[4:7], v[192:195], v[96:99]
ds_read_b64_tr_b16 v[22:23], v26 offset:33280
s_xor_b32 s68, s68, s75
v_mfma_f32_16x16x32_bf16 v[104:107], v[4:7], v[196:199], v[104:107]
ds_read_b64_tr_b16 v[4:5], v27 offset:32768
ds_read_b128 v[220:223], v29 offset:34816
v_mfma_f32_16x16x32_bf16 v[108:111], v[8:11], v[196:199], v[108:111]
ds_read_b64_tr_b16 v[6:7], v27 offset:33280
s_xor_b32 s69, s69, s68
v_mfma_f32_16x16x32_bf16 v[100:103], v[8:11], v[192:195], v[100:103]
ds_read_b64_tr_b16 v[8:9], v24 offset:36864
s_waitcnt vmcnt(2)
v_lshlrev_b32 v41, 16, v36
v_and_b32 v36, 0xffff0000, v36
v_add_f32 v80, v80, v41
v_add_f32 v81, v81, v36
v_add_f32 v88, v88, v41
v_add_f32 v89, v89, v36
s_waitcnt lgkmcnt(7)
v_mfma_f32_16x16x32_bf16 v[48:51], v[12:15], v[200:203], v[48:51]
ds_read_b64_tr_b16 v[10:11], v24 offset:37376
v_lshlrev_b32 v41, 16, v37
v_and_b32 v37, 0xffff0000, v37
v_add_f32 v82, v82, v41
v_add_f32 v83, v83, v37
v_add_f32 v90, v90, v41
v_add_f32 v91, v91, v37
s_mul_i32 s69, s69, 0xc2b2ae35
v_mfma_f32_16x16x32_bf16 v[56:59], v[12:15], v[204:207], v[56:59]
ds_read_b64_tr_b16 v[12:13], v25 offset:36864
s_waitcnt vmcnt(1)
v_lshlrev_b32 v41, 16, v38
v_and_b32 v38, 0xffff0000, v38
v_add_f32 v84, v84, v41
v_add_f32 v85, v85, v38
v_add_f32 v92, v92, v41
v_add_f32 v93, v93, v38
v_mfma_f32_16x16x32_bf16 v[60:63], v[16:19], v[204:207], v[60:63]
ds_read_b64_tr_b16 v[14:15], v25 offset:37376
v_lshlrev_b32 v41, 16, v39
v_and_b32 v39, 0xffff0000, v39
v_add_f32 v86, v86, v41
v_add_f32 v87, v87, v39
v_add_f32 v94, v94, v41
v_add_f32 v95, v95, v39
s_lshr_b32 s75, s69, 16
v_mfma_f32_16x16x32_bf16 v[52:55], v[16:19], v[200:203], v[52:55]
ds_read_b64_tr_b16 v[16:17], v26 offset:36864
s_waitcnt lgkmcnt(5)
v_mfma_f32_16x16x32_bf16 v[64:67], v[20:23], v[200:203], v[64:67]
ds_read_b64_tr_b16 v[18:19], v26 offset:37376
s_xor_b32 s69, s69, s75
v_mfma_f32_16x16x32_bf16 v[72:75], v[20:23], v[204:207], v[72:75]
ds_read_b64_tr_b16 v[20:21], v27 offset:36864
v_mfma_f32_16x16x32_bf16 v[76:79], v[4:7], v[204:207], v[76:79]
ds_read_b64_tr_b16 v[22:23], v27 offset:37376
s_xor_b32 s68, s68, s69
v_mfma_f32_16x16x32_bf16 v[68:71], v[4:7], v[200:203], v[68:71]
s_barrier
ds_read_b64_tr_b16 v[4:5], v24 offset:0
buffer_load_dwordx2 v[36:37], v40, s[52:55], s60 offen offset:192
s_waitcnt lgkmcnt(5)
v_mfma_f32_16x16x32_bf16 v[80:83], v[8:11], v[200:203], v[80:83]
ds_read_b64_tr_b16 v[6:7], v24 offset:512
buffer_load_dwordx2 v[38:39], v40, s[52:55], s60 offen offset:224
s_mul_i32 s68, s68, 0x27d4eb2f
v_mfma_f32_16x16x32_bf16 v[88:91], v[8:11], v[204:207], v[88:91]
ds_read_b64_tr_b16 v[8:9], v25 offset:0
v_mfma_f32_16x16x32_bf16 v[92:95], v[12:15], v[204:207], v[92:95]
ds_read_b64_tr_b16 v[10:11], v25 offset:512
s_lshr_b32 s75, s68, 15
v_mfma_f32_16x16x32_bf16 v[84:87], v[12:15], v[200:203], v[84:87]
ds_read_b64_tr_b16 v[12:13], v26 offset:0
s_waitcnt lgkmcnt(5)
v_mfma_f32_16x16x32_bf16 v[96:99], v[16:19], v[200:203], v[96:99]
ds_read_b64_tr_b16 v[14:15], v26 offset:512
s_xor_b32 s68, s68, s75
v_mfma_f32_16x16x32_bf16 v[104:107], v[16:19], v[204:207], v[104:107]
ds_read_b64_tr_b16 v[16:17], v27 offset:0
s_add_u32 s70, s70, 0x7f4a7c15
v_mfma_f32_16x16x32_bf16 v[108:111], v[20:23], v[204:207], v[108:111]
ds_read_b64_tr_b16 v[18:19], v27 offset:512
s_addc_u32 s71, s71, 0x9e3779b9
v_mfma_f32_16x16x32_bf16 v[100:103], v[20:23], v[200:203], v[100:103]
ds_read_b64_tr_b16 v[20:21], v24 offset:4096
s_mul_i32 s70, s70, 0x85ebca6b
s_mov_b32 s58, 1430663696
s_bitcmp1_b32 s56, 0
)ASM"
R"ASM(
s_cselect_b32 s58, 3720854168, s58
s_lshr_b32 s64, s56, 1
s_lshl_b32 s64, s64, 2
s_waitcnt lgkmcnt(5)
v_mfma_f32_16x16x32_bf16 v[48:51], v[4:7], v[208:211], v[48:51]
ds_read_b64_tr_b16 v[22:23], v24 offset:4608
s_add_u32 s64, s64, 16
s_lshr_b32 s58, s58, s64
s_and_b32 s58, s58, 15
s_lshl_b32 s58, s58, 16
s_mov_b64 exec, 0xffff
buffer_load_dword v45, v43, s[44:47], s58 offen
s_mov_b64 exec, -1
v_mfma_f32_16x16x32_bf16 v[56:59], v[4:7], v[212:215], v[56:59]
ds_read_b64_tr_b16 v[4:5], v25 offset:4096
v_mfma_f32_16x16x32_bf16 v[60:63], v[8:11], v[212:215], v[60:63]
ds_read_b64_tr_b16 v[6:7], v25 offset:4608
s_lshr_b32 s75, s70, 13
v_mfma_f32_16x16x32_bf16 v[52:55], v[8:11], v[208:211], v[52:55]
ds_read_b64_tr_b16 v[8:9], v26 offset:4096
s_waitcnt lgkmcnt(5)
v_mfma_f32_16x16x32_bf16 v[64:67], v[12:15], v[208:211], v[64:67]
ds_read_b64_tr_b16 v[10:11], v26 offset:4608
s_xor_b32 s70, s70, s75
v_mfma_f32_16x16x32_bf16 v[72:75], v[12:15], v[212:215], v[72:75]
ds_read_b64_tr_b16 v[12:13], v27 offset:4096
v_mfma_f32_16x16x32_bf16 v[76:79], v[16:19], v[212:215], v[76:79]
ds_read_b64_tr_b16 v[14:15], v27 offset:4608
s_xor_b32 s71, s71, s70
v_mfma_f32_16x16x32_bf16 v[68:71], v[16:19], v[208:211], v[68:71]
s_barrier
ds_read_b64_tr_b16 v[16:17], v24 offset:8192
ds_read_b128 v[224:227], v28 offset:0
s_waitcnt lgkmcnt(6)
v_mfma_f32_16x16x32_bf16 v[80:83], v[20:23], v[208:211], v[80:83]
ds_read_b64_tr_b16 v[18:19], v24 offset:8704
s_mul_i32 s71, s71, 0xc2b2ae35
v_mfma_f32_16x16x32_bf16 v[88:91], v[20:23], v[212:215], v[88:91]
ds_read_b64_tr_b16 v[20:21], v25 offset:8192
ds_read_b128 v[228:231], v28 offset:2048
v_mfma_f32_16x16x32_bf16 v[92:95], v[4:7], v[212:215], v[92:95]
ds_read_b64_tr_b16 v[22:23], v25 offset:8704
s_lshr_b32 s75, s71, 16
v_mfma_f32_16x16x32_bf16 v[84:87], v[4:7], v[208:211], v[84:87]
ds_read_b64_tr_b16 v[4:5], v26 offset:8192
ds_read_b128 v[232:235], v29 offset:0
s_waitcnt lgkmcnt(8)
v_mfma_f32_16x16x32_bf16 v[96:99], v[8:11], v[208:211], v[96:99]
ds_read_b64_tr_b16 v[6:7], v26 offset:8704
s_xor_b32 s71, s71, s75
v_mfma_f32_16x16x32_bf16 v[104:107], v[8:11], v[212:215], v[104:107]
ds_read_b64_tr_b16 v[8:9], v27 offset:8192
ds_read_b128 v[236:239], v29 offset:2048
v_mfma_f32_16x16x32_bf16 v[108:111], v[12:15], v[212:215], v[108:111]
ds_read_b64_tr_b16 v[10:11], v27 offset:8704
s_xor_b32 s70, s70, s71
v_mfma_f32_16x16x32_bf16 v[100:103], v[12:15], v[208:211], v[100:103]
ds_read_b64_tr_b16 v[12:13], v24 offset:12288
s_waitcnt lgkmcnt(7)
v_mfma_f32_16x16x32_bf16 v[48:51], v[16:19], v[216:219], v[48:51]
ds_read_b64_tr_b16 v[14:15], v24 offset:12800
s_mul_i32 s70, s70, 0x27d4eb2f
v_mfma_f32_16x16x32_bf16 v[56:59], v[16:19], v[220:223], v[56:59]
ds_read_b64_tr_b16 v[16:17], v25 offset:12288
v_mfma_f32_16x16x32_bf16 v[60:63], v[20:23], v[220:223], v[60:63]
ds_read_b64_tr_b16 v[18:19], v25 offset:12800
s_lshr_b32 s75, s70, 15
v_mfma_f32_16x16x32_bf16 v[52:55], v[20:23], v[216:219], v[52:55]
ds_read_b64_tr_b16 v[20:21], v26 offset:12288
s_waitcnt lgkmcnt(5)
v_mfma_f32_16x16x32_bf16 v[64:67], v[4:7], v[216:219], v[64:67]
ds_read_b64_tr_b16 v[22:23], v26 offset:12800
s_xor_b32 s70, s70, s75
v_mfma_f32_16x16x32_bf16 v[72:75], v[4:7], v[220:223], v[72:75]
ds_read_b64_tr_b16 v[4:5], v27 offset:12288
v_mfma_f32_16x16x32_bf16 v[76:79], v[8:11], v[220:223], v[76:79]
ds_read_b64_tr_b16 v[6:7], v27 offset:12800
v_mfma_f32_16x16x32_bf16 v[68:71], v[8:11], v[216:219], v[68:71]
s_barrier
ds_read_b64_tr_b16 v[8:9], v24 offset:16384
s_waitcnt lgkmcnt(5)
v_mfma_f32_16x16x32_bf16 v[80:83], v[12:15], v[216:219], v[80:83]
ds_read_b64_tr_b16 v[10:11], v24 offset:16896
v_mfma_f32_16x16x32_bf16 v[88:91], v[12:15], v[220:223], v[88:91]
ds_read_b64_tr_b16 v[12:13], v25 offset:16384
v_mfma_f32_16x16x32_bf16 v[92:95], v[16:19], v[220:223], v[92:95]
ds_read_b64_tr_b16 v[14:15], v25 offset:16896
v_mfma_f32_16x16x32_bf16 v[84:87], v[16:19], v[216:219], v[84:87]
ds_read_b64_tr_b16 v[16:17], v26 offset:16384
s_waitcnt lgkmcnt(5)
v_mfma_f32_16x16x32_bf16 v[96:99], v[20:23], v[216:219], v[96:99]
ds_read_b64_tr_b16 v[18:19], v26 offset:16896
v_mfma_f32_16x16x32_bf16 v[104:107], v[20:23], v[220:223], v[104:107]
ds_read_b64_tr_b16 v[20:21], v27 offset:16384
v_mfma_f32_16x16x32_bf16 v[108:111], v[4:7], v[220:223], v[108:111]
ds_read_b64_tr_b16 v[22:23], v27 offset:16896
v_mfma_f32_16x16x32_bf16 v[100:103], v[4:7], v[216:219], v[100:103]
ds_read_b64_tr_b16 v[4:5], v24 offset:20480
s_waitcnt lgkmcnt(5)
v_mfma_f32_16x16x32_bf16 v[48:51], v[8:11], v[224:227], v[48:51]
ds_read_b64_tr_b16 v[6:7], v24 offset:20992
v_mfma_f32_16x16x32_bf16 v[56:59], v[8:11], v[228:231], v[56:59]
ds_read_b64_tr_b16 v[8:9], v25 offset:20480
v_mfma_f32_16x16x32_bf16 v[60:63], v[12:15], v[228:231], v[60:63]
ds_read_b64_tr_b16 v[10:11], v25 offset:20992
v_mfma_f32_16x16x32_bf16 v[52:55], v[12:15], v[224:227], v[52:55]
ds_read_b64_tr_b16 v[12:13], v26 offset:20480
s_waitcnt vmcnt(2)
v_lshlrev_b32 v41, 16, v36
v_and_b32 v36, 0xffff0000, v36
v_add_f32 v96, v96, v41
v_add_f32 v97, v97, v36
v_add_f32 v104, v104, v41
v_add_f32 v105, v105, v36
s_waitcnt lgkmcnt(5)
v_mfma_f32_16x16x32_bf16 v[64:67], v[16:19], v[224:227], v[64:67]
ds_read_b64_tr_b16 v[14:15], v26 offset:20992
v_lshlrev_b32 v41, 16, v37
v_and_b32 v37, 0xffff0000, v37
v_add_f32 v98, v98, v41
v_add_f32 v99, v99, v37
v_add_f32 v106, v106, v41
v_add_f32 v107, v107, v37
v_mfma_f32_16x16x32_bf16 v[72:75], v[16:19], v[228:231], v[72:75]
ds_read_b64_tr_b16 v[16:17], v27 offset:20480
s_waitcnt vmcnt(1)
v_lshlrev_b32 v41, 16, v38
v_and_b32 v38, 0xffff0000, v38
v_add_f32 v100, v100, v41
v_add_f32 v101, v101, v38
v_add_f32 v108, v108, v41
v_add_f32 v109, v109, v38
v_mfma_f32_16x16x32_bf16 v[76:79], v[20:23], v[228:231], v[76:79]
ds_read_b64_tr_b16 v[18:19], v27 offset:20992
v_lshlrev_b32 v41, 16, v39
v_and_b32 v39, 0xffff0000, v39
v_add_f32 v102, v102, v41
v_add_f32 v103, v103, v39
v_add_f32 v110, v110, v41
v_add_f32 v111, v111, v39
v_mfma_f32_16x16x32_bf16 v[68:71], v[20:23], v[224:227], v[68:71]
s_barrier
ds_read_b64_tr_b16 v[20:21], v24 offset:24576
ds_read_b128 v[240:243], v28 offset:32768
s_lshl_b32 s75, s56, 14
v_bfe_u32 v1, v0, 3, 3
v_lshlrev_b32 v44, 11, v1
v_bfe_u32 v1, v0, 0, 3
v_lshl_add_u32 v44, v1, 4, v44
v_add_u32 v44, s75, v44
v_bfe_u32 v1, v0, 4, 1
v_lshlrev_b32 v1, 6, v1
v_xor_b32 v44, v1, v44
v_and_b32 v33, 7, v0
v_lshlrev_b32 v33, 10, v33
s_waitcnt lgkmcnt(6)
v_mfma_f32_16x16x32_bf16 v[80:83], v[4:7], v[224:227], v[80:83]
ds_read_b64_tr_b16 v[22:23], v24 offset:25088
s_mov_b32 s58, 1430663696
s_bitcmp1_b32 s56, 0
s_cselect_b32 s58, 3720854168, s58
s_lshr_b32 s64, s56, 1
s_lshl_b32 s64, s64, 2
s_add_u32 s64, s64, 24
s_lshr_b32 s58, s58, s64
s_and_b32 s58, s58, 15
s_lshl_b32 s58, s58, 16
s_mov_b64 exec, 0xffff
buffer_load_dword v45, v43, s[44:47], s58 offen
s_mov_b64 exec, -1
v_mfma_f32_16x16x32_bf16 v[88:91], v[4:7], v[228:231], v[88:91]
ds_read_b64_tr_b16 v[4:5], v25 offset:24576
v_mfma_f32_16x16x32_bf16 v[92:95], v[8:11], v[228:231], v[92:95]
ds_read_b64_tr_b16 v[6:7], v25 offset:25088
ds_read_b128 v[244:247], v28 offset:34816
v_mfma_f32_16x16x32_bf16 v[84:87], v[8:11], v[224:227], v[84:87]
ds_read_b64_tr_b16 v[8:9], v26 offset:24576
s_waitcnt lgkmcnt(7)
v_mfma_f32_16x16x32_bf16 v[96:99], v[12:15], v[224:227], v[96:99]
ds_read_b64_tr_b16 v[10:11], v26 offset:25088
ds_read_b128 v[248:251], v29 offset:32768
v_mfma_f32_16x16x32_bf16 v[104:107], v[12:15], v[228:231], v[104:107]
ds_read_b64_tr_b16 v[12:13], v27 offset:24576
v_mfma_f32_16x16x32_bf16 v[108:111], v[16:19], v[228:231], v[108:111]
ds_read_b64_tr_b16 v[14:15], v27 offset:25088
ds_read_b128 v[252:255], v29 offset:34816
v_mfma_f32_16x16x32_bf16 v[100:103], v[16:19], v[224:227], v[100:103]
ds_read_b64_tr_b16 v[16:17], v24 offset:28672
buffer_load_dwordx2 v[36:37], v40, s[52:55], s91 offen offset:0
s_waitcnt lgkmcnt(8)
v_mfma_f32_16x16x32_bf16 v[48:51], v[20:23], v[232:235], v[48:51]
ds_read_b64_tr_b16 v[18:19], v24 offset:29184
buffer_load_dwordx2 v[38:39], v40, s[52:55], s91 offen offset:32
v_mfma_f32_16x16x32_bf16 v[56:59], v[20:23], v[236:239], v[56:59]
ds_read_b64_tr_b16 v[20:21], v25 offset:28672
v_mfma_f32_16x16x32_bf16 v[60:63], v[4:7], v[236:239], v[60:63]
ds_read_b64_tr_b16 v[22:23], v25 offset:29184
v_mfma_f32_16x16x32_bf16 v[52:55], v[4:7], v[232:235], v[52:55]
ds_read_b64_tr_b16 v[4:5], v26 offset:28672
s_waitcnt lgkmcnt(6)
)ASM"
R"ASM(
v_mfma_f32_16x16x32_bf16 v[64:67], v[8:11], v[232:235], v[64:67]
ds_read_b64_tr_b16 v[6:7], v26 offset:29184
v_mfma_f32_16x16x32_bf16 v[72:75], v[8:11], v[236:239], v[72:75]
ds_read_b64_tr_b16 v[8:9], v27 offset:28672
v_mfma_f32_16x16x32_bf16 v[76:79], v[12:15], v[236:239], v[76:79]
ds_read_b64_tr_b16 v[10:11], v27 offset:29184
v_mfma_f32_16x16x32_bf16 v[68:71], v[12:15], v[232:235], v[68:71]
s_waitcnt lgkmcnt(8)
s_barrier
ds_read_b64_tr_b16 v[12:13], v24 offset:32768
s_waitcnt lgkmcnt(5)
v_mfma_f32_16x16x32_bf16 v[80:83], v[16:19], v[232:235], v[80:83]
ds_read_b64_tr_b16 v[14:15], v24 offset:33280
v_mfma_f32_16x16x32_bf16 v[88:91], v[16:19], v[236:239], v[88:91]
ds_read_b64_tr_b16 v[16:17], v25 offset:32768
v_mfma_f32_16x16x32_bf16 v[92:95], v[20:23], v[236:239], v[92:95]
ds_read_b64_tr_b16 v[18:19], v25 offset:33280
v_mfma_f32_16x16x32_bf16 v[84:87], v[20:23], v[232:235], v[84:87]
ds_read_b64_tr_b16 v[20:21], v26 offset:32768
s_waitcnt lgkmcnt(5)
v_mfma_f32_16x16x32_bf16 v[96:99], v[4:7], v[232:235], v[96:99]
ds_read_b64_tr_b16 v[22:23], v26 offset:33280
v_mfma_f32_16x16x32_bf16 v[104:107], v[4:7], v[236:239], v[104:107]
ds_read_b64_tr_b16 v[4:5], v27 offset:32768
v_mfma_f32_16x16x32_bf16 v[108:111], v[8:11], v[236:239], v[108:111]
ds_read_b64_tr_b16 v[6:7], v27 offset:33280
v_mfma_f32_16x16x32_bf16 v[100:103], v[8:11], v[232:235], v[100:103]
ds_read_b64_tr_b16 v[8:9], v24 offset:36864
s_waitcnt lgkmcnt(5)
v_mfma_f32_16x16x32_bf16 v[48:51], v[12:15], v[240:243], v[48:51]
ds_read_b64_tr_b16 v[10:11], v24 offset:37376
v_mfma_f32_16x16x32_bf16 v[56:59], v[12:15], v[244:247], v[56:59]
ds_read_b64_tr_b16 v[12:13], v25 offset:36864
v_mfma_f32_16x16x32_bf16 v[60:63], v[16:19], v[244:247], v[60:63]
ds_read_b64_tr_b16 v[14:15], v25 offset:37376
v_mfma_f32_16x16x32_bf16 v[52:55], v[16:19], v[240:243], v[52:55]
ds_read_b64_tr_b16 v[16:17], v26 offset:36864
s_waitcnt lgkmcnt(5)
v_mfma_f32_16x16x32_bf16 v[64:67], v[20:23], v[240:243], v[64:67]
ds_read_b64_tr_b16 v[18:19], v26 offset:37376
v_mfma_f32_16x16x32_bf16 v[72:75], v[20:23], v[244:247], v[72:75]
ds_read_b64_tr_b16 v[20:21], v27 offset:36864
v_mfma_f32_16x16x32_bf16 v[76:79], v[4:7], v[244:247], v[76:79]
ds_read_b64_tr_b16 v[22:23], v27 offset:37376
v_mfma_f32_16x16x32_bf16 v[68:71], v[4:7], v[240:243], v[68:71]
s_barrier
ds_read_b64_tr_b16 v[4:5], v24 offset:0
v_bfe_u32 v1, v0, 4, 2
s_waitcnt lgkmcnt(5)
v_mfma_f32_16x16x32_bf16 v[80:83], v[8:11], v[240:243], v[80:83]
ds_read_b64_tr_b16 v[6:7], v24 offset:512
v_lshlrev_b32 v28, 9, v1
v_mfma_f32_16x16x32_bf16 v[88:91], v[8:11], v[244:247], v[88:91]
ds_read_b64_tr_b16 v[8:9], v25 offset:0
v_mfma_f32_16x16x32_bf16 v[92:95], v[12:15], v[244:247], v[92:95]
ds_read_b64_tr_b16 v[10:11], v25 offset:512
v_bfe_u32 v1, v0, 2, 2
v_mfma_f32_16x16x32_bf16 v[84:87], v[12:15], v[240:243], v[84:87]
ds_read_b64_tr_b16 v[12:13], v26 offset:0
s_waitcnt lgkmcnt(5)
v_mfma_f32_16x16x32_bf16 v[96:99], v[16:19], v[240:243], v[96:99]
ds_read_b64_tr_b16 v[14:15], v26 offset:512
v_lshl_add_u32 v28, v1, 6, v28
v_mfma_f32_16x16x32_bf16 v[104:107], v[16:19], v[244:247], v[104:107]
ds_read_b64_tr_b16 v[16:17], v27 offset:0
v_mfma_f32_16x16x32_bf16 v[108:111], v[20:23], v[244:247], v[108:111]
ds_read_b64_tr_b16 v[18:19], v27 offset:512
v_bfe_u32 v1, v0, 4, 1
v_mfma_f32_16x16x32_bf16 v[100:103], v[20:23], v[240:243], v[100:103]
ds_read_b64_tr_b16 v[20:21], v24 offset:4096
v_lshl_add_u32 v28, v1, 5, v28
s_waitcnt lgkmcnt(5)
v_mfma_f32_16x16x32_bf16 v[48:51], v[4:7], v[248:251], v[48:51]
ds_read_b64_tr_b16 v[22:23], v24 offset:4608
v_bfe_u32 v1, v0, 0, 2
v_mfma_f32_16x16x32_bf16 v[56:59], v[4:7], v[252:255], v[56:59]
ds_read_b64_tr_b16 v[4:5], v25 offset:4096
v_mfma_f32_16x16x32_bf16 v[60:63], v[8:11], v[252:255], v[60:63]
ds_read_b64_tr_b16 v[6:7], v25 offset:4608
v_lshl_add_u32 v28, v1, 3, v28
v_mfma_f32_16x16x32_bf16 v[52:55], v[8:11], v[248:251], v[52:55]
ds_read_b64_tr_b16 v[8:9], v26 offset:4096
v_add_u32 v28, 40960, v28
s_waitcnt lgkmcnt(5)
v_mfma_f32_16x16x32_bf16 v[64:67], v[12:15], v[248:251], v[64:67]
ds_read_b64_tr_b16 v[10:11], v26 offset:4608
ds_write_b128 v30, v[48:51] offset:32768
v_mfma_f32_16x16x32_bf16 v[72:75], v[12:15], v[252:255], v[72:75]
ds_read_b64_tr_b16 v[12:13], v27 offset:4096
v_xor_b32 v29, 32, v28
v_mfma_f32_16x16x32_bf16 v[76:79], v[16:19], v[252:255], v[76:79]
ds_read_b64_tr_b16 v[14:15], v27 offset:4608
ds_write_b128 v30, v[52:55] offset:33792
v_mfma_f32_16x16x32_bf16 v[68:71], v[16:19], v[248:251], v[68:71]
s_waitcnt lgkmcnt(0)
s_barrier
ds_read_b64_tr_b16 v[16:17], v28 offset:8192
ds_read_b128 v[48:51], v30 offset:34816
v_mfma_f32_16x16x32_bf16 v[80:83], v[20:23], v[248:251], v[80:83]
ds_read_b64_tr_b16 v[18:19], v28 offset:8448
ds_read_b128 v[52:55], v30 offset:35840
ds_write_b128 v30, v[64:67] offset:49152
v_mfma_f32_16x16x32_bf16 v[88:91], v[20:23], v[252:255], v[88:91]
ds_read_b64_tr_b16 v[20:21], v29 offset:8192
v_mfma_f32_16x16x32_bf16 v[92:95], v[4:7], v[252:255], v[92:95]
ds_read_b64_tr_b16 v[22:23], v29 offset:8448
ds_write_b128 v30, v[68:71] offset:50176
v_mfma_f32_16x16x32_bf16 v[84:87], v[4:7], v[248:251], v[84:87]
ds_read_b64_tr_b16 v[4:5], v28 offset:10240
v_mfma_f32_16x16x32_bf16 v[96:99], v[8:11], v[248:251], v[96:99]
ds_read_b64_tr_b16 v[6:7], v28 offset:10496
v_mfma_f32_16x16x32_bf16 v[104:107], v[8:11], v[252:255], v[104:107]
ds_read_b64_tr_b16 v[8:9], v29 offset:10240
v_mfma_f32_16x16x32_bf16 v[108:111], v[12:15], v[252:255], v[108:111]
ds_read_b64_tr_b16 v[10:11], v29 offset:10496
v_mfma_f32_16x16x32_bf16 v[100:103], v[12:15], v[248:251], v[100:103]
ds_read_b64_tr_b16 v[12:13], v28 offset:12288
s_waitcnt lgkmcnt(11)
v_add_f32 v56, v56, v48
v_add_f32 v57, v57, v49
v_add_f32 v58, v58, v50
s_waitcnt lgkmcnt(6)
v_mfma_f32_16x16x32_bf16 v[112:115], v[16:19], v[128:131], 0
ds_read_b64_tr_b16 v[14:15], v28 offset:12544
v_add_f32 v59, v59, v51
v_add_f32 v60, v60, v52
v_add_f32 v61, v61, v53
v_mfma_f32_16x16x32_bf16 v[120:123], v[16:19], v[132:135], 0
ds_read_b64_tr_b16 v[16:17], v29 offset:12288
v_add_f32 v62, v62, v54
v_add_f32 v63, v63, v55
v_mul_f32 v48, v56, v56
v_mul_f32 v49, v57, v57
v_mul_f32 v50, v58, v58
v_mul_f32 v51, v59, v59
s_waitcnt vmcnt(1)
v_lshlrev_b32 v41, 16, v36
v_and_b32 v36, 0xffff0000, v36
v_add_f32 v112, v112, v41
v_mfma_f32_16x16x32_bf16 v[124:127], v[20:23], v[132:135], 0
ds_read_b64_tr_b16 v[18:19], v29 offset:12544
v_add_f32 v113, v113, v36
v_add_f32 v120, v120, v41
v_add_f32 v121, v121, v36
v_lshlrev_b32 v41, 16, v37
v_and_b32 v37, 0xffff0000, v37
v_add_f32 v114, v114, v41
v_add_f32 v115, v115, v37
v_add_f32 v122, v122, v41
v_add_f32 v123, v123, v37
v_mfma_f32_16x16x32_bf16 v[116:119], v[20:23], v[128:131], 0
ds_read_b64_tr_b16 v[20:21], v28 offset:14336
v_mul_f32 v52, v60, v60
v_mul_f32 v53, v61, v61
v_mul_f32 v54, v62, v62
v_mul_f32 v55, v63, v63
v_fma_f32 v48, v48, v32, s82
s_waitcnt vmcnt(0)
v_lshlrev_b32 v41, 16, v38
v_and_b32 v38, 0xffff0000, v38
v_add_f32 v116, v116, v41
s_waitcnt lgkmcnt(5)
v_mfma_f32_16x16x32_bf16 v[112:115], v[4:7], v[136:139], v[112:115]
ds_read_b64_tr_b16 v[22:23], v28 offset:14592
v_add_f32 v117, v117, v38
v_add_f32 v124, v124, v41
v_add_f32 v125, v125, v38
v_lshlrev_b32 v41, 16, v39
v_and_b32 v39, 0xffff0000, v39
v_add_f32 v118, v118, v41
v_add_f32 v119, v119, v39
v_add_f32 v126, v126, v41
v_add_f32 v127, v127, v39
v_mfma_f32_16x16x32_bf16 v[120:123], v[4:7], v[140:143], v[120:123]
ds_read_b64_tr_b16 v[4:5], v29 offset:14336
v_fma_f32 v49, v49, v32, s82
v_fma_f32 v50, v50, v32, s82
v_fma_f32 v51, v51, v32, s82
v_mfma_f32_16x16x32_bf16 v[124:127], v[8:11], v[140:143], v[124:127]
ds_read_b64_tr_b16 v[6:7], v29 offset:14592
v_fma_f32 v52, v52, v32, s82
v_fma_f32 v53, v53, v32, s82
v_fma_f32 v54, v54, v32, s82
v_mfma_f32_16x16x32_bf16 v[116:119], v[8:11], v[136:139], v[116:119]
s_barrier
ds_read_b64_tr_b16 v[8:9], v28 offset:16384
v_fma_f32 v55, v55, v32, s82
v_mul_f32 v48, v56, v48
v_mul_f32 v49, v57, v49
v_exp_f32 v48, v48
s_waitcnt lgkmcnt(5)
v_mfma_f32_16x16x32_bf16 v[112:115], v[12:15], v[144:147], v[112:115]
ds_read_b64_tr_b16 v[10:11], v28 offset:16640
v_mul_f32 v50, v58, v50
v_exp_f32 v49, v49
ds_read_b128 v[64:67], v30 offset:51200
ds_read_b128 v[68:71], v30 offset:52224
ds_write_b128 v30, v[80:83] offset:32768
v_mfma_f32_16x16x32_bf16 v[120:123], v[12:15], v[148:151], v[120:123]
ds_read_b64_tr_b16 v[12:13], v29 offset:16384
)ASM"
R"ASM(
v_mul_f32 v51, v59, v51
v_exp_f32 v50, v50
v_mul_f32 v52, v60, v52
v_mfma_f32_16x16x32_bf16 v[124:127], v[16:19], v[148:151], v[124:127]
ds_read_b64_tr_b16 v[14:15], v29 offset:16640
v_exp_f32 v51, v51
v_mul_f32 v53, v61, v53
ds_write_b128 v30, v[84:87] offset:33792
v_mfma_f32_16x16x32_bf16 v[116:119], v[16:19], v[144:147], v[116:119]
ds_read_b64_tr_b16 v[16:17], v28 offset:18432
v_exp_f32 v52, v52
v_mul_f32 v54, v62, v54
v_exp_f32 v53, v53
s_waitcnt lgkmcnt(9)
v_mfma_f32_16x16x32_bf16 v[112:115], v[20:23], v[152:155], v[112:115]
ds_read_b64_tr_b16 v[18:19], v28 offset:18688
v_mul_f32 v55, v63, v55
v_exp_f32 v54, v54
v_exp_f32 v55, v55
v_mfma_f32_16x16x32_bf16 v[120:123], v[20:23], v[156:159], v[120:123]
ds_read_b64_tr_b16 v[20:21], v29 offset:18432
v_add_f32 v48, 1.0, v48
v_add_f32 v49, 1.0, v49
v_rcp_f32 v48, v48
v_mfma_f32_16x16x32_bf16 v[124:127], v[4:7], v[156:159], v[124:127]
ds_read_b64_tr_b16 v[22:23], v29 offset:18688
v_add_f32 v50, 1.0, v50
v_rcp_f32 v49, v49
v_add_f32 v51, 1.0, v51
v_mfma_f32_16x16x32_bf16 v[116:119], v[4:7], v[152:155], v[116:119]
ds_read_b64_tr_b16 v[4:5], v28 offset:20480
v_rcp_f32 v50, v50
v_add_f32 v52, 1.0, v52
s_waitcnt lgkmcnt(6)
v_mfma_f32_16x16x32_bf16 v[112:115], v[8:11], v[160:163], v[112:115]
ds_read_b64_tr_b16 v[6:7], v28 offset:20736
v_rcp_f32 v51, v51
v_add_f32 v53, 1.0, v53
v_rcp_f32 v52, v52
v_mfma_f32_16x16x32_bf16 v[120:123], v[8:11], v[164:167], v[120:123]
ds_read_b64_tr_b16 v[8:9], v29 offset:20480
v_add_f32 v54, 1.0, v54
v_rcp_f32 v53, v53
v_add_f32 v55, 1.0, v55
v_mfma_f32_16x16x32_bf16 v[124:127], v[12:15], v[164:167], v[124:127]
ds_read_b64_tr_b16 v[10:11], v29 offset:20736
v_rcp_f32 v54, v54
v_rcp_f32 v55, v55
v_mul_f32 v56, v56, v48
v_mfma_f32_16x16x32_bf16 v[116:119], v[12:15], v[160:163], v[116:119]
ds_read_b64_tr_b16 v[12:13], v28 offset:22528
v_mul_f32 v57, v57, v49
v_mul_f32 v58, v58, v50
v_mul_f32 v59, v59, v51
s_waitcnt lgkmcnt(5)
v_mfma_f32_16x16x32_bf16 v[112:115], v[16:19], v[168:171], v[112:115]
ds_read_b64_tr_b16 v[14:15], v28 offset:22784
v_mul_f32 v60, v60, v52
v_mul_f32 v61, v61, v53
v_mul_f32 v62, v62, v54
v_mfma_f32_16x16x32_bf16 v[120:123], v[16:19], v[172:175], v[120:123]
ds_read_b64_tr_b16 v[16:17], v29 offset:22528
v_mul_f32 v63, v63, v55
v_cvt_pk_bf16_f32 v56, v56, v57
v_cvt_pk_bf16_f32 v57, v58, v59
v_mfma_f32_16x16x32_bf16 v[124:127], v[20:23], v[172:175], v[124:127]
ds_read_b64_tr_b16 v[18:19], v29 offset:22784
v_cvt_pk_bf16_f32 v58, v60, v61
v_cvt_pk_bf16_f32 v59, v62, v63
s_add_u32 s74, s80, 0
buffer_store_dwordx4 v[56:59], v31, s[48:51], s74 offen sc1
v_mfma_f32_16x16x32_bf16 v[116:119], v[20:23], v[168:171], v[116:119]
s_barrier
ds_read_b64_tr_b16 v[20:21], v28 offset:24576
v_add_f32 v72, v72, v64
v_add_f32 v73, v73, v65
v_add_f32 v74, v74, v66
v_add_f32 v75, v75, v67
s_waitcnt lgkmcnt(5)
v_mfma_f32_16x16x32_bf16 v[112:115], v[4:7], v[176:179], v[112:115]
ds_read_b64_tr_b16 v[22:23], v28 offset:24832
v_add_f32 v76, v76, v68
v_add_f32 v77, v77, v69
ds_read_b128 v[80:83], v30 offset:34816
ds_read_b128 v[84:87], v30 offset:35840
ds_write_b128 v30, v[96:99] offset:49152
v_mfma_f32_16x16x32_bf16 v[120:123], v[4:7], v[180:183], v[120:123]
ds_read_b64_tr_b16 v[4:5], v29 offset:24576
v_add_f32 v78, v78, v70
v_add_f32 v79, v79, v71
v_mul_f32 v64, v72, v72
v_mfma_f32_16x16x32_bf16 v[124:127], v[8:11], v[180:183], v[124:127]
ds_read_b64_tr_b16 v[6:7], v29 offset:24832
v_mul_f32 v65, v73, v73
v_mul_f32 v66, v74, v74
v_mul_f32 v67, v75, v75
ds_write_b128 v30, v[100:103] offset:50176
v_mfma_f32_16x16x32_bf16 v[116:119], v[8:11], v[176:179], v[116:119]
ds_read_b64_tr_b16 v[8:9], v28 offset:26624
v_mul_f32 v68, v76, v76
v_mul_f32 v69, v77, v77
s_waitcnt lgkmcnt(9)
v_mfma_f32_16x16x32_bf16 v[112:115], v[12:15], v[184:187], v[112:115]
ds_read_b64_tr_b16 v[10:11], v28 offset:26880
v_mul_f32 v70, v78, v78
v_mul_f32 v71, v79, v79
v_fma_f32 v64, v64, v32, s82
v_mfma_f32_16x16x32_bf16 v[120:123], v[12:15], v[188:191], v[120:123]
ds_read_b64_tr_b16 v[12:13], v29 offset:26624
v_fma_f32 v65, v65, v32, s82
v_fma_f32 v66, v66, v32, s82
v_fma_f32 v67, v67, v32, s82
v_mfma_f32_16x16x32_bf16 v[124:127], v[16:19], v[188:191], v[124:127]
ds_read_b64_tr_b16 v[14:15], v29 offset:26880
v_fma_f32 v68, v68, v32, s82
v_fma_f32 v69, v69, v32, s82
v_fma_f32 v70, v70, v32, s82
v_mfma_f32_16x16x32_bf16 v[116:119], v[16:19], v[184:187], v[116:119]
ds_read_b64_tr_b16 v[16:17], v28 offset:28672
v_fma_f32 v71, v71, v32, s82
v_mul_f32 v64, v72, v64
v_mul_f32 v65, v73, v65
s_waitcnt lgkmcnt(6)
v_mfma_f32_16x16x32_bf16 v[112:115], v[20:23], v[192:195], v[112:115]
ds_read_b64_tr_b16 v[18:19], v28 offset:28928
v_exp_f32 v64, v64
v_mul_f32 v66, v74, v66
v_exp_f32 v65, v65
v_mfma_f32_16x16x32_bf16 v[120:123], v[20:23], v[196:199], v[120:123]
ds_read_b64_tr_b16 v[20:21], v29 offset:28672
v_mul_f32 v67, v75, v67
v_exp_f32 v66, v66
v_mfma_f32_16x16x32_bf16 v[124:127], v[4:7], v[196:199], v[124:127]
ds_read_b64_tr_b16 v[22:23], v29 offset:28928
v_mul_f32 v68, v76, v68
v_exp_f32 v67, v67
v_mul_f32 v69, v77, v69
v_mfma_f32_16x16x32_bf16 v[116:119], v[4:7], v[192:195], v[116:119]
ds_read_b64_tr_b16 v[4:5], v28 offset:30720
v_exp_f32 v68, v68
v_mul_f32 v70, v78, v70
v_exp_f32 v69, v69
s_waitcnt lgkmcnt(5)
v_mfma_f32_16x16x32_bf16 v[112:115], v[8:11], v[200:203], v[112:115]
ds_read_b64_tr_b16 v[6:7], v28 offset:30976
v_mul_f32 v71, v79, v71
v_exp_f32 v70, v70
v_exp_f32 v71, v71
v_mfma_f32_16x16x32_bf16 v[120:123], v[8:11], v[204:207], v[120:123]
ds_read_b64_tr_b16 v[8:9], v29 offset:30720
v_add_f32 v64, 1.0, v64
v_add_f32 v65, 1.0, v65
v_rcp_f32 v64, v64
v_mfma_f32_16x16x32_bf16 v[124:127], v[12:15], v[204:207], v[124:127]
ds_read_b64_tr_b16 v[10:11], v29 offset:30976
v_add_f32 v66, 1.0, v66
v_rcp_f32 v65, v65
v_add_f32 v67, 1.0, v67
v_mfma_f32_16x16x32_bf16 v[116:119], v[12:15], v[200:203], v[116:119]
s_barrier
ds_read_b64_tr_b16 v[12:13], v28 offset:32768
v_rcp_f32 v66, v66
v_add_f32 v68, 1.0, v68
v_rcp_f32 v67, v67
s_waitcnt lgkmcnt(5)
v_mfma_f32_16x16x32_bf16 v[112:115], v[16:19], v[208:211], v[112:115]
ds_read_b64_tr_b16 v[14:15], v28 offset:33024
v_add_f32 v69, 1.0, v69
v_rcp_f32 v68, v68
ds_read_b128 v[96:99], v30 offset:51200
ds_read_b128 v[100:103], v30 offset:52224
v_mfma_f32_16x16x32_bf16 v[120:123], v[16:19], v[212:215], v[120:123]
ds_read_b64_tr_b16 v[16:17], v29 offset:32768
v_add_f32 v70, 1.0, v70
v_rcp_f32 v69, v69
v_add_f32 v71, 1.0, v71
v_mfma_f32_16x16x32_bf16 v[124:127], v[20:23], v[212:215], v[124:127]
ds_read_b64_tr_b16 v[18:19], v29 offset:33024
v_rcp_f32 v70, v70
v_rcp_f32 v71, v71
v_mul_f32 v72, v72, v64
v_mfma_f32_16x16x32_bf16 v[116:119], v[20:23], v[208:211], v[116:119]
ds_read_b64_tr_b16 v[20:21], v28 offset:34816
v_mul_f32 v73, v73, v65
v_mul_f32 v74, v74, v66
v_mul_f32 v75, v75, v67
s_waitcnt lgkmcnt(7)
v_mfma_f32_16x16x32_bf16 v[112:115], v[4:7], v[216:219], v[112:115]
ds_read_b64_tr_b16 v[22:23], v28 offset:35072
v_mul_f32 v76, v76, v68
v_mul_f32 v77, v77, v69
v_mul_f32 v78, v78, v70
v_mfma_f32_16x16x32_bf16 v[120:123], v[4:7], v[220:223], v[120:123]
ds_read_b64_tr_b16 v[4:5], v29 offset:34816
v_mul_f32 v79, v79, v71
v_cvt_pk_bf16_f32 v72, v72, v73
v_cvt_pk_bf16_f32 v73, v74, v75
v_mfma_f32_16x16x32_bf16 v[124:127], v[8:11], v[220:223], v[124:127]
ds_read_b64_tr_b16 v[6:7], v29 offset:35072
v_cvt_pk_bf16_f32 v74, v76, v77
)ASM"
R"ASM(
v_cvt_pk_bf16_f32 v75, v78, v79
s_add_u32 s74, s80, 8192
buffer_store_dwordx4 v[72:75], v31, s[48:51], s74 offen sc1
v_mfma_f32_16x16x32_bf16 v[116:119], v[8:11], v[216:219], v[116:119]
ds_read_b64_tr_b16 v[8:9], v28 offset:36864
v_add_f32 v88, v88, v80
v_add_f32 v89, v89, v81
v_add_f32 v90, v90, v82
s_waitcnt lgkmcnt(5)
v_mfma_f32_16x16x32_bf16 v[112:115], v[12:15], v[224:227], v[112:115]
ds_read_b64_tr_b16 v[10:11], v28 offset:37120
v_add_f32 v91, v91, v83
v_add_f32 v92, v92, v84
v_add_f32 v93, v93, v85
v_mfma_f32_16x16x32_bf16 v[120:123], v[12:15], v[228:231], v[120:123]
ds_read_b64_tr_b16 v[12:13], v29 offset:36864
v_add_f32 v94, v94, v86
v_add_f32 v95, v95, v87
v_mul_f32 v80, v88, v88
v_mfma_f32_16x16x32_bf16 v[124:127], v[16:19], v[228:231], v[124:127]
ds_read_b64_tr_b16 v[14:15], v29 offset:37120
v_mul_f32 v81, v89, v89
v_mul_f32 v82, v90, v90
v_mul_f32 v83, v91, v91
v_mfma_f32_16x16x32_bf16 v[116:119], v[16:19], v[224:227], v[116:119]
ds_read_b64_tr_b16 v[16:17], v28 offset:38912
v_mul_f32 v84, v92, v92
v_mul_f32 v85, v93, v93
s_waitcnt lgkmcnt(5)
v_mfma_f32_16x16x32_bf16 v[112:115], v[20:23], v[232:235], v[112:115]
ds_read_b64_tr_b16 v[18:19], v28 offset:39168
v_mul_f32 v86, v94, v94
v_mul_f32 v87, v95, v95
v_fma_f32 v80, v80, v32, s82
v_mfma_f32_16x16x32_bf16 v[120:123], v[20:23], v[236:239], v[120:123]
ds_read_b64_tr_b16 v[20:21], v29 offset:38912
v_fma_f32 v81, v81, v32, s82
v_fma_f32 v82, v82, v32, s82
v_fma_f32 v83, v83, v32, s82
v_mfma_f32_16x16x32_bf16 v[124:127], v[4:7], v[236:239], v[124:127]
ds_read_b64_tr_b16 v[22:23], v29 offset:39168
v_fma_f32 v84, v84, v32, s82
v_fma_f32 v85, v85, v32, s82
v_fma_f32 v86, v86, v32, s82
v_mfma_f32_16x16x32_bf16 v[116:119], v[4:7], v[232:235], v[116:119]
s_waitcnt vmcnt(1)
s_barrier
s_cmp_lg_u32 s56, 0
s_cbranch_scc1 .Lnf1
s_mov_b64 exec, 0xff
buffer_store_dwordx4 v[2:5], v31, s[48:51], s81 offen offset:128 sc1
s_mov_b64 exec, -1
.Lnf1:
ds_read_b64_tr_b16 v[4:5], v24 offset:0
v_bfe_u32 v1, v0, 2, 4
v_fma_f32 v87, v87, v32, s82
v_mul_f32 v80, v88, v80
v_mul_f32 v81, v89, v81
v_exp_f32 v80, v80
s_waitcnt lgkmcnt(5)
v_mfma_f32_16x16x32_bf16 v[112:115], v[8:11], v[240:243], v[112:115]
ds_read_b64_tr_b16 v[6:7], v24 offset:512
v_mul_f32 v82, v90, v82
v_exp_f32 v81, v81
s_add_u32 m0, s57, 98304
s_mov_b32 s59, 0
buffer_load_dwordx4 v44, s[44:47], s59 offen lds
v_mfma_f32_16x16x32_bf16 v[120:123], v[8:11], v[244:247], v[120:123]
ds_read_b64_tr_b16 v[8:9], v25 offset:0
v_lshlrev_b32 v28, 7, v1
v_mul_f32 v83, v91, v83
v_exp_f32 v82, v82
v_mul_f32 v84, v92, v84
v_mfma_f32_16x16x32_bf16 v[124:127], v[12:15], v[244:247], v[124:127]
ds_read_b64_tr_b16 v[10:11], v25 offset:512
v_exp_f32 v83, v83
v_mul_f32 v85, v93, v85
s_add_u32 m0, s57, 102400
s_mov_b32 s59, 128
buffer_load_dwordx4 v44, s[44:47], s59 offen lds
v_mfma_f32_16x16x32_bf16 v[116:119], v[12:15], v[240:243], v[116:119]
ds_read_b64_tr_b16 v[12:13], v26 offset:0
v_bfe_u32 v1, v0, 3, 1
v_exp_f32 v84, v84
v_mul_f32 v86, v94, v86
s_waitcnt lgkmcnt(5)
v_mfma_f32_16x16x32_bf16 v[112:115], v[16:19], v[248:251], v[112:115]
ds_read_b64_tr_b16 v[14:15], v26 offset:512
v_exp_f32 v85, v85
v_mul_f32 v87, v95, v87
v_exp_f32 v86, v86
v_exp_f32 v87, v87
v_mfma_f32_16x16x32_bf16 v[120:123], v[16:19], v[252:255], v[120:123]
ds_read_b64_tr_b16 v[16:17], v27 offset:0
v_lshl_add_u32 v28, v1, 6, v28
v_add_f32 v80, 1.0, v80
v_add_f32 v81, 1.0, v81
v_rcp_f32 v80, v80
v_add_f32 v82, 1.0, v82
v_mfma_f32_16x16x32_bf16 v[124:127], v[20:23], v[252:255], v[124:127]
ds_read_b64_tr_b16 v[18:19], v27 offset:512
v_rcp_f32 v81, v81
v_add_f32 v83, 1.0, v83
s_add_u32 m0, s57, 106496
s_mov_b32 s59, 917504
buffer_load_dwordx4 v44, s[44:47], s59 offen lds
v_mfma_f32_16x16x32_bf16 v[116:119], v[20:23], v[248:251], v[116:119]
ds_read_b64_tr_b16 v[20:21], v24 offset:4096
v_bfe_u32 v1, v0, 0, 2
v_rcp_f32 v82, v82
v_add_f32 v84, 1.0, v84
v_rcp_f32 v83, v83
v_add_f32 v85, 1.0, v85
s_waitcnt lgkmcnt(5)
v_mfma_f32_16x16x32_bf16 v[48:51], v[4:7], v[128:131], 0
ds_read_b64_tr_b16 v[22:23], v24 offset:4608
v_rcp_f32 v84, v84
ds_write_b128 v30, v[112:115] offset:32768
s_add_u32 m0, s57, 110592
s_mov_b32 s59, 917632
buffer_load_dwordx4 v44, s[44:47], s59 offen lds
v_mfma_f32_16x16x32_bf16 v[56:59], v[4:7], v[132:135], 0
ds_read_b64_tr_b16 v[4:5], v25 offset:4096
v_lshl_add_u32 v28, v1, 4, v28
v_add_f32 v86, 1.0, v86
v_rcp_f32 v85, v85
v_add_f32 v87, 1.0, v87
v_mfma_f32_16x16x32_bf16 v[60:63], v[8:11], v[132:135], 0
ds_read_b64_tr_b16 v[6:7], v25 offset:4608
v_rcp_f32 v86, v86
v_rcp_f32 v87, v87
v_mul_f32 v88, v88, v80
ds_write_b128 v30, v[116:119] offset:33792
v_mfma_f32_16x16x32_bf16 v[52:55], v[8:11], v[128:131], 0
ds_read_b64_tr_b16 v[8:9], v26 offset:4096
v_xor_b32 v28, s79, v28
v_mul_f32 v89, v89, v81
v_mul_f32 v90, v90, v82
s_waitcnt lgkmcnt(7)
v_mfma_f32_16x16x32_bf16 v[64:67], v[12:15], v[128:131], 0
ds_read_b64_tr_b16 v[10:11], v26 offset:4608
v_mul_f32 v91, v91, v83
v_mul_f32 v92, v92, v84
v_mul_f32 v93, v93, v85
v_mul_f32 v94, v94, v86
v_mfma_f32_16x16x32_bf16 v[72:75], v[12:15], v[132:135], 0
ds_read_b64_tr_b16 v[12:13], v27 offset:4096
v_mul_f32 v95, v95, v87
v_cvt_pk_bf16_f32 v88, v88, v89
v_cvt_pk_bf16_f32 v89, v90, v91
v_mfma_f32_16x16x32_bf16 v[76:79], v[16:19], v[132:135], 0
ds_read_b64_tr_b16 v[14:15], v27 offset:4608
v_cvt_pk_bf16_f32 v90, v92, v93
v_cvt_pk_bf16_f32 v91, v94, v95
s_add_u32 s74, s80, 16384
buffer_store_dwordx4 v[88:91], v31, s[48:51], s74 offen sc1
v_mfma_f32_16x16x32_bf16 v[68:71], v[16:19], v[128:131], 0
s_waitcnt lgkmcnt(4)
s_barrier
ds_read_b64_tr_b16 v[16:17], v24 offset:8192
v_add_f32 v104, v104, v96
v_add_f32 v105, v105, v97
v_add_f32 v106, v106, v98
v_add_f32 v107, v107, v99
v_add_f32 v108, v108, v100
v_mfma_f32_16x16x32_bf16 v[48:51], v[20:23], v[136:139], v[48:51]
ds_read_b64_tr_b16 v[18:19], v24 offset:8704
v_add_f32 v109, v109, v101
ds_read_b128 v[112:115], v30 offset:34816
ds_read_b128 v[116:119], v30 offset:35840
s_add_u32 m0, s57, 114688
s_mov_b32 s59, 524288
buffer_load_dwordx4 v44, s[44:47], s59 offen lds
v_mfma_f32_16x16x32_bf16 v[56:59], v[20:23], v[140:143], v[56:59]
ds_read_b64_tr_b16 v[20:21], v25 offset:8192
v_add_f32 v110, v110, v102
v_add_f32 v111, v111, v103
v_mul_f32 v96, v104, v104
v_mul_f32 v97, v105, v105
v_mfma_f32_16x16x32_bf16 v[60:63], v[4:7], v[140:143], v[60:63]
ds_read_b64_tr_b16 v[22:23], v25 offset:8704
v_mul_f32 v98, v106, v106
v_mul_f32 v99, v107, v107
s_add_u32 m0, s57, 118784
s_mov_b32 s59, 524416
buffer_load_dwordx4 v44, s[44:47], s59 offen lds
v_mfma_f32_16x16x32_bf16 v[52:55], v[4:7], v[136:139], v[52:55]
ds_read_b64_tr_b16 v[4:5], v26 offset:8192
v_mul_f32 v100, v108, v108
v_mul_f32 v101, v109, v109
s_waitcnt lgkmcnt(7)
v_mfma_f32_16x16x32_bf16 v[64:67], v[8:11], v[136:139], v[64:67]
ds_read_b64_tr_b16 v[6:7], v26 offset:8704
v_mul_f32 v102, v110, v110
v_mul_f32 v103, v111, v111
v_fma_f32 v96, v96, v32, s82
v_mfma_f32_16x16x32_bf16 v[72:75], v[8:11], v[140:143], v[72:75]
ds_read_b64_tr_b16 v[8:9], v27 offset:8192
v_fma_f32 v97, v97, v32, s82
v_fma_f32 v98, v98, v32, s82
)ASM"
R"ASM(
v_fma_f32 v99, v99, v32, s82
v_fma_f32 v100, v100, v32, s82
v_mfma_f32_16x16x32_bf16 v[76:79], v[12:15], v[140:143], v[76:79]
ds_read_b64_tr_b16 v[10:11], v27 offset:8704
v_fma_f32 v101, v101, v32, s82
v_fma_f32 v102, v102, v32, s82
s_add_u32 m0, s57, 122880
s_mov_b32 s59, 1441792
buffer_load_dwordx4 v44, s[44:47], s59 offen lds
v_mfma_f32_16x16x32_bf16 v[68:71], v[12:15], v[136:139], v[68:71]
ds_read_b64_tr_b16 v[12:13], v24 offset:12288
v_fma_f32 v103, v103, v32, s82
v_mul_f32 v96, v104, v96
v_mul_f32 v97, v105, v97
v_exp_f32 v96, v96
v_mul_f32 v98, v106, v98
s_waitcnt lgkmcnt(5)
v_mfma_f32_16x16x32_bf16 v[48:51], v[16:19], v[144:147], v[48:51]
ds_read_b64_tr_b16 v[14:15], v24 offset:12800
v_exp_f32 v97, v97
s_add_u32 m0, s57, 126976
s_mov_b32 s59, 1441920
buffer_load_dwordx4 v44, s[44:47], s59 offen lds
buffer_load_dwordx2 v[36:37], v40, s[52:55], s65 offen offset:0
buffer_load_dwordx2 v[38:39], v40, s[52:55], s65 offen offset:32
v_mfma_f32_16x16x32_bf16 v[56:59], v[16:19], v[148:151], v[56:59]
ds_read_b64_tr_b16 v[16:17], v25 offset:12288
v_mul_f32 v99, v107, v99
v_exp_f32 v98, v98
v_mfma_f32_16x16x32_bf16 v[60:63], v[20:23], v[148:151], v[60:63]
ds_read_b64_tr_b16 v[18:19], v25 offset:12800
v_mul_f32 v100, v108, v100
v_exp_f32 v99, v99
v_mul_f32 v101, v109, v101
v_mfma_f32_16x16x32_bf16 v[52:55], v[20:23], v[144:147], v[52:55]
ds_read_b64_tr_b16 v[20:21], v26 offset:12288
v_exp_f32 v100, v100
v_mul_f32 v102, v110, v102
v_exp_f32 v101, v101
s_waitcnt lgkmcnt(5)
v_mfma_f32_16x16x32_bf16 v[64:67], v[4:7], v[144:147], v[64:67]
ds_read_b64_tr_b16 v[22:23], v26 offset:12800
v_mul_f32 v103, v111, v103
v_exp_f32 v102, v102
v_exp_f32 v103, v103
v_mfma_f32_16x16x32_bf16 v[72:75], v[4:7], v[148:151], v[72:75]
ds_read_b64_tr_b16 v[4:5], v27 offset:12288
v_add_f32 v96, 1.0, v96
v_add_f32 v97, 1.0, v97
v_rcp_f32 v96, v96
v_mfma_f32_16x16x32_bf16 v[76:79], v[8:11], v[148:151], v[76:79]
ds_read_b64_tr_b16 v[6:7], v27 offset:12800
v_add_f32 v98, 1.0, v98
v_rcp_f32 v97, v97
v_add_f32 v99, 1.0, v99
v_mfma_f32_16x16x32_bf16 v[68:71], v[8:11], v[144:147], v[68:71]
s_barrier
ds_read_b64_tr_b16 v[8:9], v24 offset:16384
v_rcp_f32 v98, v98
v_add_f32 v100, 1.0, v100
v_rcp_f32 v99, v99
v_add_f32 v101, 1.0, v101
s_waitcnt lgkmcnt(5)
v_mfma_f32_16x16x32_bf16 v[48:51], v[12:15], v[152:155], v[48:51]
ds_read_b64_tr_b16 v[10:11], v24 offset:16896
v_rcp_f32 v100, v100
s_add_u32 m0, s57, 81920
s_mov_b32 s59, 65536
buffer_load_dwordx4 v44, s[44:47], s59 offen lds
v_mfma_f32_16x16x32_bf16 v[56:59], v[12:15], v[156:159], v[56:59]
ds_read_b64_tr_b16 v[12:13], v25 offset:16384
v_add_f32 v102, 1.0, v102
v_rcp_f32 v101, v101
v_add_f32 v103, 1.0, v103
v_rcp_f32 v102, v102
v_mfma_f32_16x16x32_bf16 v[60:63], v[16:19], v[156:159], v[60:63]
ds_read_b64_tr_b16 v[14:15], v25 offset:16896
v_rcp_f32 v103, v103
v_mul_f32 v104, v104, v96
s_add_u32 m0, s57, 86016
s_mov_b32 s59, 65664
buffer_load_dwordx4 v44, s[44:47], s59 offen lds
v_mfma_f32_16x16x32_bf16 v[52:55], v[16:19], v[152:155], v[52:55]
ds_read_b64_tr_b16 v[16:17], v26 offset:16384
v_mul_f32 v105, v105, v97
v_mul_f32 v106, v106, v98
v_mul_f32 v107, v107, v99
s_waitcnt lgkmcnt(5)
v_mfma_f32_16x16x32_bf16 v[64:67], v[20:23], v[152:155], v[64:67]
ds_read_b64_tr_b16 v[18:19], v26 offset:16896
v_mul_f32 v108, v108, v100
v_mul_f32 v109, v109, v101
v_mul_f32 v110, v110, v102
v_mfma_f32_16x16x32_bf16 v[72:75], v[20:23], v[156:159], v[72:75]
ds_read_b64_tr_b16 v[20:21], v27 offset:16384
v_mul_f32 v111, v111, v103
v_cvt_pk_bf16_f32 v104, v104, v105
v_cvt_pk_bf16_f32 v105, v106, v107
v_cvt_pk_bf16_f32 v106, v108, v109
v_mfma_f32_16x16x32_bf16 v[76:79], v[4:7], v[156:159], v[76:79]
ds_read_b64_tr_b16 v[22:23], v27 offset:16896
v_cvt_pk_bf16_f32 v107, v110, v111
s_add_u32 m0, s57, 90112
s_mov_b32 s59, 983040
buffer_load_dwordx4 v44, s[44:47], s59 offen lds
v_mfma_f32_16x16x32_bf16 v[68:71], v[4:7], v[152:155], v[68:71]
ds_read_b64_tr_b16 v[4:5], v24 offset:20480
v_add_f32 v120, v120, v112
v_add_f32 v121, v121, v113
v_add_f32 v122, v122, v114
v_add_f32 v123, v123, v115
s_waitcnt lgkmcnt(5)
v_mfma_f32_16x16x32_bf16 v[48:51], v[8:11], v[160:163], v[48:51]
ds_read_b64_tr_b16 v[6:7], v24 offset:20992
v_add_f32 v124, v124, v116
v_add_f32 v125, v125, v117
s_add_u32 m0, s57, 94208
s_mov_b32 s59, 983168
buffer_load_dwordx4 v44, s[44:47], s59 offen lds
v_mfma_f32_16x16x32_bf16 v[56:59], v[8:11], v[164:167], v[56:59]
ds_read_b64_tr_b16 v[8:9], v25 offset:20480
v_add_f32 v126, v126, v118
v_add_f32 v127, v127, v119
v_mul_f32 v112, v120, v120
v_mfma_f32_16x16x32_bf16 v[60:63], v[12:15], v[164:167], v[60:63]
ds_read_b64_tr_b16 v[10:11], v25 offset:20992
v_mul_f32 v113, v121, v121
v_mul_f32 v114, v122, v122
v_mul_f32 v115, v123, v123
v_mfma_f32_16x16x32_bf16 v[52:55], v[12:15], v[160:163], v[52:55]
ds_read_b64_tr_b16 v[12:13], v26 offset:20480
s_add_u32 s74, s80, 24576
buffer_store_dwordx4 v[104:107], v31, s[48:51], s74 offen sc1
v_mul_f32 v116, v124, v124
s_waitcnt lgkmcnt(5)
v_mfma_f32_16x16x32_bf16 v[64:67], v[16:19], v[160:163], v[64:67]
ds_read_b64_tr_b16 v[14:15], v26 offset:20992
v_mul_f32 v117, v125, v125
v_mul_f32 v118, v126, v126
v_mul_f32 v119, v127, v127
v_fma_f32 v112, v112, v32, s82
v_mfma_f32_16x16x32_bf16 v[72:75], v[16:19], v[164:167], v[72:75]
ds_read_b64_tr_b16 v[16:17], v27 offset:20480
v_fma_f32 v113, v113, v32, s82
v_fma_f32 v114, v114, v32, s82
v_fma_f32 v115, v115, v32, s82
v_mfma_f32_16x16x32_bf16 v[76:79], v[20:23], v[164:167], v[76:79]
ds_read_b64_tr_b16 v[18:19], v27 offset:20992
v_fma_f32 v116, v116, v32, s82
v_fma_f32 v117, v117, v32, s82
v_fma_f32 v118, v118, v32, s82
v_mfma_f32_16x16x32_bf16 v[68:71], v[20:23], v[160:163], v[68:71]
s_waitcnt vmcnt(12)
s_waitcnt vmcnt(11)
s_barrier
s_cmp_lg_u32 s56, 0
s_cbranch_scc1 .Lnf3
s_mov_b64 exec, 0xff
buffer_store_dwordx4 v[2:5], v31, s[48:51], s81 offen offset:256 sc1
s_mov_b64 exec, -1
.Lnf3:
ds_read_b64_tr_b16 v[20:21], v24 offset:24576
v_fma_f32 v119, v119, v32, s82
v_mul_f32 v112, v120, v112
v_mul_f32 v113, v121, v113
s_waitcnt lgkmcnt(5)
v_mfma_f32_16x16x32_bf16 v[48:51], v[4:7], v[168:171], v[48:51]
ds_read_b64_tr_b16 v[22:23], v24 offset:25088
v_exp_f32 v112, v112
v_mul_f32 v114, v122, v114
v_exp_f32 v113, v113
v_mfma_f32_16x16x32_bf16 v[56:59], v[4:7], v[172:175], v[56:59]
ds_read_b64_tr_b16 v[4:5], v25 offset:24576
ds_read_b64_tr_b16 v[128:129], v28 offset:16384
ds_read_b64_tr_b16 v[130:131], v28 offset:18432
v_mul_f32 v115, v123, v115
v_mfma_f32_16x16x32_bf16 v[60:63], v[8:11], v[172:175], v[60:63]
ds_read_b64_tr_b16 v[6:7], v25 offset:25088
v_exp_f32 v114, v114
v_mul_f32 v116, v124, v116
v_exp_f32 v115, v115
v_mul_f32 v117, v125, v117
v_mfma_f32_16x16x32_bf16 v[52:55], v[8:11], v[168:171], v[52:55]
ds_read_b64_tr_b16 v[8:9], v26 offset:24576
v_exp_f32 v116, v116
v_mul_f32 v118, v126, v118
v_exp_f32 v117, v117
s_waitcnt lgkmcnt(7)
v_mfma_f32_16x16x32_bf16 v[64:67], v[12:15], v[168:171], v[64:67]
ds_read_b64_tr_b16 v[10:11], v26 offset:25088
v_mul_f32 v119, v127, v119
v_exp_f32 v118, v118
v_exp_f32 v119, v119
v_mfma_f32_16x16x32_bf16 v[72:75], v[12:15], v[172:175], v[72:75]
ds_read_b64_tr_b16 v[12:13], v27 offset:24576
ds_read_b64_tr_b16 v[132:133], v28 offset:16392
ds_read_b64_tr_b16 v[134:135], v28 offset:18440
v_add_f32 v112, 1.0, v112
v_add_f32 v113, 1.0, v113
v_mfma_f32_16x16x32_bf16 v[76:79], v[16:19], v[172:175], v[76:79]
)ASM"
R"ASM(
ds_read_b64_tr_b16 v[14:15], v27 offset:25088
v_rcp_f32 v112, v112
v_add_f32 v114, 1.0, v114
v_rcp_f32 v113, v113
v_add_f32 v115, 1.0, v115
v_mfma_f32_16x16x32_bf16 v[68:71], v[16:19], v[168:171], v[68:71]
ds_read_b64_tr_b16 v[16:17], v24 offset:28672
v_rcp_f32 v114, v114
v_add_f32 v116, 1.0, v116
s_waitcnt lgkmcnt(7)
v_mfma_f32_16x16x32_bf16 v[48:51], v[20:23], v[176:179], v[48:51]
ds_read_b64_tr_b16 v[18:19], v24 offset:29184
v_rcp_f32 v115, v115
v_add_f32 v117, 1.0, v117
v_rcp_f32 v116, v116
v_mfma_f32_16x16x32_bf16 v[56:59], v[20:23], v[180:183], v[56:59]
ds_read_b64_tr_b16 v[20:21], v25 offset:28672
v_add_f32 v118, 1.0, v118
v_rcp_f32 v117, v117
v_add_f32 v119, 1.0, v119
v_mfma_f32_16x16x32_bf16 v[60:63], v[4:7], v[180:183], v[60:63]
ds_read_b64_tr_b16 v[22:23], v25 offset:29184
v_rcp_f32 v118, v118
v_rcp_f32 v119, v119
v_mul_f32 v120, v120, v112
v_mfma_f32_16x16x32_bf16 v[52:55], v[4:7], v[176:179], v[52:55]
ds_read_b64_tr_b16 v[4:5], v26 offset:28672
v_mul_f32 v121, v121, v113
v_mul_f32 v122, v122, v114
v_mul_f32 v123, v123, v115
v_mul_f32 v124, v124, v116
v_mul_f32 v125, v125, v117
v_mul_f32 v126, v126, v118
s_waitcnt vmcnt(6)
v_lshlrev_b32 v41, 16, v36
v_and_b32 v36, 0xffff0000, v36
v_add_f32 v48, v48, v41
s_waitcnt lgkmcnt(5)
v_mfma_f32_16x16x32_bf16 v[64:67], v[8:11], v[176:179], v[64:67]
ds_read_b64_tr_b16 v[6:7], v26 offset:29184
v_add_f32 v49, v49, v36
v_add_f32 v56, v56, v41
v_add_f32 v57, v57, v36
v_lshlrev_b32 v41, 16, v37
v_and_b32 v37, 0xffff0000, v37
v_add_f32 v50, v50, v41
v_add_f32 v51, v51, v37
v_add_f32 v58, v58, v41
v_add_f32 v59, v59, v37
v_mfma_f32_16x16x32_bf16 v[72:75], v[8:11], v[180:183], v[72:75]
ds_read_b64_tr_b16 v[8:9], v27 offset:28672
v_mul_f32 v127, v127, v119
v_cvt_pk_bf16_f32 v120, v120, v121
v_cvt_pk_bf16_f32 v121, v122, v123
v_cvt_pk_bf16_f32 v122, v124, v125
v_cvt_pk_bf16_f32 v123, v126, v127
s_add_u32 s74, s80, 32768
buffer_store_dwordx4 v[120:123], v31, s[48:51], s74 offen sc1
s_waitcnt vmcnt(6)
v_lshlrev_b32 v41, 16, v38
v_and_b32 v38, 0xffff0000, v38
v_mfma_f32_16x16x32_bf16 v[76:79], v[12:15], v[180:183], v[76:79]
ds_read_b64_tr_b16 v[10:11], v27 offset:29184
v_add_f32 v52, v52, v41
v_add_f32 v53, v53, v38
v_add_f32 v60, v60, v41
v_add_f32 v61, v61, v38
v_lshlrev_b32 v41, 16, v39
v_and_b32 v39, 0xffff0000, v39
v_add_f32 v54, v54, v41
v_add_f32 v55, v55, v39
v_add_f32 v62, v62, v41
v_add_f32 v63, v63, v39
v_mfma_f32_16x16x32_bf16 v[68:71], v[12:15], v[176:179], v[68:71]
s_barrier
ds_read_b64_tr_b16 v[12:13], v24 offset:32768
s_add_u32 m0, s57, 98304
s_mov_b32 s59, 589824
buffer_load_dwordx4 v44, s[44:47], s59 offen lds
s_waitcnt lgkmcnt(5)
v_mfma_f32_16x16x32_bf16 v[48:51], v[16:19], v[184:187], v[48:51]
ds_read_b64_tr_b16 v[14:15], v24 offset:33280
s_mov_b64 exec, 1
buffer_load_dwordx2 v[34:35], off, s[48:51], s66 offset:128 sc1
s_mov_b64 exec, -1
v_mfma_f32_16x16x32_bf16 v[56:59], v[16:19], v[188:191], v[56:59]
ds_read_b64_tr_b16 v[16:17], v25 offset:32768
ds_read_b64_tr_b16 v[136:137], v28 offset:32768
ds_read_b64_tr_b16 v[138:139], v28 offset:34816
v_mfma_f32_16x16x32_bf16 v[60:63], v[20:23], v[188:191], v[60:63]
ds_read_b64_tr_b16 v[18:19], v25 offset:33280
s_add_u32 m0, s57, 102400
s_mov_b32 s59, 589952
buffer_load_dwordx4 v44, s[44:47], s59 offen lds
v_mfma_f32_16x16x32_bf16 v[52:55], v[20:23], v[184:187], v[52:55]
ds_read_b64_tr_b16 v[20:21], v26 offset:32768
s_waitcnt lgkmcnt(7)
v_mfma_f32_16x16x32_bf16 v[64:67], v[4:7], v[184:187], v[64:67]
ds_read_b64_tr_b16 v[22:23], v26 offset:33280
v_mfma_f32_16x16x32_bf16 v[72:75], v[4:7], v[188:191], v[72:75]
ds_read_b64_tr_b16 v[4:5], v27 offset:32768
ds_read_b64_tr_b16 v[140:141], v28 offset:32776
ds_read_b64_tr_b16 v[142:143], v28 offset:34824
v_mfma_f32_16x16x32_bf16 v[76:79], v[8:11], v[188:191], v[76:79]
ds_read_b64_tr_b16 v[6:7], v27 offset:33280
s_add_u32 m0, s57, 106496
s_mov_b32 s59, 1507328
buffer_load_dwordx4 v44, s[44:47], s59 offen lds
v_mfma_f32_16x16x32_bf16 v[68:71], v[8:11], v[184:187], v[68:71]
ds_read_b64_tr_b16 v[8:9], v24 offset:36864
s_add_u32 m0, s57, 110592
s_waitcnt lgkmcnt(7)
v_mfma_f32_16x16x32_bf16 v[48:51], v[12:15], v[192:195], v[48:51]
ds_read_b64_tr_b16 v[10:11], v24 offset:37376
s_mov_b32 s59, 1507456
buffer_load_dwordx4 v44, s[44:47], s59 offen lds
v_mfma_f32_16x16x32_bf16 v[56:59], v[12:15], v[196:199], v[56:59]
ds_read_b64_tr_b16 v[12:13], v25 offset:36864
v_mfma_f32_16x16x32_bf16 v[60:63], v[16:19], v[196:199], v[60:63]
ds_read_b64_tr_b16 v[14:15], v25 offset:37376
v_mfma_f32_16x16x32_bf16 v[52:55], v[16:19], v[192:195], v[52:55]
ds_read_b64_tr_b16 v[16:17], v26 offset:36864
s_waitcnt lgkmcnt(5)
v_mfma_f32_16x16x32_bf16 v[64:67], v[20:23], v[192:195], v[64:67]
ds_read_b64_tr_b16 v[18:19], v26 offset:37376
v_mfma_f32_16x16x32_bf16 v[72:75], v[20:23], v[196:199], v[72:75]
ds_read_b64_tr_b16 v[20:21], v27 offset:36864
v_mfma_f32_16x16x32_bf16 v[76:79], v[4:7], v[196:199], v[76:79]
ds_read_b64_tr_b16 v[22:23], v27 offset:37376
v_mfma_f32_16x16x32_bf16 v[68:71], v[4:7], v[192:195], v[68:71]
s_waitcnt vmcnt(7)
s_barrier
ds_read_b64_tr_b16 v[4:5], v24 offset:0
s_add_u32 m0, s57, 114688
s_waitcnt lgkmcnt(5)
v_mfma_f32_16x16x32_bf16 v[48:51], v[8:11], v[200:203], v[48:51]
ds_read_b64_tr_b16 v[6:7], v24 offset:512
s_mov_b32 s59, 131072
buffer_load_dwordx4 v44, s[44:47], s59 offen lds
v_mfma_f32_16x16x32_bf16 v[56:59], v[8:11], v[204:207], v[56:59]
ds_read_b64_tr_b16 v[8:9], v25 offset:0
ds_read_b64_tr_b16 v[144:145], v28 offset:0
ds_read_b64_tr_b16 v[146:147], v28 offset:2048
v_mfma_f32_16x16x32_bf16 v[60:63], v[12:15], v[204:207], v[60:63]
ds_read_b64_tr_b16 v[10:11], v25 offset:512
s_add_u32 m0, s57, 118784
s_mov_b32 s59, 131200
buffer_load_dwordx4 v44, s[44:47], s59 offen lds
v_mfma_f32_16x16x32_bf16 v[52:55], v[12:15], v[200:203], v[52:55]
ds_read_b64_tr_b16 v[12:13], v26 offset:0
buffer_load_dwordx2 v[36:37], v40, s[52:55], s65 offen offset:64
s_waitcnt lgkmcnt(7)
v_mfma_f32_16x16x32_bf16 v[64:67], v[16:19], v[200:203], v[64:67]
ds_read_b64_tr_b16 v[14:15], v26 offset:512
buffer_load_dwordx2 v[38:39], v40, s[52:55], s65 offen offset:96
v_mfma_f32_16x16x32_bf16 v[72:75], v[16:19], v[204:207], v[72:75]
ds_read_b64_tr_b16 v[16:17], v27 offset:0
ds_read_b64_tr_b16 v[148:149], v28 offset:8
ds_read_b64_tr_b16 v[150:151], v28 offset:2056
v_mfma_f32_16x16x32_bf16 v[76:79], v[20:23], v[204:207], v[76:79]
ds_read_b64_tr_b16 v[18:19], v27 offset:512
s_add_u32 m0, s57, 122880
s_mov_b32 s59, 1048576
buffer_load_dwordx4 v44, s[44:47], s59 offen lds
v_mfma_f32_16x16x32_bf16 v[68:71], v[20:23], v[200:203], v[68:71]
ds_read_b64_tr_b16 v[20:21], v24 offset:4096
s_add_u32 m0, s57, 126976
s_waitcnt lgkmcnt(7)
v_mfma_f32_16x16x32_bf16 v[48:51], v[4:7], v[208:211], v[48:51]
ds_read_b64_tr_b16 v[22:23], v24 offset:4608
s_mov_b32 s59, 1048704
buffer_load_dwordx4 v44, s[44:47], s59 offen lds
v_mfma_f32_16x16x32_bf16 v[56:59], v[4:7], v[212:215], v[56:59]
ds_read_b64_tr_b16 v[4:5], v25 offset:4096
v_mfma_f32_16x16x32_bf16 v[60:63], v[8:11], v[212:215], v[60:63]
ds_read_b64_tr_b16 v[6:7], v25 offset:4608
v_mfma_f32_16x16x32_bf16 v[52:55], v[8:11], v[208:211], v[52:55]
ds_read_b64_tr_b16 v[8:9], v26 offset:4096
s_waitcnt lgkmcnt(5)
v_mfma_f32_16x16x32_bf16 v[64:67], v[12:15], v[208:211], v[64:67]
ds_read_b64_tr_b16 v[10:11], v26 offset:4608
v_mfma_f32_16x16x32_bf16 v[72:75], v[12:15], v[212:215], v[72:75]
ds_read_b64_tr_b16 v[12:13], v27 offset:4096
v_mfma_f32_16x16x32_bf16 v[76:79], v[16:19], v[212:215], v[76:79]
ds_read_b64_tr_b16 v[14:15], v27 offset:4608
v_mfma_f32_16x16x32_bf16 v[68:71], v[16:19], v[208:211], v[68:71]
s_waitcnt vmcnt(6)
.Lchk0:
v_cmp_eq_u64 vcc, s[68:69], v[34:35]
s_bitcmp1_b32 vcc_lo, 0
s_cbranch_scc0 .Lretry0
.Lok0:
s_barrier
s_cmp_lg_u32 s56, 0
s_cbranch_scc1 .Lnf5
s_mov_b64 exec, 0xff
buffer_store_dwordx4 v[2:5], v31, s[48:51], s81 offen offset:384 sc1
s_mov_b64 exec, -1
.Lnf5:
ds_read_b64_tr_b16 v[16:17], v24 offset:8192
)ASM"
R"ASM(
s_add_u32 m0, s57, 81920
s_mov_b32 s59, 655360
buffer_load_dwordx4 v44, s[44:47], s59 offen lds
s_waitcnt lgkmcnt(5)
v_mfma_f32_16x16x32_bf16 v[48:51], v[20:23], v[216:219], v[48:51]
ds_read_b64_tr_b16 v[18:19], v24 offset:8704
s_mov_b64 exec, 1
buffer_load_dwordx2 v[34:35], off, s[48:51], s66 offset:256 sc1
s_mov_b64 exec, -1
v_mfma_f32_16x16x32_bf16 v[56:59], v[20:23], v[220:223], v[56:59]
ds_read_b64_tr_b16 v[20:21], v25 offset:8192
ds_read_b64_tr_b16 v[152:153], v28 offset:16384
ds_read_b64_tr_b16 v[154:155], v28 offset:18432
v_mfma_f32_16x16x32_bf16 v[60:63], v[4:7], v[220:223], v[60:63]
ds_read_b64_tr_b16 v[22:23], v25 offset:8704
s_add_u32 m0, s57, 86016
s_mov_b32 s59, 655488
buffer_load_dwordx4 v44, s[44:47], s59 offen lds
v_mfma_f32_16x16x32_bf16 v[52:55], v[4:7], v[216:219], v[52:55]
ds_read_b64_tr_b16 v[4:5], v26 offset:8192
s_waitcnt lgkmcnt(7)
v_mfma_f32_16x16x32_bf16 v[64:67], v[8:11], v[216:219], v[64:67]
ds_read_b64_tr_b16 v[6:7], v26 offset:8704
v_mfma_f32_16x16x32_bf16 v[72:75], v[8:11], v[220:223], v[72:75]
ds_read_b64_tr_b16 v[8:9], v27 offset:8192
ds_read_b64_tr_b16 v[156:157], v28 offset:16392
ds_read_b64_tr_b16 v[158:159], v28 offset:18440
v_mfma_f32_16x16x32_bf16 v[76:79], v[12:15], v[220:223], v[76:79]
ds_read_b64_tr_b16 v[10:11], v27 offset:8704
s_add_u32 m0, s57, 90112
s_mov_b32 s59, 1572864
buffer_load_dwordx4 v44, s[44:47], s59 offen lds
v_mfma_f32_16x16x32_bf16 v[68:71], v[12:15], v[216:219], v[68:71]
ds_read_b64_tr_b16 v[12:13], v24 offset:12288
s_add_u32 m0, s57, 94208
s_waitcnt lgkmcnt(7)
v_mfma_f32_16x16x32_bf16 v[48:51], v[16:19], v[224:227], v[48:51]
ds_read_b64_tr_b16 v[14:15], v24 offset:12800
s_mov_b32 s59, 1572992
buffer_load_dwordx4 v44, s[44:47], s59 offen lds
v_mfma_f32_16x16x32_bf16 v[56:59], v[16:19], v[228:231], v[56:59]
ds_read_b64_tr_b16 v[16:17], v25 offset:12288
v_mfma_f32_16x16x32_bf16 v[60:63], v[20:23], v[228:231], v[60:63]
ds_read_b64_tr_b16 v[18:19], v25 offset:12800
v_mfma_f32_16x16x32_bf16 v[52:55], v[20:23], v[224:227], v[52:55]
ds_read_b64_tr_b16 v[20:21], v26 offset:12288
s_waitcnt lgkmcnt(5)
v_mfma_f32_16x16x32_bf16 v[64:67], v[4:7], v[224:227], v[64:67]
ds_read_b64_tr_b16 v[22:23], v26 offset:12800
v_mfma_f32_16x16x32_bf16 v[72:75], v[4:7], v[228:231], v[72:75]
ds_read_b64_tr_b16 v[4:5], v27 offset:12288
v_mfma_f32_16x16x32_bf16 v[76:79], v[8:11], v[228:231], v[76:79]
ds_read_b64_tr_b16 v[6:7], v27 offset:12800
v_mfma_f32_16x16x32_bf16 v[68:71], v[8:11], v[224:227], v[68:71]
s_waitcnt vmcnt(5)
s_barrier
ds_read_b64_tr_b16 v[8:9], v24 offset:16384
s_add_u32 m0, s57, 98304
s_waitcnt lgkmcnt(5)
v_mfma_f32_16x16x32_bf16 v[48:51], v[12:15], v[232:235], v[48:51]
ds_read_b64_tr_b16 v[10:11], v24 offset:16896
s_mov_b32 s59, 196608
buffer_load_dwordx4 v44, s[44:47], s59 offen lds
v_mfma_f32_16x16x32_bf16 v[56:59], v[12:15], v[236:239], v[56:59]
ds_read_b64_tr_b16 v[12:13], v25 offset:16384
ds_read_b64_tr_b16 v[160:161], v28 offset:32768
ds_read_b64_tr_b16 v[162:163], v28 offset:34816
v_mfma_f32_16x16x32_bf16 v[60:63], v[16:19], v[236:239], v[60:63]
ds_read_b64_tr_b16 v[14:15], v25 offset:16896
s_add_u32 m0, s57, 102400
s_mov_b32 s59, 196736
buffer_load_dwordx4 v44, s[44:47], s59 offen lds
v_mfma_f32_16x16x32_bf16 v[52:55], v[16:19], v[232:235], v[52:55]
ds_read_b64_tr_b16 v[16:17], v26 offset:16384
s_waitcnt lgkmcnt(7)
v_mfma_f32_16x16x32_bf16 v[64:67], v[20:23], v[232:235], v[64:67]
ds_read_b64_tr_b16 v[18:19], v26 offset:16896
v_mfma_f32_16x16x32_bf16 v[72:75], v[20:23], v[236:239], v[72:75]
ds_read_b64_tr_b16 v[20:21], v27 offset:16384
ds_read_b64_tr_b16 v[164:165], v28 offset:32776
ds_read_b64_tr_b16 v[166:167], v28 offset:34824
v_mfma_f32_16x16x32_bf16 v[76:79], v[4:7], v[236:239], v[76:79]
ds_read_b64_tr_b16 v[22:23], v27 offset:16896
s_add_u32 m0, s57, 106496
s_mov_b32 s59, 1114112
buffer_load_dwordx4 v44, s[44:47], s59 offen lds
v_mfma_f32_16x16x32_bf16 v[68:71], v[4:7], v[232:235], v[68:71]
ds_read_b64_tr_b16 v[4:5], v24 offset:20480
s_add_u32 m0, s57, 110592
s_mov_b32 s59, 1114240
buffer_load_dwordx4 v44, s[44:47], s59 offen lds
v_lshlrev_b32 v41, 16, v36
v_and_b32 v36, 0xffff0000, v36
v_add_f32 v64, v64, v41
v_add_f32 v65, v65, v36
s_waitcnt lgkmcnt(7)
v_mfma_f32_16x16x32_bf16 v[48:51], v[8:11], v[240:243], v[48:51]
ds_read_b64_tr_b16 v[6:7], v24 offset:20992
v_add_f32 v72, v72, v41
v_add_f32 v73, v73, v36
v_lshlrev_b32 v41, 16, v37
v_and_b32 v37, 0xffff0000, v37
v_add_f32 v66, v66, v41
v_add_f32 v67, v67, v37
v_add_f32 v74, v74, v41
v_add_f32 v75, v75, v37
v_mfma_f32_16x16x32_bf16 v[56:59], v[8:11], v[244:247], v[56:59]
ds_read_b64_tr_b16 v[8:9], v25 offset:20480
v_lshlrev_b32 v41, 16, v38
v_and_b32 v38, 0xffff0000, v38
v_add_f32 v68, v68, v41
v_add_f32 v69, v69, v38
v_add_f32 v76, v76, v41
v_add_f32 v77, v77, v38
v_mfma_f32_16x16x32_bf16 v[60:63], v[12:15], v[244:247], v[60:63]
ds_read_b64_tr_b16 v[10:11], v25 offset:20992
v_lshlrev_b32 v41, 16, v39
v_and_b32 v39, 0xffff0000, v39
v_add_f32 v70, v70, v41
v_add_f32 v71, v71, v39
v_add_f32 v78, v78, v41
v_add_f32 v79, v79, v39
v_mfma_f32_16x16x32_bf16 v[52:55], v[12:15], v[240:243], v[52:55]
ds_read_b64_tr_b16 v[12:13], v26 offset:20480
s_waitcnt lgkmcnt(5)
v_mfma_f32_16x16x32_bf16 v[64:67], v[16:19], v[240:243], v[64:67]
ds_read_b64_tr_b16 v[14:15], v26 offset:20992
v_mfma_f32_16x16x32_bf16 v[72:75], v[16:19], v[244:247], v[72:75]
ds_read_b64_tr_b16 v[16:17], v27 offset:20480
v_mfma_f32_16x16x32_bf16 v[76:79], v[20:23], v[244:247], v[76:79]
ds_read_b64_tr_b16 v[18:19], v27 offset:20992
v_mfma_f32_16x16x32_bf16 v[68:71], v[20:23], v[240:243], v[68:71]
s_waitcnt vmcnt(4)
.Lchk1:
v_cmp_eq_u64 vcc, s[68:69], v[34:35]
s_bitcmp1_b32 vcc_lo, 0
s_cbranch_scc0 .Lretry1
.Lok1:
s_barrier
v_add_u32 v24, 40960, v31
ds_read_b128 v[20:23], v24 offset:24576
s_add_u32 m0, s57, 114688
s_waitcnt lgkmcnt(5)
v_mfma_f32_16x16x32_bf16 v[48:51], v[4:7], v[248:251], v[48:51]
s_mov_b32 s59, 720896
buffer_load_dwordx4 v44, s[44:47], s59 offen lds
v_mfma_f32_16x16x32_bf16 v[56:59], v[4:7], v[252:255], v[56:59]
ds_read_b128 v[4:7], v24 offset:25600
ds_read_b64_tr_b16 v[168:169], v28 offset:0
ds_read_b64_tr_b16 v[170:171], v28 offset:2048
v_mfma_f32_16x16x32_bf16 v[60:63], v[8:11], v[252:255], v[60:63]
s_add_u32 m0, s57, 118784
s_mov_b32 s59, 721024
buffer_load_dwordx4 v44, s[44:47], s59 offen lds
v_mfma_f32_16x16x32_bf16 v[52:55], v[8:11], v[248:251], v[52:55]
ds_read_b128 v[8:11], v24 offset:26624
s_waitcnt lgkmcnt(5)
v_mfma_f32_16x16x32_bf16 v[64:67], v[12:15], v[248:251], v[64:67]
ds_write_b128 v30, v[48:51] offset:49152
v_mfma_f32_16x16x32_bf16 v[72:75], v[12:15], v[252:255], v[72:75]
ds_read_b128 v[12:15], v24 offset:27648
ds_read_b64_tr_b16 v[172:173], v28 offset:8
ds_read_b64_tr_b16 v[174:175], v28 offset:2056
ds_write_b128 v30, v[52:55] offset:50176
v_mfma_f32_16x16x32_bf16 v[76:79], v[16:19], v[252:255], v[76:79]
s_add_u32 m0, s57, 122880
s_mov_b32 s59, 1638400
buffer_load_dwordx4 v44, s[44:47], s59 offen lds
v_mfma_f32_16x16x32_bf16 v[68:71], v[16:19], v[248:251], v[68:71]
ds_read_b128 v[16:19], v24 offset:28672
ds_write_b128 v30, v[64:67] offset:32768
s_add_u32 m0, s57, 126976
s_waitcnt lgkmcnt(10)
v_mfma_f32_16x16x32_bf16 v[80:83], v[128:131], v[20:23], 0
s_mov_b32 s59, 1638528
buffer_load_dwordx4 v44, s[44:47], s59 offen lds
v_mfma_f32_16x16x32_bf16 v[84:87], v[132:135], v[20:23], 0
ds_read_b128 v[20:23], v24 offset:29696
v_mfma_f32_16x16x32_bf16 v[92:95], v[132:135], v[4:7], 0
ds_write_b128 v30, v[68:71] offset:33792
v_mfma_f32_16x16x32_bf16 v[88:91], v[128:131], v[4:7], 0
ds_read_b128 v[4:7], v24 offset:30720
s_waitcnt lgkmcnt(8)
v_mfma_f32_16x16x32_bf16 v[96:99], v[128:131], v[8:11], 0
v_mfma_f32_16x16x32_bf16 v[100:103], v[132:135], v[8:11], 0
ds_read_b128 v[8:11], v24 offset:31744
v_mfma_f32_16x16x32_bf16 v[108:111], v[132:135], v[12:15], 0
v_mfma_f32_16x16x32_bf16 v[104:107], v[128:131], v[12:15], 0
s_waitcnt lgkmcnt(7)
s_waitcnt vmcnt(4)
s_waitcnt lgkmcnt(6)
s_waitcnt lgkmcnt(2)
s_barrier
ds_read_b128 v[12:15], v24 offset:32768
ds_read_b128 v[48:51], v30 offset:51200
ds_read_b128 v[52:55], v30 offset:52224
v_mfma_f32_16x16x32_bf16 v[112:115], v[128:131], v[16:19], 0
ds_read_b128 v[64:67], v30 offset:34816
ds_read_b128 v[68:71], v30 offset:35840
v_mfma_f32_16x16x32_bf16 v[116:119], v[132:135], v[16:19], 0
)ASM"
R"ASM(
ds_read_b128 v[16:19], v24 offset:33792
ds_read_b64_tr_b16 v[176:177], v28 offset:16384
v_mfma_f32_16x16x32_bf16 v[124:127], v[132:135], v[20:23], 0
ds_read_b64_tr_b16 v[178:179], v28 offset:18432
v_mfma_f32_16x16x32_bf16 v[120:123], v[128:131], v[20:23], 0
ds_read_b128 v[20:23], v24 offset:34816
s_waitcnt lgkmcnt(9)
v_mfma_f32_16x16x32_bf16 v[240:243], v[128:131], v[4:7], 0
v_mfma_f32_16x16x32_bf16 v[244:247], v[132:135], v[4:7], 0
ds_read_b128 v[4:7], v24 offset:35840
ds_read_b64_tr_b16 v[180:181], v28 offset:16392
v_mfma_f32_16x16x32_bf16 v[252:255], v[132:135], v[8:11], 0
ds_read_b64_tr_b16 v[182:183], v28 offset:18440
v_mfma_f32_16x16x32_bf16 v[248:251], v[128:131], v[8:11], 0
ds_read_b128 v[8:11], v24 offset:36864
s_waitcnt lgkmcnt(11)
v_add_f32 v56, v56, v48
v_add_f32 v57, v57, v49
v_add_f32 v58, v58, v50
s_waitcnt lgkmcnt(7)
v_mfma_f32_16x16x32_bf16 v[80:83], v[136:139], v[12:15], v[80:83]
v_add_f32 v59, v59, v51
v_add_f32 v60, v60, v52
v_add_f32 v61, v61, v53
v_mfma_f32_16x16x32_bf16 v[84:87], v[140:143], v[12:15], v[84:87]
ds_read_b128 v[12:15], v24 offset:37888
v_add_f32 v62, v62, v54
v_add_f32 v63, v63, v55
v_mul_f32 v48, v56, v56
v_mfma_f32_16x16x32_bf16 v[92:95], v[140:143], v[16:19], v[92:95]
v_mul_f32 v49, v57, v57
v_mul_f32 v50, v58, v58
v_mul_f32 v51, v59, v59
v_mfma_f32_16x16x32_bf16 v[88:91], v[136:139], v[16:19], v[88:91]
ds_read_b128 v[16:19], v24 offset:38912
v_mul_f32 v52, v60, v60
v_mul_f32 v53, v61, v61
s_waitcnt lgkmcnt(5)
v_mfma_f32_16x16x32_bf16 v[96:99], v[136:139], v[20:23], v[96:99]
v_mul_f32 v54, v62, v62
v_mul_f32 v55, v63, v63
v_fma_f32 v48, v48, v32, s82
v_mfma_f32_16x16x32_bf16 v[100:103], v[140:143], v[20:23], v[100:103]
ds_read_b128 v[20:23], v24 offset:39936
v_fma_f32 v49, v49, v32, s82
v_fma_f32 v50, v50, v32, s82
v_fma_f32 v51, v51, v32, s82
v_mfma_f32_16x16x32_bf16 v[108:111], v[140:143], v[4:7], v[108:111]
v_fma_f32 v52, v52, v32, s82
v_fma_f32 v53, v53, v32, s82
v_fma_f32 v54, v54, v32, s82
v_mfma_f32_16x16x32_bf16 v[104:107], v[136:139], v[4:7], v[104:107]
s_waitcnt lgkmcnt(4)
s_barrier
ds_read_b128 v[4:7], v24 offset:0
v_fma_f32 v55, v55, v32, s82
v_mul_f32 v48, v56, v48
v_mul_f32 v49, v57, v49
v_exp_f32 v48, v48
v_mul_f32 v50, v58, v50
v_exp_f32 v49, v49
s_add_u32 m0, s57, 81920
s_mov_b32 s59, 393216
buffer_load_dwordx4 v44, s[44:47], s59 offen lds
s_waitcnt lgkmcnt(3)
v_mfma_f32_16x16x32_bf16 v[112:115], v[136:139], v[8:11], v[112:115]
s_mov_b64 exec, 1
buffer_load_dwordx2 v[34:35], off, s[48:51], s66 offset:384 sc1
s_mov_b64 exec, -1
v_bfe_u32 v1, v0, 4, 2
v_lshlrev_b32 v40, 4, v1
s_mov_b32 s52, s16
s_and_b32 s53, s17, 0xffff
s_mov_b32 s54, 2048
s_lshl_b32 s75, s56, 6
s_lshl_b32 s73, s78, 8
s_add_u32 s75, s75, s73
buffer_load_dwordx4 v[36:39], v40, s[52:55], s75 offen
v_mfma_f32_16x16x32_bf16 v[116:119], v[140:143], v[8:11], v[116:119]
ds_read_b128 v[8:11], v24 offset:1024
v_mul_f32 v51, v59, v51
v_exp_f32 v50, v50
v_mul_f32 v52, v60, v52
v_exp_f32 v51, v51
v_mfma_f32_16x16x32_bf16 v[124:127], v[140:143], v[12:15], v[124:127]
v_mul_f32 v53, v61, v53
s_add_u32 m0, s57, 86016
s_mov_b32 s59, 393344
buffer_load_dwordx4 v44, s[44:47], s59 offen lds
v_mfma_f32_16x16x32_bf16 v[120:123], v[136:139], v[12:15], v[120:123]
ds_read_b128 v[12:15], v24 offset:2048
v_exp_f32 v52, v52
v_mul_f32 v54, v62, v54
v_exp_f32 v53, v53
s_waitcnt lgkmcnt(3)
v_mfma_f32_16x16x32_bf16 v[240:243], v[136:139], v[16:19], v[240:243]
v_mul_f32 v55, v63, v55
v_exp_f32 v54, v54
v_exp_f32 v55, v55
v_mfma_f32_16x16x32_bf16 v[244:247], v[140:143], v[16:19], v[244:247]
ds_read_b128 v[16:19], v24 offset:3072
v_add_f32 v48, 1.0, v48
v_add_f32 v49, 1.0, v49
v_rcp_f32 v48, v48
v_add_f32 v50, 1.0, v50
v_mfma_f32_16x16x32_bf16 v[252:255], v[140:143], v[20:23], v[252:255]
v_rcp_f32 v49, v49
v_add_f32 v51, 1.0, v51
s_add_u32 m0, s57, 90112
s_mov_b32 s59, 1310720
buffer_load_dwordx4 v44, s[44:47], s59 offen lds
v_mfma_f32_16x16x32_bf16 v[248:251], v[136:139], v[20:23], v[248:251]
ds_read_b128 v[20:23], v24 offset:4096
v_rcp_f32 v50, v50
v_add_f32 v52, 1.0, v52
v_rcp_f32 v51, v51
v_add_f32 v53, 1.0, v53
s_waitcnt lgkmcnt(3)
v_mfma_f32_16x16x32_bf16 v[80:83], v[144:147], v[4:7], v[80:83]
v_rcp_f32 v52, v52
s_add_u32 m0, s57, 94208
s_mov_b32 s59, 1310848
buffer_load_dwordx4 v44, s[44:47], s59 offen lds
v_mfma_f32_16x16x32_bf16 v[84:87], v[148:151], v[4:7], v[84:87]
ds_read_b128 v[4:7], v24 offset:5120
v_add_f32 v54, 1.0, v54
v_rcp_f32 v53, v53
v_add_f32 v55, 1.0, v55
v_mfma_f32_16x16x32_bf16 v[92:95], v[148:151], v[8:11], v[92:95]
v_rcp_f32 v54, v54
v_rcp_f32 v55, v55
v_mul_f32 v56, v56, v48
v_mfma_f32_16x16x32_bf16 v[88:91], v[144:147], v[8:11], v[88:91]
ds_read_b128 v[8:11], v24 offset:6144
v_mul_f32 v57, v57, v49
v_mul_f32 v58, v58, v50
v_mul_f32 v59, v59, v51
s_waitcnt lgkmcnt(3)
v_mfma_f32_16x16x32_bf16 v[96:99], v[144:147], v[12:15], v[96:99]
v_mul_f32 v60, v60, v52
v_mul_f32 v61, v61, v53
v_mul_f32 v62, v62, v54
v_mfma_f32_16x16x32_bf16 v[100:103], v[148:151], v[12:15], v[100:103]
ds_read_b128 v[12:15], v24 offset:7168
v_mul_f32 v63, v63, v55
v_cvt_pk_bf16_f32 v56, v56, v57
v_cvt_pk_bf16_f32 v57, v58, v59
v_mfma_f32_16x16x32_bf16 v[108:111], v[148:151], v[16:19], v[108:111]
v_cvt_pk_bf16_f32 v58, v60, v61
v_cvt_pk_bf16_f32 v59, v62, v63
s_add_u32 s74, s80, 40960
buffer_store_dwordx4 v[56:59], v31, s[48:51], s74 offen sc1
v_mfma_f32_16x16x32_bf16 v[104:107], v[144:147], v[16:19], v[104:107]
s_waitcnt vmcnt(7)
s_barrier
ds_read_b128 v[16:19], v24 offset:8192
v_add_f32 v72, v72, v64
v_add_f32 v73, v73, v65
v_add_f32 v74, v74, v66
v_add_f32 v75, v75, v67
s_waitcnt lgkmcnt(3)
v_mfma_f32_16x16x32_bf16 v[112:115], v[144:147], v[20:23], v[112:115]
v_add_f32 v76, v76, v68
v_add_f32 v77, v77, v69
s_add_u32 m0, s57, 98304
s_mov_b32 s59, 458752
buffer_load_dwordx4 v44, s[44:47], s59 offen lds
v_mfma_f32_16x16x32_bf16 v[116:119], v[148:151], v[20:23], v[116:119]
ds_read_b128 v[20:23], v24 offset:9216
ds_read_b64_tr_b16 v[184:185], v28 offset:32768
ds_read_b64_tr_b16 v[186:187], v28 offset:34816
v_add_f32 v78, v78, v70
v_add_f32 v79, v79, v71
v_mul_f32 v64, v72, v72
v_mfma_f32_16x16x32_bf16 v[124:127], v[148:151], v[4:7], v[124:127]
v_mul_f32 v65, v73, v73
v_mul_f32 v66, v74, v74
v_mul_f32 v67, v75, v75
s_add_u32 m0, s57, 102400
s_mov_b32 s59, 458880
buffer_load_dwordx4 v44, s[44:47], s59 offen lds
v_mfma_f32_16x16x32_bf16 v[120:123], v[144:147], v[4:7], v[120:123]
ds_read_b128 v[4:7], v24 offset:10240
v_mul_f32 v68, v76, v76
v_mul_f32 v69, v77, v77
s_waitcnt lgkmcnt(5)
v_mfma_f32_16x16x32_bf16 v[240:243], v[144:147], v[8:11], v[240:243]
v_mul_f32 v70, v78, v78
v_mul_f32 v71, v79, v79
v_fma_f32 v64, v64, v32, s82
v_mfma_f32_16x16x32_bf16 v[244:247], v[148:151], v[8:11], v[244:247]
ds_read_b128 v[8:11], v24 offset:11264
ds_read_b64_tr_b16 v[188:189], v28 offset:32776
ds_read_b64_tr_b16 v[190:191], v28 offset:34824
v_fma_f32 v65, v65, v32, s82
v_fma_f32 v66, v66, v32, s82
v_fma_f32 v67, v67, v32, s82
v_mfma_f32_16x16x32_bf16 v[252:255], v[148:151], v[12:15], v[252:255]
v_fma_f32 v68, v68, v32, s82
v_fma_f32 v69, v69, v32, s82
)ASM"
R"ASM(
v_fma_f32 v70, v70, v32, s82
s_add_u32 m0, s57, 106496
s_mov_b32 s59, 1376256
buffer_load_dwordx4 v44, s[44:47], s59 offen lds
v_mfma_f32_16x16x32_bf16 v[248:251], v[144:147], v[12:15], v[248:251]
ds_read_b128 v[12:15], v24 offset:12288
v_fma_f32 v71, v71, v32, s82
v_mul_f32 v64, v72, v64
v_mul_f32 v65, v73, v65
v_exp_f32 v64, v64
s_waitcnt lgkmcnt(7)
v_mfma_f32_16x16x32_bf16 v[80:83], v[152:155], v[16:19], v[80:83]
v_mul_f32 v66, v74, v66
v_exp_f32 v65, v65
s_add_u32 m0, s57, 110592
s_mov_b32 s59, 1376384
buffer_load_dwordx4 v44, s[44:47], s59 offen lds
v_mfma_f32_16x16x32_bf16 v[84:87], v[156:159], v[16:19], v[84:87]
ds_read_b128 v[16:19], v24 offset:13312
v_mul_f32 v67, v75, v67
v_exp_f32 v66, v66
v_mfma_f32_16x16x32_bf16 v[92:95], v[156:159], v[20:23], v[92:95]
v_mul_f32 v68, v76, v68
v_exp_f32 v67, v67
v_mul_f32 v69, v77, v69
v_mfma_f32_16x16x32_bf16 v[88:91], v[152:155], v[20:23], v[88:91]
ds_read_b128 v[20:23], v24 offset:14336
v_exp_f32 v68, v68
v_mul_f32 v70, v78, v70
v_exp_f32 v69, v69
s_waitcnt lgkmcnt(5)
v_mfma_f32_16x16x32_bf16 v[96:99], v[152:155], v[4:7], v[96:99]
v_mul_f32 v71, v79, v71
v_exp_f32 v70, v70
v_exp_f32 v71, v71
v_mfma_f32_16x16x32_bf16 v[100:103], v[156:159], v[4:7], v[100:103]
ds_read_b128 v[4:7], v24 offset:15360
v_add_f32 v64, 1.0, v64
v_add_f32 v65, 1.0, v65
v_rcp_f32 v64, v64
v_mfma_f32_16x16x32_bf16 v[108:111], v[156:159], v[8:11], v[108:111]
v_add_f32 v66, 1.0, v66
v_rcp_f32 v65, v65
v_add_f32 v67, 1.0, v67
v_mfma_f32_16x16x32_bf16 v[104:107], v[152:155], v[8:11], v[104:107]
s_waitcnt lgkmcnt(4)
s_waitcnt vmcnt(9)
.Lchk2:
v_cmp_eq_u64 vcc, s[68:69], v[34:35]
s_bitcmp1_b32 vcc_lo, 0
s_cbranch_scc0 .Lretry2
.Lok2:
s_barrier
ds_read_b128 v[8:11], v24 offset:16384
v_rcp_f32 v66, v66
v_add_f32 v68, 1.0, v68
s_waitcnt lgkmcnt(3)
v_mfma_f32_16x16x32_bf16 v[112:115], v[152:155], v[12:15], v[112:115]
v_rcp_f32 v67, v67
v_add_f32 v69, 1.0, v69
v_rcp_f32 v68, v68
v_mfma_f32_16x16x32_bf16 v[116:119], v[156:159], v[12:15], v[116:119]
ds_read_b128 v[12:15], v24 offset:17408
v_add_f32 v70, 1.0, v70
v_rcp_f32 v69, v69
v_add_f32 v71, 1.0, v71
v_mfma_f32_16x16x32_bf16 v[124:127], v[156:159], v[16:19], v[124:127]
v_rcp_f32 v70, v70
v_rcp_f32 v71, v71
v_mul_f32 v72, v72, v64
v_mfma_f32_16x16x32_bf16 v[120:123], v[152:155], v[16:19], v[120:123]
ds_read_b128 v[16:19], v24 offset:18432
v_mul_f32 v73, v73, v65
v_mul_f32 v74, v74, v66
v_mul_f32 v75, v75, v67
s_waitcnt lgkmcnt(3)
v_mfma_f32_16x16x32_bf16 v[240:243], v[152:155], v[20:23], v[240:243]
v_mul_f32 v76, v76, v68
v_mul_f32 v77, v77, v69
v_mul_f32 v78, v78, v70
v_mfma_f32_16x16x32_bf16 v[244:247], v[156:159], v[20:23], v[244:247]
ds_read_b128 v[20:23], v24 offset:19456
v_mul_f32 v79, v79, v71
v_cvt_pk_bf16_f32 v72, v72, v73
v_mfma_f32_16x16x32_bf16 v[252:255], v[156:159], v[4:7], v[252:255]
v_cvt_pk_bf16_f32 v73, v74, v75
v_cvt_pk_bf16_f32 v74, v76, v77
v_cvt_pk_bf16_f32 v75, v78, v79
v_mfma_f32_16x16x32_bf16 v[248:251], v[152:155], v[4:7], v[248:251]
ds_read_b128 v[4:7], v24 offset:20480
s_waitcnt lgkmcnt(3)
v_mfma_f32_16x16x32_bf16 v[80:83], v[160:163], v[8:11], v[80:83]
v_mfma_f32_16x16x32_bf16 v[84:87], v[164:167], v[8:11], v[84:87]
ds_read_b128 v[8:11], v24 offset:21504
v_mfma_f32_16x16x32_bf16 v[92:95], v[164:167], v[12:15], v[92:95]
v_mfma_f32_16x16x32_bf16 v[88:91], v[160:163], v[12:15], v[88:91]
ds_read_b128 v[12:15], v24 offset:22528
s_add_u32 s74, s80, 49152
s_waitcnt lgkmcnt(3)
v_mfma_f32_16x16x32_bf16 v[96:99], v[160:163], v[16:19], v[96:99]
buffer_store_dwordx4 v[72:75], v31, s[48:51], s74 offen sc1
v_mfma_f32_16x16x32_bf16 v[100:103], v[164:167], v[16:19], v[100:103]
ds_read_b128 v[16:19], v24 offset:23552
v_mfma_f32_16x16x32_bf16 v[108:111], v[164:167], v[20:23], v[108:111]
v_mfma_f32_16x16x32_bf16 v[104:107], v[160:163], v[20:23], v[104:107]
s_waitcnt vmcnt(6)
s_waitcnt vmcnt(5)
s_barrier
s_cmp_lg_u32 s56, 0
s_cbranch_scc1 .Lnf6
s_mov_b64 exec, 0xff
buffer_store_dwordx4 v[2:5], v31, s[48:51], s81 offen offset:512 sc1
s_mov_b64 exec, -1
.Lnf6:
ds_read_b128 v[20:23], v24 offset:24576
s_add_u32 m0, s57, 114688
s_waitcnt lgkmcnt(3)
v_mfma_f32_16x16x32_bf16 v[112:115], v[160:163], v[4:7], v[112:115]
s_mov_b32 s59, 262144
buffer_load_dwordx4 v44, s[44:47], s59 offen lds
v_mfma_f32_16x16x32_bf16 v[116:119], v[164:167], v[4:7], v[116:119]
ds_read_b128 v[4:7], v24 offset:25600
ds_read_b64_tr_b16 v[192:193], v28 offset:0
ds_read_b64_tr_b16 v[194:195], v28 offset:2048
v_mfma_f32_16x16x32_bf16 v[124:127], v[164:167], v[8:11], v[124:127]
s_add_u32 m0, s57, 118784
s_mov_b32 s59, 262272
buffer_load_dwordx4 v44, s[44:47], s59 offen lds
v_mfma_f32_16x16x32_bf16 v[120:123], v[160:163], v[8:11], v[120:123]
ds_read_b128 v[8:11], v24 offset:26624
s_waitcnt lgkmcnt(5)
v_mfma_f32_16x16x32_bf16 v[240:243], v[160:163], v[12:15], v[240:243]
v_mfma_f32_16x16x32_bf16 v[244:247], v[164:167], v[12:15], v[244:247]
ds_read_b128 v[12:15], v24 offset:27648
ds_read_b64_tr_b16 v[196:197], v28 offset:8
ds_read_b64_tr_b16 v[198:199], v28 offset:2056
v_mfma_f32_16x16x32_bf16 v[252:255], v[164:167], v[16:19], v[252:255]
s_add_u32 m0, s57, 122880
s_mov_b32 s59, 1179648
buffer_load_dwordx4 v44, s[44:47], s59 offen lds
v_mfma_f32_16x16x32_bf16 v[248:251], v[160:163], v[16:19], v[248:251]
ds_read_b128 v[16:19], v24 offset:28672
s_add_u32 m0, s57, 126976
s_waitcnt lgkmcnt(7)
v_mfma_f32_16x16x32_bf16 v[80:83], v[168:171], v[20:23], v[80:83]
s_mov_b32 s59, 1179776
buffer_load_dwordx4 v44, s[44:47], s59 offen lds
v_mfma_f32_16x16x32_bf16 v[84:87], v[172:175], v[20:23], v[84:87]
ds_read_b128 v[20:23], v24 offset:29696
v_mfma_f32_16x16x32_bf16 v[92:95], v[172:175], v[4:7], v[92:95]
v_mfma_f32_16x16x32_bf16 v[88:91], v[168:171], v[4:7], v[88:91]
ds_read_b128 v[4:7], v24 offset:30720
s_waitcnt lgkmcnt(5)
v_mfma_f32_16x16x32_bf16 v[96:99], v[168:171], v[8:11], v[96:99]
v_mfma_f32_16x16x32_bf16 v[100:103], v[172:175], v[8:11], v[100:103]
ds_read_b128 v[8:11], v24 offset:31744
v_mfma_f32_16x16x32_bf16 v[108:111], v[172:175], v[12:15], v[108:111]
v_mfma_f32_16x16x32_bf16 v[104:107], v[168:171], v[12:15], v[104:107]
s_waitcnt lgkmcnt(4)
s_waitcnt vmcnt(5)
s_barrier
ds_read_b128 v[12:15], v24 offset:32768
s_waitcnt lgkmcnt(3)
v_mfma_f32_16x16x32_bf16 v[112:115], v[168:171], v[16:19], v[112:115]
s_mov_b64 exec, 1
buffer_load_dwordx2 v[34:35], off, s[48:51], s67 offset:384 sc1
s_mov_b64 exec, -1
v_mfma_f32_16x16x32_bf16 v[116:119], v[172:175], v[16:19], v[116:119]
ds_read_b128 v[16:19], v24 offset:33792
ds_read_b64_tr_b16 v[200:201], v28 offset:16384
v_mfma_f32_16x16x32_bf16 v[124:127], v[172:175], v[20:23], v[124:127]
ds_read_b64_tr_b16 v[202:203], v28 offset:18432
v_mfma_f32_16x16x32_bf16 v[120:123], v[168:171], v[20:23], v[120:123]
ds_read_b128 v[20:23], v24 offset:34816
s_waitcnt lgkmcnt(5)
v_mfma_f32_16x16x32_bf16 v[240:243], v[168:171], v[4:7], v[240:243]
v_mfma_f32_16x16x32_bf16 v[244:247], v[172:175], v[4:7], v[244:247]
ds_read_b128 v[4:7], v24 offset:35840
ds_read_b64_tr_b16 v[204:205], v28 offset:16392
v_mfma_f32_16x16x32_bf16 v[252:255], v[172:175], v[8:11], v[252:255]
ds_read_b64_tr_b16 v[206:207], v28 offset:18440
v_mfma_f32_16x16x32_bf16 v[248:251], v[168:171], v[8:11], v[248:251]
ds_read_b128 v[8:11], v24 offset:36864
s_waitcnt lgkmcnt(7)
v_mfma_f32_16x16x32_bf16 v[80:83], v[176:179], v[12:15], v[80:83]
v_mfma_f32_16x16x32_bf16 v[84:87], v[180:183], v[12:15], v[84:87]
ds_read_b128 v[12:15], v24 offset:37888
v_mfma_f32_16x16x32_bf16 v[92:95], v[180:183], v[16:19], v[92:95]
v_mfma_f32_16x16x32_bf16 v[88:91], v[176:179], v[16:19], v[88:91]
ds_read_b128 v[16:19], v24 offset:38912
s_waitcnt lgkmcnt(5)
v_mfma_f32_16x16x32_bf16 v[96:99], v[176:179], v[20:23], v[96:99]
v_mfma_f32_16x16x32_bf16 v[100:103], v[180:183], v[20:23], v[100:103]
ds_read_b128 v[20:23], v24 offset:39936
v_mfma_f32_16x16x32_bf16 v[108:111], v[180:183], v[4:7], v[108:111]
v_mfma_f32_16x16x32_bf16 v[104:107], v[176:179], v[4:7], v[104:107]
s_waitcnt lgkmcnt(4)
s_waitcnt vmcnt(5)
s_barrier
s_cmp_lg_u32 s56, 0
)ASM"
R"ASM(
s_cbranch_scc1 .Lnf7
s_mov_b64 exec, 0xff
buffer_store_dwordx4 v[2:5], v31, s[48:51], s81 offen offset:640 sc1
s_mov_b64 exec, -1
.Lnf7:
ds_read_b128 v[4:7], v24 offset:0
s_add_u32 m0, s57, 81920
s_waitcnt lgkmcnt(3)
v_mfma_f32_16x16x32_bf16 v[112:115], v[176:179], v[8:11], v[112:115]
s_mov_b32 s59, 786432
buffer_load_dwordx4 v44, s[44:47], s59 offen lds
v_mfma_f32_16x16x32_bf16 v[116:119], v[180:183], v[8:11], v[116:119]
ds_read_b128 v[8:11], v24 offset:1024
s_add_u32 m0, s57, 86016
v_mfma_f32_16x16x32_bf16 v[124:127], v[180:183], v[12:15], v[124:127]
s_mov_b32 s59, 786560
buffer_load_dwordx4 v44, s[44:47], s59 offen lds
v_mfma_f32_16x16x32_bf16 v[120:123], v[176:179], v[12:15], v[120:123]
ds_read_b128 v[12:15], v24 offset:2048
s_waitcnt lgkmcnt(3)
v_mfma_f32_16x16x32_bf16 v[240:243], v[176:179], v[16:19], v[240:243]
v_mfma_f32_16x16x32_bf16 v[244:247], v[180:183], v[16:19], v[244:247]
ds_read_b128 v[16:19], v24 offset:3072
s_add_u32 m0, s57, 90112
v_mfma_f32_16x16x32_bf16 v[252:255], v[180:183], v[20:23], v[252:255]
s_mov_b32 s59, 1703936
buffer_load_dwordx4 v44, s[44:47], s59 offen lds
v_mfma_f32_16x16x32_bf16 v[248:251], v[176:179], v[20:23], v[248:251]
ds_read_b128 v[20:23], v24 offset:4096
s_add_u32 m0, s57, 94208
s_waitcnt lgkmcnt(3)
v_mfma_f32_16x16x32_bf16 v[80:83], v[184:187], v[4:7], v[80:83]
s_mov_b32 s59, 1704064
buffer_load_dwordx4 v44, s[44:47], s59 offen lds
v_mfma_f32_16x16x32_bf16 v[84:87], v[188:191], v[4:7], v[84:87]
ds_read_b128 v[4:7], v24 offset:5120
v_mfma_f32_16x16x32_bf16 v[92:95], v[188:191], v[8:11], v[92:95]
v_mfma_f32_16x16x32_bf16 v[88:91], v[184:187], v[8:11], v[88:91]
ds_read_b128 v[8:11], v24 offset:6144
s_waitcnt lgkmcnt(3)
v_mfma_f32_16x16x32_bf16 v[96:99], v[184:187], v[12:15], v[96:99]
v_mfma_f32_16x16x32_bf16 v[100:103], v[188:191], v[12:15], v[100:103]
ds_read_b128 v[12:15], v24 offset:7168
v_mfma_f32_16x16x32_bf16 v[108:111], v[188:191], v[16:19], v[108:111]
v_mfma_f32_16x16x32_bf16 v[104:107], v[184:187], v[16:19], v[104:107]
s_waitcnt vmcnt(5)
s_waitcnt vmcnt(4)
.Lchk3:
v_cmp_eq_u64 vcc, s[70:71], v[34:35]
s_bitcmp1_b32 vcc_lo, 0
s_cbranch_scc0 .Lretry3
.Lok3:
s_barrier
ds_read_b128 v[16:19], v24 offset:8192
s_add_u32 m0, s57, 98304
s_mov_b32 s59, 327680
buffer_load_dwordx4 v44, s[44:47], s59 offen lds
s_waitcnt lgkmcnt(3)
v_mfma_f32_16x16x32_bf16 v[112:115], v[184:187], v[20:23], v[112:115]
s_mov_b64 exec, 1
buffer_load_dwordx2 v[34:35], off, s[48:51], s66 offset:512 sc1
s_mov_b64 exec, -1
v_mfma_f32_16x16x32_bf16 v[116:119], v[188:191], v[20:23], v[116:119]
ds_read_b128 v[20:23], v24 offset:9216
ds_read_b64_tr_b16 v[208:209], v28 offset:32768
ds_read_b64_tr_b16 v[210:211], v28 offset:34816
v_mfma_f32_16x16x32_bf16 v[124:127], v[188:191], v[4:7], v[124:127]
s_add_u32 m0, s57, 102400
s_mov_b32 s59, 327808
buffer_load_dwordx4 v44, s[44:47], s59 offen lds
v_mfma_f32_16x16x32_bf16 v[120:123], v[184:187], v[4:7], v[120:123]
ds_read_b128 v[4:7], v24 offset:10240
s_waitcnt lgkmcnt(5)
v_mfma_f32_16x16x32_bf16 v[240:243], v[184:187], v[8:11], v[240:243]
v_mfma_f32_16x16x32_bf16 v[244:247], v[188:191], v[8:11], v[244:247]
ds_read_b128 v[8:11], v24 offset:11264
ds_read_b64_tr_b16 v[212:213], v28 offset:32776
ds_read_b64_tr_b16 v[214:215], v28 offset:34824
v_mfma_f32_16x16x32_bf16 v[252:255], v[188:191], v[12:15], v[252:255]
s_add_u32 m0, s57, 106496
s_mov_b32 s59, 1245184
buffer_load_dwordx4 v44, s[44:47], s59 offen lds
v_mfma_f32_16x16x32_bf16 v[248:251], v[184:187], v[12:15], v[248:251]
ds_read_b128 v[12:15], v24 offset:12288
s_add_u32 m0, s57, 110592
s_waitcnt lgkmcnt(7)
v_mfma_f32_16x16x32_bf16 v[80:83], v[192:195], v[16:19], v[80:83]
s_mov_b32 s59, 1245312
buffer_load_dwordx4 v44, s[44:47], s59 offen lds
v_mfma_f32_16x16x32_bf16 v[84:87], v[196:199], v[16:19], v[84:87]
ds_read_b128 v[16:19], v24 offset:13312
v_mfma_f32_16x16x32_bf16 v[92:95], v[196:199], v[20:23], v[92:95]
v_mfma_f32_16x16x32_bf16 v[88:91], v[192:195], v[20:23], v[88:91]
ds_read_b128 v[20:23], v24 offset:14336
s_waitcnt lgkmcnt(5)
v_mfma_f32_16x16x32_bf16 v[96:99], v[192:195], v[4:7], v[96:99]
v_mfma_f32_16x16x32_bf16 v[100:103], v[196:199], v[4:7], v[100:103]
ds_read_b128 v[4:7], v24 offset:15360
v_mfma_f32_16x16x32_bf16 v[108:111], v[196:199], v[8:11], v[108:111]
v_mfma_f32_16x16x32_bf16 v[104:107], v[192:195], v[8:11], v[104:107]
s_waitcnt lgkmcnt(4)
s_barrier
ds_read_b128 v[8:11], v24 offset:16384
s_waitcnt lgkmcnt(3)
v_mfma_f32_16x16x32_bf16 v[112:115], v[192:195], v[12:15], v[112:115]
v_mfma_f32_16x16x32_bf16 v[116:119], v[196:199], v[12:15], v[116:119]
ds_read_b128 v[12:15], v24 offset:17408
v_mfma_f32_16x16x32_bf16 v[124:127], v[196:199], v[16:19], v[124:127]
v_mfma_f32_16x16x32_bf16 v[120:123], v[192:195], v[16:19], v[120:123]
ds_read_b128 v[16:19], v24 offset:18432
s_waitcnt lgkmcnt(3)
v_mfma_f32_16x16x32_bf16 v[240:243], v[192:195], v[20:23], v[240:243]
v_mfma_f32_16x16x32_bf16 v[244:247], v[196:199], v[20:23], v[244:247]
ds_read_b128 v[20:23], v24 offset:19456
v_mfma_f32_16x16x32_bf16 v[252:255], v[196:199], v[4:7], v[252:255]
v_mfma_f32_16x16x32_bf16 v[248:251], v[192:195], v[4:7], v[248:251]
ds_read_b128 v[4:7], v24 offset:20480
s_waitcnt lgkmcnt(3)
v_mfma_f32_16x16x32_bf16 v[80:83], v[200:203], v[8:11], v[80:83]
v_mfma_f32_16x16x32_bf16 v[84:87], v[204:207], v[8:11], v[84:87]
ds_read_b128 v[8:11], v24 offset:21504
v_mfma_f32_16x16x32_bf16 v[92:95], v[204:207], v[12:15], v[92:95]
v_mfma_f32_16x16x32_bf16 v[88:91], v[200:203], v[12:15], v[88:91]
ds_read_b128 v[12:15], v24 offset:22528
s_waitcnt lgkmcnt(3)
v_mfma_f32_16x16x32_bf16 v[96:99], v[200:203], v[16:19], v[96:99]
v_mfma_f32_16x16x32_bf16 v[100:103], v[204:207], v[16:19], v[100:103]
ds_read_b128 v[16:19], v24 offset:23552
v_mfma_f32_16x16x32_bf16 v[108:111], v[204:207], v[20:23], v[108:111]
v_mfma_f32_16x16x32_bf16 v[104:107], v[200:203], v[20:23], v[104:107]
s_waitcnt vmcnt(5)
s_waitcnt vmcnt(3)
.Lchk4:
v_cmp_eq_u64 vcc, s[68:69], v[34:35]
s_bitcmp1_b32 vcc_lo, 0
s_cbranch_scc0 .Lretry4
.Lok4:
s_barrier
ds_read_b128 v[20:23], v24 offset:24576
s_add_u32 m0, s57, 114688
s_mov_b32 s59, 851968
buffer_load_dwordx4 v44, s[44:47], s59 offen lds
s_waitcnt lgkmcnt(3)
v_mfma_f32_16x16x32_bf16 v[112:115], v[200:203], v[4:7], v[112:115]
s_mov_b64 exec, 1
buffer_load_dwordx2 v[34:35], off, s[48:51], s66 offset:640 sc1
s_mov_b64 exec, -1
v_mfma_f32_16x16x32_bf16 v[116:119], v[204:207], v[4:7], v[116:119]
ds_read_b128 v[4:7], v24 offset:25600
ds_read_b64_tr_b16 v[216:217], v28 offset:0
ds_read_b64_tr_b16 v[218:219], v28 offset:2048
v_mfma_f32_16x16x32_bf16 v[124:127], v[204:207], v[8:11], v[124:127]
s_add_u32 m0, s57, 118784
s_mov_b32 s59, 852096
buffer_load_dwordx4 v44, s[44:47], s59 offen lds
v_mfma_f32_16x16x32_bf16 v[120:123], v[200:203], v[8:11], v[120:123]
ds_read_b128 v[8:11], v24 offset:26624
s_waitcnt lgkmcnt(5)
v_mfma_f32_16x16x32_bf16 v[240:243], v[200:203], v[12:15], v[240:243]
v_mfma_f32_16x16x32_bf16 v[244:247], v[204:207], v[12:15], v[244:247]
ds_read_b128 v[12:15], v24 offset:27648
ds_read_b64_tr_b16 v[220:221], v28 offset:8
ds_read_b64_tr_b16 v[222:223], v28 offset:2056
v_mfma_f32_16x16x32_bf16 v[252:255], v[204:207], v[16:19], v[252:255]
s_add_u32 m0, s57, 122880
s_mov_b32 s59, 1769472
buffer_load_dwordx4 v44, s[44:47], s59 offen lds
v_mfma_f32_16x16x32_bf16 v[248:251], v[200:203], v[16:19], v[248:251]
ds_read_b128 v[16:19], v24 offset:28672
s_add_u32 m0, s57, 126976
s_waitcnt lgkmcnt(7)
v_mfma_f32_16x16x32_bf16 v[48:51], v[128:131], v[20:23], 0
s_mov_b32 s59, 1769600
buffer_load_dwordx4 v44, s[44:47], s59 offen lds
v_mfma_f32_16x16x32_bf16 v[52:55], v[132:135], v[20:23], 0
ds_read_b128 v[20:23], v24 offset:29696
v_mfma_f32_16x16x32_bf16 v[60:63], v[132:135], v[4:7], 0
v_mfma_f32_16x16x32_bf16 v[56:59], v[128:131], v[4:7], 0
ds_read_b128 v[4:7], v24 offset:30720
s_waitcnt lgkmcnt(5)
v_mfma_f32_16x16x32_bf16 v[64:67], v[128:131], v[8:11], 0
v_mfma_f32_16x16x32_bf16 v[68:71], v[132:135], v[8:11], 0
ds_read_b128 v[8:11], v24 offset:31744
v_mfma_f32_16x16x32_bf16 v[76:79], v[132:135], v[12:15], 0
v_mfma_f32_16x16x32_bf16 v[72:75], v[128:131], v[12:15], 0
s_waitcnt lgkmcnt(4)
s_waitcnt vmcnt(5)
s_barrier
ds_read_b128 v[12:15], v24 offset:32768
s_waitcnt lgkmcnt(3)
v_mfma_f32_16x16x32_bf16 v[48:51], v[136:139], v[16:19], v[48:51]
v_mfma_f32_16x16x32_bf16 v[52:55], v[140:143], v[16:19], v[52:55]
ds_read_b128 v[16:19], v24 offset:33792
ds_read_b64_tr_b16 v[224:225], v28 offset:16384
v_mfma_f32_16x16x32_bf16 v[60:63], v[140:143], v[20:23], v[60:63]
ds_read_b64_tr_b16 v[226:227], v28 offset:18432
v_mfma_f32_16x16x32_bf16 v[56:59], v[136:139], v[20:23], v[56:59]
ds_read_b128 v[20:23], v24 offset:34816
s_waitcnt lgkmcnt(5)
v_mfma_f32_16x16x32_bf16 v[64:67], v[136:139], v[4:7], v[64:67]
)ASM"
R"ASM(
v_mfma_f32_16x16x32_bf16 v[68:71], v[140:143], v[4:7], v[68:71]
ds_read_b128 v[4:7], v24 offset:35840
ds_read_b64_tr_b16 v[228:229], v28 offset:16392
v_mfma_f32_16x16x32_bf16 v[76:79], v[140:143], v[8:11], v[76:79]
ds_read_b64_tr_b16 v[230:231], v28 offset:18440
v_mfma_f32_16x16x32_bf16 v[72:75], v[136:139], v[8:11], v[72:75]
ds_read_b128 v[8:11], v24 offset:36864
s_waitcnt lgkmcnt(7)
v_mfma_f32_16x16x32_bf16 v[48:51], v[144:147], v[12:15], v[48:51]
v_mfma_f32_16x16x32_bf16 v[52:55], v[148:151], v[12:15], v[52:55]
ds_read_b128 v[12:15], v24 offset:37888
v_mfma_f32_16x16x32_bf16 v[60:63], v[148:151], v[16:19], v[60:63]
v_mfma_f32_16x16x32_bf16 v[56:59], v[144:147], v[16:19], v[56:59]
ds_read_b128 v[16:19], v24 offset:38912
s_waitcnt lgkmcnt(5)
v_mfma_f32_16x16x32_bf16 v[64:67], v[144:147], v[20:23], v[64:67]
v_mfma_f32_16x16x32_bf16 v[68:71], v[148:151], v[20:23], v[68:71]
ds_read_b128 v[20:23], v24 offset:39936
v_mfma_f32_16x16x32_bf16 v[76:79], v[148:151], v[4:7], v[76:79]
v_mfma_f32_16x16x32_bf16 v[72:75], v[144:147], v[4:7], v[72:75]
s_waitcnt lgkmcnt(4)
s_waitcnt vmcnt(3)
.Lchk5:
v_cmp_eq_u64 vcc, s[68:69], v[34:35]
s_bitcmp1_b32 vcc_lo, 0
s_cbranch_scc0 .Lretry5
.Lok5:
s_barrier
ds_read_b128 v[4:7], v24 offset:0
s_waitcnt lgkmcnt(3)
v_mfma_f32_16x16x32_bf16 v[48:51], v[152:155], v[8:11], v[48:51]
v_mfma_f32_16x16x32_bf16 v[52:55], v[156:159], v[8:11], v[52:55]
ds_read_b128 v[8:11], v24 offset:1024
v_mfma_f32_16x16x32_bf16 v[60:63], v[156:159], v[12:15], v[60:63]
v_mfma_f32_16x16x32_bf16 v[56:59], v[152:155], v[12:15], v[56:59]
ds_read_b128 v[12:15], v24 offset:2048
s_waitcnt lgkmcnt(3)
v_mfma_f32_16x16x32_bf16 v[64:67], v[152:155], v[16:19], v[64:67]
v_mfma_f32_16x16x32_bf16 v[68:71], v[156:159], v[16:19], v[68:71]
ds_read_b128 v[16:19], v24 offset:3072
v_mfma_f32_16x16x32_bf16 v[76:79], v[156:159], v[20:23], v[76:79]
v_mfma_f32_16x16x32_bf16 v[72:75], v[152:155], v[20:23], v[72:75]
ds_read_b128 v[20:23], v24 offset:4096
s_waitcnt lgkmcnt(3)
v_mfma_f32_16x16x32_bf16 v[80:83], v[208:211], v[4:7], v[80:83]
v_mfma_f32_16x16x32_bf16 v[84:87], v[212:215], v[4:7], v[84:87]
ds_read_b128 v[4:7], v24 offset:5120
v_mfma_f32_16x16x32_bf16 v[92:95], v[212:215], v[8:11], v[92:95]
v_mfma_f32_16x16x32_bf16 v[88:91], v[208:211], v[8:11], v[88:91]
ds_read_b128 v[8:11], v24 offset:6144
s_waitcnt lgkmcnt(3)
v_mfma_f32_16x16x32_bf16 v[96:99], v[208:211], v[12:15], v[96:99]
v_mfma_f32_16x16x32_bf16 v[100:103], v[212:215], v[12:15], v[100:103]
ds_read_b128 v[12:15], v24 offset:7168
v_mfma_f32_16x16x32_bf16 v[108:111], v[212:215], v[16:19], v[108:111]
v_mfma_f32_16x16x32_bf16 v[104:107], v[208:211], v[16:19], v[104:107]
s_waitcnt vmcnt(0)
s_barrier
ds_read_b128 v[16:19], v24 offset:8192
s_waitcnt lgkmcnt(3)
v_mfma_f32_16x16x32_bf16 v[112:115], v[208:211], v[20:23], v[112:115]
v_mfma_f32_16x16x32_bf16 v[116:119], v[212:215], v[20:23], v[116:119]
ds_read_b128 v[20:23], v24 offset:9216
ds_read_b64_tr_b16 v[232:233], v28 offset:32768
v_mfma_f32_16x16x32_bf16 v[124:127], v[212:215], v[4:7], v[124:127]
ds_read_b64_tr_b16 v[234:235], v28 offset:34816
v_mfma_f32_16x16x32_bf16 v[120:123], v[208:211], v[4:7], v[120:123]
ds_read_b128 v[4:7], v24 offset:10240
s_waitcnt lgkmcnt(5)
v_mfma_f32_16x16x32_bf16 v[240:243], v[208:211], v[8:11], v[240:243]
v_mfma_f32_16x16x32_bf16 v[244:247], v[212:215], v[8:11], v[244:247]
ds_read_b128 v[8:11], v24 offset:11264
ds_read_b64_tr_b16 v[236:237], v28 offset:32776
v_mfma_f32_16x16x32_bf16 v[252:255], v[212:215], v[12:15], v[252:255]
ds_read_b64_tr_b16 v[238:239], v28 offset:34824
v_mfma_f32_16x16x32_bf16 v[248:251], v[208:211], v[12:15], v[248:251]
ds_read_b128 v[12:15], v24 offset:12288
s_waitcnt lgkmcnt(7)
v_mfma_f32_16x16x32_bf16 v[80:83], v[216:219], v[16:19], v[80:83]
v_mfma_f32_16x16x32_bf16 v[84:87], v[220:223], v[16:19], v[84:87]
ds_read_b128 v[16:19], v24 offset:13312
v_mfma_f32_16x16x32_bf16 v[92:95], v[220:223], v[20:23], v[92:95]
v_mfma_f32_16x16x32_bf16 v[88:91], v[216:219], v[20:23], v[88:91]
ds_read_b128 v[20:23], v24 offset:14336
s_waitcnt lgkmcnt(5)
v_mfma_f32_16x16x32_bf16 v[96:99], v[216:219], v[4:7], v[96:99]
v_mfma_f32_16x16x32_bf16 v[100:103], v[220:223], v[4:7], v[100:103]
ds_read_b128 v[4:7], v24 offset:15360
v_mfma_f32_16x16x32_bf16 v[108:111], v[220:223], v[8:11], v[108:111]
v_mfma_f32_16x16x32_bf16 v[104:107], v[216:219], v[8:11], v[104:107]
s_waitcnt lgkmcnt(4)
s_barrier
ds_read_b128 v[8:11], v24 offset:16384
s_waitcnt lgkmcnt(3)
v_mfma_f32_16x16x32_bf16 v[112:115], v[216:219], v[12:15], v[112:115]
v_mfma_f32_16x16x32_bf16 v[116:119], v[220:223], v[12:15], v[116:119]
ds_read_b128 v[12:15], v24 offset:17408
v_mfma_f32_16x16x32_bf16 v[124:127], v[220:223], v[16:19], v[124:127]
v_mfma_f32_16x16x32_bf16 v[120:123], v[216:219], v[16:19], v[120:123]
ds_read_b128 v[16:19], v24 offset:18432
s_waitcnt lgkmcnt(3)
v_mfma_f32_16x16x32_bf16 v[240:243], v[216:219], v[20:23], v[240:243]
v_mfma_f32_16x16x32_bf16 v[244:247], v[220:223], v[20:23], v[244:247]
ds_read_b128 v[20:23], v24 offset:19456
v_mfma_f32_16x16x32_bf16 v[252:255], v[220:223], v[4:7], v[252:255]
v_mfma_f32_16x16x32_bf16 v[248:251], v[216:219], v[4:7], v[248:251]
ds_read_b128 v[4:7], v24 offset:20480
s_waitcnt lgkmcnt(3)
v_mfma_f32_16x16x32_bf16 v[80:83], v[224:227], v[8:11], v[80:83]
v_mfma_f32_16x16x32_bf16 v[84:87], v[228:231], v[8:11], v[84:87]
ds_read_b128 v[8:11], v24 offset:21504
v_mfma_f32_16x16x32_bf16 v[92:95], v[228:231], v[12:15], v[92:95]
v_mfma_f32_16x16x32_bf16 v[88:91], v[224:227], v[12:15], v[88:91]
ds_read_b128 v[12:15], v24 offset:22528
s_waitcnt lgkmcnt(3)
v_mfma_f32_16x16x32_bf16 v[96:99], v[224:227], v[16:19], v[96:99]
v_mfma_f32_16x16x32_bf16 v[100:103], v[228:231], v[16:19], v[100:103]
ds_read_b128 v[16:19], v24 offset:23552
v_mfma_f32_16x16x32_bf16 v[108:111], v[228:231], v[20:23], v[108:111]
v_mfma_f32_16x16x32_bf16 v[104:107], v[224:227], v[20:23], v[104:107]
s_barrier
ds_read_b128 v[20:23], v24 offset:24576
s_waitcnt lgkmcnt(3)
v_mfma_f32_16x16x32_bf16 v[112:115], v[224:227], v[4:7], v[112:115]
s_mov_b64 exec, 1
buffer_load_dwordx2 v[34:35], off, s[48:51], s67 offset:640 sc1
s_mov_b64 exec, -1
v_mfma_f32_16x16x32_bf16 v[116:119], v[228:231], v[4:7], v[116:119]
ds_read_b128 v[4:7], v24 offset:25600
v_mfma_f32_16x16x32_bf16 v[124:127], v[228:231], v[8:11], v[124:127]
v_mfma_f32_16x16x32_bf16 v[120:123], v[224:227], v[8:11], v[120:123]
ds_read_b128 v[8:11], v24 offset:26624
s_waitcnt lgkmcnt(3)
v_mfma_f32_16x16x32_bf16 v[240:243], v[224:227], v[12:15], v[240:243]
v_mfma_f32_16x16x32_bf16 v[244:247], v[228:231], v[12:15], v[244:247]
ds_read_b128 v[12:15], v24 offset:27648
v_mfma_f32_16x16x32_bf16 v[252:255], v[228:231], v[16:19], v[252:255]
v_mfma_f32_16x16x32_bf16 v[248:251], v[224:227], v[16:19], v[248:251]
ds_read_b128 v[16:19], v24 offset:28672
s_waitcnt lgkmcnt(3)
v_mfma_f32_16x16x32_bf16 v[80:83], v[232:235], v[20:23], v[80:83]
v_mfma_f32_16x16x32_bf16 v[84:87], v[236:239], v[20:23], v[84:87]
ds_read_b128 v[20:23], v24 offset:29696
v_mfma_f32_16x16x32_bf16 v[92:95], v[236:239], v[4:7], v[92:95]
v_mfma_f32_16x16x32_bf16 v[88:91], v[232:235], v[4:7], v[88:91]
ds_read_b128 v[4:7], v24 offset:30720
s_waitcnt lgkmcnt(3)
v_mfma_f32_16x16x32_bf16 v[96:99], v[232:235], v[8:11], v[96:99]
v_mfma_f32_16x16x32_bf16 v[100:103], v[236:239], v[8:11], v[100:103]
ds_read_b128 v[8:11], v24 offset:31744
v_cvt_pk_bf16_f32 v80, v80, v81
v_cvt_pk_bf16_f32 v81, v82, v83
v_mfma_f32_16x16x32_bf16 v[108:111], v[236:239], v[12:15], v[108:111]
v_cvt_pk_bf16_f32 v82, v84, v85
v_cvt_pk_bf16_f32 v83, v86, v87
ds_write_b128 v30, v[80:83] offset:32768
v_mfma_f32_16x16x32_bf16 v[104:107], v[232:235], v[12:15], v[104:107]
s_waitcnt lgkmcnt(0)
s_barrier
ds_read_b128 v[12:15], v24 offset:32768
v_mfma_f32_16x16x32_bf16 v[112:115], v[232:235], v[16:19], v[112:115]
ds_read_b128 v[84:87], v30 offset:34816
v_mfma_f32_16x16x32_bf16 v[116:119], v[236:239], v[16:19], v[116:119]
ds_read_b128 v[16:19], v24 offset:33792
v_cvt_pk_bf16_f32 v96, v96, v97
v_cvt_pk_bf16_f32 v97, v98, v99
v_mfma_f32_16x16x32_bf16 v[124:127], v[236:239], v[20:23], v[124:127]
v_cvt_pk_bf16_f32 v98, v100, v101
v_cvt_pk_bf16_f32 v99, v102, v103
ds_write_b128 v30, v[96:99] offset:49152
v_mfma_f32_16x16x32_bf16 v[120:123], v[232:235], v[20:23], v[120:123]
ds_read_b128 v[20:23], v24 offset:34816
v_mfma_f32_16x16x32_bf16 v[240:243], v[232:235], v[4:7], v[240:243]
v_mfma_f32_16x16x32_bf16 v[244:247], v[236:239], v[4:7], v[244:247]
ds_read_b128 v[4:7], v24 offset:35840
v_cvt_pk_bf16_f32 v112, v112, v113
v_cvt_pk_bf16_f32 v113, v114, v115
v_mfma_f32_16x16x32_bf16 v[252:255], v[236:239], v[8:11], v[252:255]
v_cvt_pk_bf16_f32 v114, v116, v117
v_cvt_pk_bf16_f32 v115, v118, v119
ds_write_b128 v30, v[112:115] offset:0
v_mfma_f32_16x16x32_bf16 v[248:251], v[232:235], v[8:11], v[248:251]
ds_read_b128 v[8:11], v24 offset:36864
s_waitcnt lgkmcnt(6)
v_lshlrev_b32 v80, 16, v84
v_and_b32 v84, 0xffff0000, v84
s_waitcnt lgkmcnt(5)
v_mfma_f32_16x16x32_bf16 v[48:51], v[160:163], v[12:15], v[48:51]
v_lshlrev_b32 v81, 16, v85
v_and_b32 v85, 0xffff0000, v85
v_lshlrev_b32 v82, 16, v86
v_mfma_f32_16x16x32_bf16 v[52:55], v[164:167], v[12:15], v[52:55]
ds_read_b128 v[12:15], v24 offset:37888
v_and_b32 v86, 0xffff0000, v86
v_lshlrev_b32 v83, 16, v87
v_and_b32 v87, 0xffff0000, v87
v_add_f32 v88, v88, v80
v_add_f32 v89, v89, v84
v_mfma_f32_16x16x32_bf16 v[60:63], v[164:167], v[16:19], v[60:63]
v_cvt_pk_bf16_f32 v240, v240, v241
)ASM"
R"ASM(
v_cvt_pk_bf16_f32 v241, v242, v243
v_cvt_pk_bf16_f32 v242, v244, v245
v_cvt_pk_bf16_f32 v243, v246, v247
ds_write_b128 v30, v[240:243] offset:16384
v_mfma_f32_16x16x32_bf16 v[56:59], v[160:163], v[16:19], v[56:59]
ds_read_b128 v[16:19], v24 offset:38912
v_add_f32 v90, v90, v81
v_add_f32 v91, v91, v85
s_waitcnt lgkmcnt(5)
v_mfma_f32_16x16x32_bf16 v[64:67], v[160:163], v[20:23], v[64:67]
v_add_f32 v92, v92, v82
v_add_f32 v93, v93, v86
v_add_f32 v94, v94, v83
v_mfma_f32_16x16x32_bf16 v[68:71], v[164:167], v[20:23], v[68:71]
ds_read_b128 v[20:23], v24 offset:39936
v_add_f32 v95, v95, v87
v_cvt_pk_bf16_f32 v88, v88, v89
v_cvt_pk_bf16_f32 v89, v90, v91
v_mfma_f32_16x16x32_bf16 v[76:79], v[164:167], v[4:7], v[76:79]
v_cvt_pk_bf16_f32 v90, v92, v93
v_cvt_pk_bf16_f32 v91, v94, v95
buffer_store_dwordx4 v[88:91], v31, s[48:51], s85 offen offset:0 sc1
v_mfma_f32_16x16x32_bf16 v[72:75], v[160:163], v[4:7], v[72:75]
s_waitcnt lgkmcnt(5)
s_waitcnt lgkmcnt(2)
s_waitcnt vmcnt(1)
.Lchk6:
v_cmp_eq_u64 vcc, s[70:71], v[34:35]
s_bitcmp1_b32 vcc_lo, 0
s_cbranch_scc0 .Lretry6
.Lok6:
s_barrier
ds_read_b128 v[4:7], v24 offset:0
ds_read_b128 v[100:103], v30 offset:51200
v_mfma_f32_16x16x32_bf16 v[48:51], v[168:171], v[8:11], v[48:51]
ds_read_b128 v[116:119], v30 offset:2048
ds_read_b128 v[244:247], v30 offset:18432
v_mfma_f32_16x16x32_bf16 v[52:55], v[172:175], v[8:11], v[52:55]
ds_read_b128 v[8:11], v24 offset:1024
v_mfma_f32_16x16x32_bf16 v[60:63], v[172:175], v[12:15], v[60:63]
v_mfma_f32_16x16x32_bf16 v[56:59], v[168:171], v[12:15], v[56:59]
ds_read_b128 v[12:15], v24 offset:2048
s_waitcnt lgkmcnt(6)
v_mfma_f32_16x16x32_bf16 v[64:67], v[168:171], v[16:19], v[64:67]
v_mfma_f32_16x16x32_bf16 v[68:71], v[172:175], v[16:19], v[68:71]
ds_read_b128 v[16:19], v24 offset:3072
v_mfma_f32_16x16x32_bf16 v[76:79], v[172:175], v[20:23], v[76:79]
v_mfma_f32_16x16x32_bf16 v[72:75], v[168:171], v[20:23], v[72:75]
ds_read_b128 v[20:23], v24 offset:4096
s_waitcnt lgkmcnt(6)
v_lshlrev_b32 v96, 16, v100
v_and_b32 v100, 0xffff0000, v100
s_waitcnt lgkmcnt(3)
v_mfma_f32_16x16x32_bf16 v[48:51], v[176:179], v[4:7], v[48:51]
v_lshlrev_b32 v97, 16, v101
v_and_b32 v101, 0xffff0000, v101
v_lshlrev_b32 v98, 16, v102
v_mfma_f32_16x16x32_bf16 v[52:55], v[180:183], v[4:7], v[52:55]
ds_read_b128 v[4:7], v24 offset:5120
v_and_b32 v102, 0xffff0000, v102
v_lshlrev_b32 v99, 16, v103
v_mfma_f32_16x16x32_bf16 v[60:63], v[180:183], v[8:11], v[60:63]
v_and_b32 v103, 0xffff0000, v103
v_add_f32 v104, v104, v96
v_add_f32 v105, v105, v100
v_mfma_f32_16x16x32_bf16 v[56:59], v[176:179], v[8:11], v[56:59]
ds_read_b128 v[8:11], v24 offset:6144
v_add_f32 v106, v106, v97
v_add_f32 v107, v107, v101
v_add_f32 v108, v108, v98
v_add_f32 v109, v109, v102
v_add_f32 v110, v110, v99
s_waitcnt lgkmcnt(3)
v_mfma_f32_16x16x32_bf16 v[64:67], v[176:179], v[12:15], v[64:67]
v_lshlrev_b32 v112, 16, v116
v_and_b32 v116, 0xffff0000, v116
v_lshlrev_b32 v113, 16, v117
v_and_b32 v117, 0xffff0000, v117
v_lshlrev_b32 v114, 16, v118
v_mfma_f32_16x16x32_bf16 v[68:71], v[180:183], v[12:15], v[68:71]
ds_read_b128 v[12:15], v24 offset:7168
v_add_f32 v111, v111, v103
v_cvt_pk_bf16_f32 v104, v104, v105
v_cvt_pk_bf16_f32 v105, v106, v107
v_cvt_pk_bf16_f32 v106, v108, v109
v_cvt_pk_bf16_f32 v107, v110, v111
v_mfma_f32_16x16x32_bf16 v[76:79], v[180:183], v[16:19], v[76:79]
buffer_store_dwordx4 v[104:107], v31, s[48:51], s85 offen offset:1024 sc1
v_and_b32 v118, 0xffff0000, v118
v_lshlrev_b32 v115, 16, v119
v_and_b32 v119, 0xffff0000, v119
v_add_f32 v120, v120, v112
v_add_f32 v121, v121, v116
v_mfma_f32_16x16x32_bf16 v[72:75], v[176:179], v[16:19], v[72:75]
s_barrier
ds_read_b128 v[16:19], v24 offset:8192
v_add_f32 v122, v122, v113
v_add_f32 v123, v123, v117
v_add_f32 v124, v124, v114
v_add_f32 v125, v125, v118
v_add_f32 v126, v126, v115
s_waitcnt lgkmcnt(3)
v_mfma_f32_16x16x32_bf16 v[48:51], v[184:187], v[20:23], v[48:51]
v_lshlrev_b32 v240, 16, v244
v_and_b32 v244, 0xffff0000, v244
v_lshlrev_b32 v241, 16, v245
v_and_b32 v245, 0xffff0000, v245
v_lshlrev_b32 v242, 16, v246
v_mfma_f32_16x16x32_bf16 v[52:55], v[188:191], v[20:23], v[52:55]
ds_read_b128 v[20:23], v24 offset:9216
v_add_f32 v127, v127, v119
v_cvt_pk_bf16_f32 v120, v120, v121
v_cvt_pk_bf16_f32 v121, v122, v123
v_cvt_pk_bf16_f32 v122, v124, v125
v_cvt_pk_bf16_f32 v123, v126, v127
v_mfma_f32_16x16x32_bf16 v[60:63], v[188:191], v[4:7], v[60:63]
v_and_b32 v246, 0xffff0000, v246
v_lshlrev_b32 v243, 16, v247
v_and_b32 v247, 0xffff0000, v247
v_add_f32 v248, v248, v240
v_add_f32 v249, v249, v244
v_mfma_f32_16x16x32_bf16 v[56:59], v[184:187], v[4:7], v[56:59]
ds_read_b128 v[4:7], v24 offset:10240
v_add_f32 v250, v250, v241
v_add_f32 v251, v251, v245
s_waitcnt lgkmcnt(3)
v_mfma_f32_16x16x32_bf16 v[64:67], v[184:187], v[8:11], v[64:67]
v_add_f32 v252, v252, v242
v_add_f32 v253, v253, v246
v_add_f32 v254, v254, v243
v_mfma_f32_16x16x32_bf16 v[68:71], v[188:191], v[8:11], v[68:71]
ds_read_b128 v[8:11], v24 offset:11264
v_add_f32 v255, v255, v247
v_cvt_pk_bf16_f32 v248, v248, v249
v_mfma_f32_16x16x32_bf16 v[76:79], v[188:191], v[12:15], v[76:79]
v_cvt_pk_bf16_f32 v249, v250, v251
v_cvt_pk_bf16_f32 v250, v252, v253
v_cvt_pk_bf16_f32 v251, v254, v255
v_mfma_f32_16x16x32_bf16 v[72:75], v[184:187], v[12:15], v[72:75]
ds_read_b128 v[12:15], v24 offset:12288
s_waitcnt lgkmcnt(3)
v_mfma_f32_16x16x32_bf16 v[48:51], v[192:195], v[16:19], v[48:51]
v_mfma_f32_16x16x32_bf16 v[52:55], v[196:199], v[16:19], v[52:55]
ds_read_b128 v[16:19], v24 offset:13312
v_mfma_f32_16x16x32_bf16 v[60:63], v[196:199], v[20:23], v[60:63]
v_mfma_f32_16x16x32_bf16 v[56:59], v[192:195], v[20:23], v[56:59]
ds_read_b128 v[20:23], v24 offset:14336
buffer_store_dwordx4 v[120:123], v31, s[48:51], s86 offen offset:0 sc1
s_waitcnt lgkmcnt(3)
v_mfma_f32_16x16x32_bf16 v[64:67], v[192:195], v[4:7], v[64:67]
buffer_store_dwordx4 v[248:251], v31, s[48:51], s86 offen offset:1024 sc1
v_mfma_f32_16x16x32_bf16 v[68:71], v[196:199], v[4:7], v[68:71]
ds_read_b128 v[4:7], v24 offset:15360
v_lshlrev_b32 v112, 16, v36
v_mfma_f32_16x16x32_bf16 v[76:79], v[196:199], v[8:11], v[76:79]
v_and_b32 v113, 0xffff0000, v36
v_lshlrev_b32 v114, 16, v37
v_mfma_f32_16x16x32_bf16 v[72:75], v[192:195], v[8:11], v[72:75]
s_barrier
ds_read_b128 v[8:11], v24 offset:16384
v_and_b32 v115, 0xffff0000, v37
s_waitcnt lgkmcnt(3)
v_mfma_f32_16x16x32_bf16 v[48:51], v[200:203], v[12:15], v[48:51]
v_lshlrev_b32 v116, 16, v38
v_and_b32 v117, 0xffff0000, v38
v_mfma_f32_16x16x32_bf16 v[52:55], v[204:207], v[12:15], v[52:55]
ds_read_b128 v[12:15], v24 offset:17408
v_lshlrev_b32 v118, 16, v39
v_mfma_f32_16x16x32_bf16 v[60:63], v[204:207], v[16:19], v[60:63]
v_and_b32 v119, 0xffff0000, v39
v_mfma_f32_16x16x32_bf16 v[56:59], v[200:203], v[16:19], v[56:59]
ds_read_b128 v[16:19], v24 offset:18432
v_mov_b32 v120, v112
s_waitcnt lgkmcnt(3)
v_mfma_f32_16x16x32_bf16 v[64:67], v[200:203], v[20:23], v[64:67]
v_mov_b32 v121, v113
v_mov_b32 v122, v114
v_mfma_f32_16x16x32_bf16 v[68:71], v[204:207], v[20:23], v[68:71]
ds_read_b128 v[20:23], v24 offset:19456
v_mov_b32 v123, v115
v_mfma_f32_16x16x32_bf16 v[76:79], v[204:207], v[4:7], v[76:79]
v_mov_b32 v124, v116
v_mov_b32 v125, v117
v_mfma_f32_16x16x32_bf16 v[72:75], v[200:203], v[4:7], v[72:75]
ds_read_b128 v[4:7], v24 offset:20480
v_mov_b32 v126, v118
s_waitcnt lgkmcnt(3)
v_mfma_f32_16x16x32_bf16 v[48:51], v[208:211], v[8:11], v[48:51]
v_mov_b32 v127, v119
v_mfma_f32_16x16x32_bf16 v[52:55], v[212:215], v[8:11], v[52:55]
ds_read_b128 v[8:11], v24 offset:21504
v_mfma_f32_16x16x32_bf16 v[60:63], v[212:215], v[12:15], v[60:63]
v_mfma_f32_16x16x32_bf16 v[56:59], v[208:211], v[12:15], v[56:59]
ds_read_b128 v[12:15], v24 offset:22528
s_waitcnt lgkmcnt(3)
v_mfma_f32_16x16x32_bf16 v[64:67], v[208:211], v[16:19], v[64:67]
v_mfma_f32_16x16x32_bf16 v[68:71], v[212:215], v[16:19], v[68:71]
ds_read_b128 v[16:19], v24 offset:23552
v_mfma_f32_16x16x32_bf16 v[76:79], v[212:215], v[20:23], v[76:79]
v_mfma_f32_16x16x32_bf16 v[72:75], v[208:211], v[20:23], v[72:75]
)ASM"
R"ASM(
s_barrier
ds_read_b128 v[20:23], v24 offset:24576
s_waitcnt lgkmcnt(3)
v_mfma_f32_16x16x32_bf16 v[48:51], v[216:219], v[4:7], v[48:51]
v_mfma_f32_16x16x32_bf16 v[52:55], v[220:223], v[4:7], v[52:55]
ds_read_b128 v[4:7], v24 offset:25600
v_mfma_f32_16x16x32_bf16 v[60:63], v[220:223], v[8:11], v[60:63]
v_mfma_f32_16x16x32_bf16 v[56:59], v[216:219], v[8:11], v[56:59]
ds_read_b128 v[8:11], v24 offset:26624
s_waitcnt lgkmcnt(3)
v_mfma_f32_16x16x32_bf16 v[64:67], v[216:219], v[12:15], v[64:67]
v_mfma_f32_16x16x32_bf16 v[68:71], v[220:223], v[12:15], v[68:71]
ds_read_b128 v[12:15], v24 offset:27648
v_mfma_f32_16x16x32_bf16 v[76:79], v[220:223], v[16:19], v[76:79]
v_mfma_f32_16x16x32_bf16 v[72:75], v[216:219], v[16:19], v[72:75]
ds_read_b128 v[16:19], v24 offset:28672
s_waitcnt lgkmcnt(3)
v_mfma_f32_16x16x32_bf16 v[48:51], v[224:227], v[20:23], v[48:51]
v_mfma_f32_16x16x32_bf16 v[52:55], v[228:231], v[20:23], v[52:55]
ds_read_b128 v[20:23], v24 offset:29696
v_mfma_f32_16x16x32_bf16 v[60:63], v[228:231], v[4:7], v[60:63]
v_mfma_f32_16x16x32_bf16 v[56:59], v[224:227], v[4:7], v[56:59]
ds_read_b128 v[4:7], v24 offset:30720
s_waitcnt lgkmcnt(3)
v_mfma_f32_16x16x32_bf16 v[64:67], v[224:227], v[8:11], v[64:67]
v_mfma_f32_16x16x32_bf16 v[68:71], v[228:231], v[8:11], v[68:71]
ds_read_b128 v[8:11], v24 offset:31744
v_mfma_f32_16x16x32_bf16 v[76:79], v[228:231], v[12:15], v[76:79]
v_mfma_f32_16x16x32_bf16 v[72:75], v[224:227], v[12:15], v[72:75]
s_waitcnt vmcnt(0)
s_barrier
s_cmp_lg_u32 s56, 0
s_cbranch_scc1 .Lnf8
s_mov_b64 exec, 0xff
buffer_store_dwordx4 v[2:5], v31, s[48:51], s81 offen offset:768 sc1
s_mov_b64 exec, -1
.Lnf8:
ds_read_b128 v[12:15], v24 offset:32768
s_waitcnt lgkmcnt(3)
v_mfma_f32_16x16x32_bf16 v[48:51], v[232:235], v[16:19], v[48:51]
v_mfma_f32_16x16x32_bf16 v[52:55], v[236:239], v[16:19], v[52:55]
ds_read_b128 v[16:19], v24 offset:33792
v_mfma_f32_16x16x32_bf16 v[60:63], v[236:239], v[20:23], v[60:63]
v_mfma_f32_16x16x32_bf16 v[56:59], v[232:235], v[20:23], v[56:59]
ds_read_b128 v[20:23], v24 offset:34816
s_waitcnt lgkmcnt(3)
v_mfma_f32_16x16x32_bf16 v[64:67], v[232:235], v[4:7], v[64:67]
v_mfma_f32_16x16x32_bf16 v[68:71], v[236:239], v[4:7], v[68:71]
ds_read_b128 v[4:7], v24 offset:35840
v_cvt_pk_bf16_f32 v48, v48, v49
v_cvt_pk_bf16_f32 v49, v50, v51
v_mfma_f32_16x16x32_bf16 v[76:79], v[236:239], v[8:11], v[76:79]
v_cvt_pk_bf16_f32 v50, v52, v53
v_cvt_pk_bf16_f32 v51, v54, v55
ds_write_b128 v30, v[48:51] offset:32768
v_mfma_f32_16x16x32_bf16 v[72:75], v[232:235], v[8:11], v[72:75]
ds_read_b128 v[8:11], v24 offset:36864
s_waitcnt lgkmcnt(4)
v_mfma_f32_16x16x32_bf16 v[80:83], v[128:131], v[12:15], 0
v_mfma_f32_16x16x32_bf16 v[84:87], v[132:135], v[12:15], 0
ds_read_b128 v[12:15], v24 offset:37888
v_cvt_pk_bf16_f32 v64, v64, v65
v_cvt_pk_bf16_f32 v65, v66, v67
v_mfma_f32_16x16x32_bf16 v[92:95], v[132:135], v[16:19], 0
v_cvt_pk_bf16_f32 v66, v68, v69
v_cvt_pk_bf16_f32 v67, v70, v71
ds_write_b128 v30, v[64:67] offset:49152
v_mfma_f32_16x16x32_bf16 v[88:91], v[128:131], v[16:19], 0
ds_read_b128 v[16:19], v24 offset:38912
s_waitcnt lgkmcnt(5)
v_mfma_f32_16x16x32_bf16 v[96:99], v[128:131], v[20:23], 0
v_mfma_f32_16x16x32_bf16 v[100:103], v[132:135], v[20:23], 0
ds_read_b128 v[20:23], v24 offset:39936
v_mfma_f32_16x16x32_bf16 v[108:111], v[132:135], v[4:7], 0
v_mfma_f32_16x16x32_bf16 v[104:107], v[128:131], v[4:7], 0
s_waitcnt lgkmcnt(5)
s_waitcnt lgkmcnt(2)
s_barrier
ds_read_b128 v[4:7], v24 offset:0
ds_read_b128 v[52:55], v30 offset:34816
v_mfma_f32_16x16x32_bf16 v[80:83], v[136:139], v[8:11], v[80:83]
ds_read_b128 v[68:71], v30 offset:51200
v_mfma_f32_16x16x32_bf16 v[84:87], v[140:143], v[8:11], v[84:87]
ds_read_b128 v[8:11], v24 offset:1024
v_mfma_f32_16x16x32_bf16 v[92:95], v[140:143], v[12:15], v[92:95]
v_mfma_f32_16x16x32_bf16 v[88:91], v[136:139], v[12:15], v[88:91]
ds_read_b128 v[12:15], v24 offset:2048
s_waitcnt lgkmcnt(3)
v_lshlrev_b32 v48, 16, v52
v_and_b32 v52, 0xffff0000, v52
v_lshlrev_b32 v49, 16, v53
v_and_b32 v53, 0xffff0000, v53
v_lshlrev_b32 v50, 16, v54
v_mfma_f32_16x16x32_bf16 v[96:99], v[136:139], v[16:19], v[96:99]
v_and_b32 v54, 0xffff0000, v54
v_lshlrev_b32 v51, 16, v55
v_and_b32 v55, 0xffff0000, v55
v_add_f32 v56, v56, v48
v_add_f32 v57, v57, v52
v_mfma_f32_16x16x32_bf16 v[100:103], v[140:143], v[16:19], v[100:103]
ds_read_b128 v[16:19], v24 offset:3072
v_add_f32 v58, v58, v49
v_add_f32 v59, v59, v53
v_add_f32 v60, v60, v50
v_add_f32 v61, v61, v54
v_add_f32 v62, v62, v51
v_add_f32 v63, v63, v55
v_cvt_pk_bf16_f32 v56, v56, v57
v_cvt_pk_bf16_f32 v57, v58, v59
v_cvt_pk_bf16_f32 v58, v60, v61
v_cvt_pk_bf16_f32 v59, v62, v63
v_mfma_f32_16x16x32_bf16 v[108:111], v[140:143], v[20:23], v[108:111]
buffer_store_dwordx4 v[56:59], v31, s[48:51], s87 offen offset:0 sc1
s_waitcnt lgkmcnt(3)
v_lshlrev_b32 v64, 16, v68
v_and_b32 v68, 0xffff0000, v68
v_lshlrev_b32 v65, 16, v69
v_and_b32 v69, 0xffff0000, v69
v_lshlrev_b32 v66, 16, v70
v_and_b32 v70, 0xffff0000, v70
v_lshlrev_b32 v67, 16, v71
v_and_b32 v71, 0xffff0000, v71
v_add_f32 v72, v72, v64
v_add_f32 v73, v73, v68
v_mfma_f32_16x16x32_bf16 v[104:107], v[136:139], v[20:23], v[104:107]
ds_read_b128 v[20:23], v24 offset:4096
v_add_f32 v74, v74, v65
v_add_f32 v75, v75, v69
v_add_f32 v76, v76, v66
v_add_f32 v77, v77, v70
v_add_f32 v78, v78, v67
s_waitcnt lgkmcnt(3)
v_mfma_f32_16x16x32_bf16 v[80:83], v[144:147], v[4:7], v[80:83]
v_add_f32 v79, v79, v71
v_cvt_pk_bf16_f32 v72, v72, v73
v_cvt_pk_bf16_f32 v73, v74, v75
v_cvt_pk_bf16_f32 v74, v76, v77
v_cvt_pk_bf16_f32 v75, v78, v79
buffer_store_dwordx4 v[72:75], v31, s[48:51], s87 offen offset:1024 sc1
v_mfma_f32_16x16x32_bf16 v[84:87], v[148:151], v[4:7], v[84:87]
ds_read_b128 v[4:7], v24 offset:5120
v_mfma_f32_16x16x32_bf16 v[92:95], v[148:151], v[8:11], v[92:95]
v_mfma_f32_16x16x32_bf16 v[88:91], v[144:147], v[8:11], v[88:91]
ds_read_b128 v[8:11], v24 offset:6144
s_waitcnt lgkmcnt(3)
v_mfma_f32_16x16x32_bf16 v[96:99], v[144:147], v[12:15], v[96:99]
v_mfma_f32_16x16x32_bf16 v[100:103], v[148:151], v[12:15], v[100:103]
ds_read_b128 v[12:15], v24 offset:7168
v_mfma_f32_16x16x32_bf16 v[108:111], v[148:151], v[16:19], v[108:111]
v_mfma_f32_16x16x32_bf16 v[104:107], v[144:147], v[16:19], v[104:107]
s_barrier
ds_read_b128 v[16:19], v24 offset:8192
s_waitcnt lgkmcnt(3)
v_mfma_f32_16x16x32_bf16 v[80:83], v[152:155], v[20:23], v[80:83]
v_mfma_f32_16x16x32_bf16 v[84:87], v[156:159], v[20:23], v[84:87]
ds_read_b128 v[20:23], v24 offset:9216
v_mfma_f32_16x16x32_bf16 v[92:95], v[156:159], v[4:7], v[92:95]
v_mfma_f32_16x16x32_bf16 v[88:91], v[152:155], v[4:7], v[88:91]
ds_read_b128 v[4:7], v24 offset:10240
s_waitcnt lgkmcnt(3)
v_mfma_f32_16x16x32_bf16 v[96:99], v[152:155], v[8:11], v[96:99]
v_mfma_f32_16x16x32_bf16 v[100:103], v[156:159], v[8:11], v[100:103]
ds_read_b128 v[8:11], v24 offset:11264
v_mfma_f32_16x16x32_bf16 v[108:111], v[156:159], v[12:15], v[108:111]
v_mfma_f32_16x16x32_bf16 v[104:107], v[152:155], v[12:15], v[104:107]
ds_read_b128 v[12:15], v24 offset:12288
s_waitcnt lgkmcnt(3)
v_mfma_f32_16x16x32_bf16 v[80:83], v[160:163], v[16:19], v[80:83]
v_mfma_f32_16x16x32_bf16 v[84:87], v[164:167], v[16:19], v[84:87]
ds_read_b128 v[16:19], v24 offset:13312
v_mfma_f32_16x16x32_bf16 v[92:95], v[164:167], v[20:23], v[92:95]
v_mfma_f32_16x16x32_bf16 v[88:91], v[160:163], v[20:23], v[88:91]
ds_read_b128 v[20:23], v24 offset:14336
s_waitcnt lgkmcnt(3)
v_mfma_f32_16x16x32_bf16 v[96:99], v[160:163], v[4:7], v[96:99]
v_mfma_f32_16x16x32_bf16 v[100:103], v[164:167], v[4:7], v[100:103]
ds_read_b128 v[4:7], v24 offset:15360
v_mfma_f32_16x16x32_bf16 v[108:111], v[164:167], v[8:11], v[108:111]
v_mfma_f32_16x16x32_bf16 v[104:107], v[160:163], v[8:11], v[104:107]
s_waitcnt vmcnt(0)
s_barrier
s_cmp_lg_u32 s56, 0
s_cbranch_scc1 .Lnf9
s_mov_b64 exec, 0xff
buffer_store_dwordx4 v[2:5], v31, s[48:51], s81 offen offset:896 sc1
s_mov_b64 exec, -1
.Lnf9:
ds_read_b128 v[8:11], v24 offset:16384
s_mov_b64 exec, 1
buffer_load_dwordx2 v[26:27], off, s[48:51], s89 offset:768 sc1
s_mov_b64 exec, -1
s_waitcnt lgkmcnt(3)
v_mfma_f32_16x16x32_bf16 v[80:83], v[168:171], v[12:15], v[80:83]
s_mov_b64 exec, 1
buffer_load_dwordx2 v[28:29], off, s[48:51], s90 offset:768 sc1
s_mov_b64 exec, -1
v_mfma_f32_16x16x32_bf16 v[84:87], v[172:175], v[12:15], v[84:87]
ds_read_b128 v[12:15], v24 offset:17408
v_mfma_f32_16x16x32_bf16 v[92:95], v[172:175], v[16:19], v[92:95]
v_mfma_f32_16x16x32_bf16 v[88:91], v[168:171], v[16:19], v[88:91]
)ASM"
R"ASM(
ds_read_b128 v[16:19], v24 offset:18432
s_waitcnt lgkmcnt(3)
v_mfma_f32_16x16x32_bf16 v[96:99], v[168:171], v[20:23], v[96:99]
v_mfma_f32_16x16x32_bf16 v[100:103], v[172:175], v[20:23], v[100:103]
ds_read_b128 v[20:23], v24 offset:19456
v_mfma_f32_16x16x32_bf16 v[108:111], v[172:175], v[4:7], v[108:111]
v_mfma_f32_16x16x32_bf16 v[104:107], v[168:171], v[4:7], v[104:107]
ds_read_b128 v[4:7], v24 offset:20480
s_waitcnt lgkmcnt(3)
v_mfma_f32_16x16x32_bf16 v[80:83], v[176:179], v[8:11], v[80:83]
v_mfma_f32_16x16x32_bf16 v[84:87], v[180:183], v[8:11], v[84:87]
ds_read_b128 v[8:11], v24 offset:21504
v_mfma_f32_16x16x32_bf16 v[92:95], v[180:183], v[12:15], v[92:95]
v_mfma_f32_16x16x32_bf16 v[88:91], v[176:179], v[12:15], v[88:91]
ds_read_b128 v[12:15], v24 offset:22528
s_mov_b32 s52, s18
s_and_b32 s53, s19, 0xffff
s_mov_b32 s54, 4194304
s_mov_b32 s55, 0x20000
s_lshl_b32 s42, s77, 8
s_waitcnt lgkmcnt(3)
v_mfma_f32_16x16x32_bf16 v[96:99], v[176:179], v[16:19], v[96:99]
s_lshl_b32 s43, s76, 6
s_add_u32 s42, s42, s43
s_add_u32 s42, s42, 16
s_lshl_b32 s42, s42, 11
s_lshl_b32 s43, s78, 8
v_mfma_f32_16x16x32_bf16 v[100:103], v[180:183], v[16:19], v[100:103]
ds_read_b128 v[16:19], v24 offset:23552
s_add_u32 s42, s42, s43
s_lshl_b32 s43, s56, 6
s_add_u32 s42, s42, s43
s_add_u32 s43, s42, 65536
v_mfma_f32_16x16x32_bf16 v[108:111], v[180:183], v[20:23], v[108:111]
v_bfe_u32 v1, v0, 0, 4
v_lshlrev_b32 v25, 11, v1
v_bfe_u32 v1, v0, 4, 2
v_lshl_add_u32 v25, v1, 4, v25
v_mfma_f32_16x16x32_bf16 v[104:107], v[176:179], v[20:23], v[104:107]
s_barrier
ds_read_b128 v[20:23], v24 offset:24576
s_waitcnt lgkmcnt(3)
v_mfma_f32_16x16x32_bf16 v[80:83], v[184:187], v[4:7], v[80:83]
v_mfma_f32_16x16x32_bf16 v[84:87], v[188:191], v[4:7], v[84:87]
ds_read_b128 v[4:7], v24 offset:25600
v_mfma_f32_16x16x32_bf16 v[92:95], v[188:191], v[8:11], v[92:95]
v_mfma_f32_16x16x32_bf16 v[88:91], v[184:187], v[8:11], v[88:91]
ds_read_b128 v[8:11], v24 offset:26624
s_waitcnt lgkmcnt(3)
v_mfma_f32_16x16x32_bf16 v[96:99], v[184:187], v[12:15], v[96:99]
v_mfma_f32_16x16x32_bf16 v[100:103], v[188:191], v[12:15], v[100:103]
ds_read_b128 v[12:15], v24 offset:27648
v_mfma_f32_16x16x32_bf16 v[108:111], v[188:191], v[16:19], v[108:111]
v_mfma_f32_16x16x32_bf16 v[104:107], v[184:187], v[16:19], v[104:107]
ds_read_b128 v[16:19], v24 offset:28672
.LchkoH2:
s_waitcnt vmcnt(1)
v_cmp_eq_u64 vcc, s[96:97], v[26:27]
s_mov_b32 s38, 0x0c0c0c0c
s_bitcmp1_b32 vcc_lo, 0
s_cselect_b32 s38, 0x01000c0c, s38
s_cselect_b32 s39, 0xffff0000, 0
.LokoH2:
s_add_u32 s74, s84, 16384
buffer_load_dwordx4 v[240:243], v31, s[48:51], s74 offen offset:0 sc1
buffer_load_dwordx4 v[244:247], v31, s[48:51], s74 offen offset:1024 sc1
s_waitcnt lgkmcnt(3)
v_mfma_f32_16x16x32_bf16 v[80:83], v[192:195], v[20:23], v[80:83]
.LchkoH3:
s_waitcnt vmcnt(2)
v_cmp_eq_u64 vcc, s[98:99], v[28:29]
s_mov_b32 s40, 0x0c0c0c0c
s_bitcmp1_b32 vcc_lo, 0
s_cselect_b32 s40, s38, s40
s_cselect_b32 s41, s39, 0
.LokoH3:
s_add_u32 s74, s84, 32768
buffer_load_dwordx4 v[248:251], v31, s[48:51], s74 offen offset:0 sc1
buffer_load_dwordx4 v[252:255], v31, s[48:51], s74 offen offset:1024 sc1
v_mfma_f32_16x16x32_bf16 v[84:87], v[196:199], v[20:23], v[84:87]
ds_read_b128 v[20:23], v24 offset:29696
v_mfma_f32_16x16x32_bf16 v[92:95], v[196:199], v[4:7], v[92:95]
v_mfma_f32_16x16x32_bf16 v[88:91], v[192:195], v[4:7], v[88:91]
ds_read_b128 v[4:7], v24 offset:30720
s_waitcnt lgkmcnt(3)
v_mfma_f32_16x16x32_bf16 v[96:99], v[192:195], v[8:11], v[96:99]
v_mfma_f32_16x16x32_bf16 v[100:103], v[196:199], v[8:11], v[100:103]
ds_read_b128 v[8:11], v24 offset:31744
v_mfma_f32_16x16x32_bf16 v[108:111], v[196:199], v[12:15], v[108:111]
v_mfma_f32_16x16x32_bf16 v[104:107], v[192:195], v[12:15], v[104:107]
s_barrier
ds_read_b128 v[12:15], v24 offset:32768
s_waitcnt lgkmcnt(3)
v_mfma_f32_16x16x32_bf16 v[80:83], v[200:203], v[16:19], v[80:83]
s_mov_b64 exec, 1
buffer_load_dwordx2 v[34:35], off, s[48:51], s88 offset:896 sc1
s_mov_b64 exec, -1
v_mfma_f32_16x16x32_bf16 v[84:87], v[204:207], v[16:19], v[84:87]
ds_read_b128 v[16:19], v24 offset:33792
v_mfma_f32_16x16x32_bf16 v[92:95], v[204:207], v[20:23], v[92:95]
v_mfma_f32_16x16x32_bf16 v[88:91], v[200:203], v[20:23], v[88:91]
ds_read_b128 v[20:23], v24 offset:34816
s_waitcnt lgkmcnt(3)
v_mfma_f32_16x16x32_bf16 v[96:99], v[200:203], v[4:7], v[96:99]
v_mfma_f32_16x16x32_bf16 v[100:103], v[204:207], v[4:7], v[100:103]
ds_read_b128 v[4:7], v24 offset:35840
v_mfma_f32_16x16x32_bf16 v[108:111], v[204:207], v[8:11], v[108:111]
v_mfma_f32_16x16x32_bf16 v[104:107], v[200:203], v[8:11], v[104:107]
ds_read_b128 v[8:11], v24 offset:36864
s_waitcnt lgkmcnt(3)
v_mfma_f32_16x16x32_bf16 v[80:83], v[208:211], v[12:15], v[80:83]
v_mfma_f32_16x16x32_bf16 v[84:87], v[212:215], v[12:15], v[84:87]
ds_read_b128 v[12:15], v24 offset:37888
v_mfma_f32_16x16x32_bf16 v[92:95], v[212:215], v[16:19], v[92:95]
v_mfma_f32_16x16x32_bf16 v[88:91], v[208:211], v[16:19], v[88:91]
ds_read_b128 v[16:19], v24 offset:38912
s_waitcnt lgkmcnt(3)
v_mfma_f32_16x16x32_bf16 v[96:99], v[208:211], v[20:23], v[96:99]
v_mfma_f32_16x16x32_bf16 v[100:103], v[212:215], v[20:23], v[100:103]
ds_read_b128 v[20:23], v24 offset:39936
v_mfma_f32_16x16x32_bf16 v[108:111], v[212:215], v[4:7], v[108:111]
v_mfma_f32_16x16x32_bf16 v[104:107], v[208:211], v[4:7], v[104:107]
s_barrier
ds_read_b128 v[4:7], v24 offset:0
s_waitcnt vmcnt(4)
v_perm_b32 v1, v240, v240, s38
v_and_b32 v240, s39, v240
v_add_f32 v112, v112, v1
v_add_f32 v113, v113, v240
s_waitcnt lgkmcnt(3)
v_mfma_f32_16x16x32_bf16 v[80:83], v[216:219], v[8:11], v[80:83]
v_perm_b32 v1, v241, v241, s38
v_and_b32 v241, s39, v241
v_add_f32 v114, v114, v1
v_add_f32 v115, v115, v241
v_mfma_f32_16x16x32_bf16 v[84:87], v[220:223], v[8:11], v[84:87]
ds_read_b128 v[8:11], v24 offset:4096
v_perm_b32 v1, v242, v242, s38
v_and_b32 v242, s39, v242
v_add_f32 v116, v116, v1
v_add_f32 v117, v117, v242
v_mfma_f32_16x16x32_bf16 v[92:95], v[220:223], v[12:15], v[92:95]
v_perm_b32 v1, v243, v243, s38
v_and_b32 v243, s39, v243
v_add_f32 v118, v118, v1
v_add_f32 v119, v119, v243
v_mfma_f32_16x16x32_bf16 v[88:91], v[216:219], v[12:15], v[88:91]
ds_read_b128 v[12:15], v24 offset:2048
s_waitcnt vmcnt(3)
v_perm_b32 v1, v244, v244, s38
v_and_b32 v244, s39, v244
v_add_f32 v120, v120, v1
v_add_f32 v121, v121, v244
s_waitcnt lgkmcnt(3)
v_mfma_f32_16x16x32_bf16 v[96:99], v[216:219], v[16:19], v[96:99]
v_perm_b32 v1, v245, v245, s38
v_and_b32 v245, s39, v245
v_add_f32 v122, v122, v1
v_add_f32 v123, v123, v245
v_mfma_f32_16x16x32_bf16 v[100:103], v[220:223], v[16:19], v[100:103]
ds_read_b128 v[16:19], v24 offset:6144
v_perm_b32 v1, v246, v246, s38
v_and_b32 v246, s39, v246
v_add_f32 v124, v124, v1
v_add_f32 v125, v125, v246
v_mfma_f32_16x16x32_bf16 v[108:111], v[220:223], v[20:23], v[108:111]
v_perm_b32 v1, v247, v247, s38
v_and_b32 v247, s39, v247
v_add_f32 v126, v126, v1
v_add_f32 v127, v127, v247
v_mfma_f32_16x16x32_bf16 v[104:107], v[216:219], v[20:23], v[104:107]
ds_read_b128 v[20:23], v24 offset:1024
s_waitcnt vmcnt(2)
v_perm_b32 v1, v248, v248, s40
v_and_b32 v248, s41, v248
v_add_f32 v112, v112, v1
v_add_f32 v113, v113, v248
v_perm_b32 v1, v249, v249, s40
v_and_b32 v249, s41, v249
v_add_f32 v114, v114, v1
v_add_f32 v115, v115, v249
s_waitcnt lgkmcnt(3)
v_mfma_f32_16x16x32_bf16 v[80:83], v[224:227], v[4:7], v[80:83]
.LchkoH1:
s_waitcnt vmcnt(0)
v_cmp_eq_u64 vcc, s[94:95], v[34:35]
s_mov_b32 s36, 0x0c0c0c0c
s_bitcmp1_b32 vcc_lo, 0
s_cselect_b32 s36, s40, s36
s_cselect_b32 s37, s41, 0
.LokoH1:
s_add_u32 s74, s84, 0
buffer_load_dwordx4 v[36:39], v31, s[48:51], s74 offen offset:0 sc1
buffer_load_dwordx4 v[40:43], v31, s[48:51], s74 offen offset:1024 sc1
v_mfma_f32_16x16x32_bf16 v[84:87], v[228:231], v[4:7], v[84:87]
ds_read_b128 v[4:7], v24 offset:5120
v_perm_b32 v1, v250, v250, s40
v_and_b32 v250, s41, v250
v_add_f32 v116, v116, v1
v_add_f32 v117, v117, v250
)ASM"
R"ASM(
v_mfma_f32_16x16x32_bf16 v[84:87], v[236:239], v[8:11], v[84:87]
v_perm_b32 v1, v251, v251, s40
v_and_b32 v251, s41, v251
v_add_f32 v118, v118, v1
v_add_f32 v119, v119, v251
v_mfma_f32_16x16x32_bf16 v[80:83], v[232:235], v[8:11], v[80:83]
ds_read_b128 v[8:11], v24 offset:3072
v_perm_b32 v1, v252, v252, s40
v_and_b32 v252, s41, v252
v_add_f32 v120, v120, v1
v_add_f32 v121, v121, v252
s_waitcnt lgkmcnt(3)
v_mfma_f32_16x16x32_bf16 v[96:99], v[224:227], v[12:15], v[96:99]
v_perm_b32 v1, v253, v253, s40
v_and_b32 v253, s41, v253
v_add_f32 v122, v122, v1
v_add_f32 v123, v123, v253
v_mfma_f32_16x16x32_bf16 v[100:103], v[228:231], v[12:15], v[100:103]
ds_read_b128 v[12:15], v24 offset:7168
ds_write_b128 v30, v[80:83] offset:32768
v_perm_b32 v1, v254, v254, s40
v_and_b32 v254, s41, v254
v_add_f32 v124, v124, v1
v_mfma_f32_16x16x32_bf16 v[100:103], v[236:239], v[16:19], v[100:103]
v_add_f32 v125, v125, v254
v_perm_b32 v1, v255, v255, s40
v_and_b32 v255, s41, v255
v_add_f32 v126, v126, v1
v_add_f32 v127, v127, v255
v_mfma_f32_16x16x32_bf16 v[96:99], v[232:235], v[16:19], v[96:99]
s_waitcnt lgkmcnt(3)
v_mfma_f32_16x16x32_bf16 v[88:91], v[224:227], v[20:23], v[88:91]
ds_write_b128 v30, v[84:87] offset:33792
v_mfma_f32_16x16x32_bf16 v[92:95], v[228:231], v[20:23], v[92:95]
v_mfma_f32_16x16x32_bf16 v[92:95], v[236:239], v[4:7], v[92:95]
s_nop 2
ds_write_b128 v30, v[96:99] offset:49152
v_mfma_f32_16x16x32_bf16 v[88:91], v[232:235], v[4:7], v[88:91]
s_waitcnt lgkmcnt(3)
v_mfma_f32_16x16x32_bf16 v[104:107], v[224:227], v[8:11], v[104:107]
ds_write_b128 v30, v[100:103] offset:50176
v_mfma_f32_16x16x32_bf16 v[108:111], v[228:231], v[8:11], v[108:111]
v_mfma_f32_16x16x32_bf16 v[108:111], v[236:239], v[12:15], v[108:111]
v_mfma_f32_16x16x32_bf16 v[104:107], v[232:235], v[12:15], v[104:107]
s_waitcnt lgkmcnt(0)
s_barrier
ds_read_b128 v[80:83], v30 offset:34816
ds_read_b128 v[84:87], v30 offset:35840
ds_read_b128 v[96:99], v30 offset:51200
ds_read_b128 v[100:103], v30 offset:52224
s_waitcnt lgkmcnt(3)
v_add_f32 v88, v88, v80
v_add_f32 v89, v89, v81
v_add_f32 v90, v90, v82
v_add_f32 v91, v91, v83
s_waitcnt lgkmcnt(2)
v_add_f32 v92, v92, v84
v_add_f32 v93, v93, v85
v_add_f32 v94, v94, v86
v_add_f32 v95, v95, v87
s_waitcnt lgkmcnt(1)
v_add_f32 v104, v104, v96
v_add_f32 v105, v105, v97
v_add_f32 v106, v106, v98
v_add_f32 v107, v107, v99
s_waitcnt lgkmcnt(0)
v_add_f32 v108, v108, v100
v_add_f32 v109, v109, v101
v_add_f32 v110, v110, v102
v_add_f32 v111, v111, v103
s_waitcnt vmcnt(1)
v_perm_b32 v1, v36, v36, s36
v_and_b32 v36, s37, v36
v_add_f32 v112, v112, v1
v_add_f32 v113, v113, v36
v_perm_b32 v1, v37, v37, s36
v_and_b32 v37, s37, v37
v_add_f32 v114, v114, v1
v_add_f32 v115, v115, v37
v_perm_b32 v1, v38, v38, s36
v_and_b32 v38, s37, v38
v_add_f32 v116, v116, v1
v_add_f32 v117, v117, v38
v_perm_b32 v1, v39, v39, s36
v_and_b32 v39, s37, v39
v_add_f32 v118, v118, v1
v_add_f32 v119, v119, v39
s_waitcnt vmcnt(0)
v_perm_b32 v1, v40, v40, s36
v_and_b32 v40, s37, v40
v_add_f32 v120, v120, v1
v_add_f32 v121, v121, v40
v_perm_b32 v1, v41, v41, s36
v_and_b32 v41, s37, v41
v_add_f32 v122, v122, v1
v_add_f32 v123, v123, v41
v_perm_b32 v1, v42, v42, s36
v_and_b32 v42, s37, v42
v_add_f32 v124, v124, v1
v_add_f32 v125, v125, v42
v_perm_b32 v1, v43, v43, s36
v_and_b32 v43, s37, v43
v_add_f32 v126, v126, v1
v_add_f32 v127, v127, v43
s_cmp_eq_u32 s39, 0
s_cbranch_scc1 .LslowH2
.LbackH2:
s_cmp_eq_u32 s41, 0
s_cbranch_scc1 .LslowH3
.LbackH3:
s_cmp_eq_u32 s37, 0
s_cbranch_scc1 .LslowH1
.LbackH1:
v_add_f32 v88, v88, v112
v_add_f32 v89, v89, v113
v_add_f32 v90, v90, v114
v_add_f32 v91, v91, v115
v_add_f32 v92, v92, v116
v_add_f32 v93, v93, v117
v_add_f32 v94, v94, v118
v_add_f32 v95, v95, v119
v_cvt_pk_bf16_f32 v88, v88, v89
v_cvt_pk_bf16_f32 v89, v90, v91
v_cvt_pk_bf16_f32 v90, v92, v93
v_cvt_pk_bf16_f32 v91, v94, v95
buffer_store_dwordx4 v[88:91], v25, s[52:55], s42 offen sc1
v_add_f32 v104, v104, v120
v_add_f32 v105, v105, v121
v_add_f32 v106, v106, v122
v_add_f32 v107, v107, v123
v_add_f32 v108, v108, v124
v_add_f32 v109, v109, v125
v_add_f32 v110, v110, v126
v_add_f32 v111, v111, v127
v_cvt_pk_bf16_f32 v104, v104, v105
v_cvt_pk_bf16_f32 v105, v106, v107
v_cvt_pk_bf16_f32 v106, v108, v109
v_cvt_pk_bf16_f32 v107, v110, v111
buffer_store_dwordx4 v[104:107], v25, s[52:55], s43 offen sc1
s_barrier
s_cmp_lg_u32 s56, 0
s_cbranch_scc1 .Lnoep
s_andn2_b32 s74, s81, 8191
s_mov_b64 exec, 1
buffer_store_dwordx2 v[2:3], off, s[48:51], s74 sc1
s_mov_b64 exec, -1
.Lnoep:
s_endpgm
.Lretry0:
s_sub_u32 s83, s83, 1
s_cbranch_scc1 .Lgive0
s_sleep 1
buffer_load_dwordx2 v[34:35], off, s[48:51], s66 offset:128 sc1
s_waitcnt vmcnt(0)
s_branch .Lchk0
.Lgive0:
s_mov_b32 s83, 0
s_branch .Lok0
.Lretry1:
s_sub_u32 s83, s83, 1
s_cbranch_scc1 .Lgive1
s_sleep 1
buffer_load_dwordx2 v[34:35], off, s[48:51], s66 offset:256 sc1
s_waitcnt vmcnt(0)
s_branch .Lchk1
.Lgive1:
s_mov_b32 s83, 0
s_branch .Lok1
.Lretry2:
s_sub_u32 s83, s83, 1
s_cbranch_scc1 .Lgive2
s_sleep 1
buffer_load_dwordx2 v[34:35], off, s[48:51], s66 offset:384 sc1
s_waitcnt vmcnt(0)
s_branch .Lchk2
.Lgive2:
s_mov_b32 s83, 0
s_branch .Lok2
.Lretry3:
s_sub_u32 s83, s83, 1
s_cbranch_scc1 .Lgive3
s_sleep 1
buffer_load_dwordx2 v[34:35], off, s[48:51], s67 offset:384 sc1
s_waitcnt vmcnt(0)
s_branch .Lchk3
.Lgive3:
s_mov_b32 s83, 0
s_branch .Lok3
.Lretry4:
s_sub_u32 s83, s83, 1
s_cbranch_scc1 .Lgive4
s_sleep 1
buffer_load_dwordx2 v[34:35], off, s[48:51], s66 offset:512 sc1
s_waitcnt vmcnt(0)
s_branch .Lchk4
.Lgive4:
s_mov_b32 s83, 0
s_branch .Lok4
.Lretry5:
s_sub_u32 s83, s83, 1
)ASM"
R"ASM(
s_cbranch_scc1 .Lgive5
s_sleep 1
buffer_load_dwordx2 v[34:35], off, s[48:51], s66 offset:640 sc1
s_waitcnt vmcnt(0)
s_branch .Lchk5
.Lgive5:
s_mov_b32 s83, 0
s_branch .Lok5
.Lretry6:
s_sub_u32 s83, s83, 1
s_cbranch_scc1 .Lgive6
s_sleep 1
buffer_load_dwordx2 v[34:35], off, s[48:51], s67 offset:640 sc1
s_waitcnt vmcnt(0)
s_branch .Lchk6
.Lgive6:
s_mov_b32 s83, 0
s_branch .Lok6
.LslowH2:
buffer_load_dwordx2 v[26:27], off, s[48:51], s89 offset:768 sc1
s_waitcnt vmcnt(0)
v_cmp_eq_u64 vcc, s[96:97], v[26:27]
s_cmp_eq_u64 vcc, exec
s_cbranch_scc1 .LsgH2
s_sub_u32 s83, s83, 1
s_cbranch_scc0 .LslowH2
s_mov_b32 s83, 0
.LsgH2:
s_mov_b32 s38, 0x01000c0c
s_mov_b32 s39, 0xffff0000
s_add_u32 s74, s84, 16384
buffer_load_dwordx4 v[240:243], v31, s[48:51], s74 offen offset:0 sc1
buffer_load_dwordx4 v[244:247], v31, s[48:51], s74 offen offset:1024 sc1
s_waitcnt vmcnt(0)
v_perm_b32 v1, v240, v240, s38
v_and_b32 v240, s39, v240
v_add_f32 v112, v112, v1
v_add_f32 v113, v113, v240
v_perm_b32 v1, v241, v241, s38
v_and_b32 v241, s39, v241
v_add_f32 v114, v114, v1
v_add_f32 v115, v115, v241
v_perm_b32 v1, v242, v242, s38
v_and_b32 v242, s39, v242
v_add_f32 v116, v116, v1
v_add_f32 v117, v117, v242
v_perm_b32 v1, v243, v243, s38
v_and_b32 v243, s39, v243
v_add_f32 v118, v118, v1
v_add_f32 v119, v119, v243
v_perm_b32 v1, v244, v244, s38
v_and_b32 v244, s39, v244
v_add_f32 v120, v120, v1
v_add_f32 v121, v121, v244
v_perm_b32 v1, v245, v245, s38
v_and_b32 v245, s39, v245
v_add_f32 v122, v122, v1
v_add_f32 v123, v123, v245
v_perm_b32 v1, v246, v246, s38
v_and_b32 v246, s39, v246
v_add_f32 v124, v124, v1
v_add_f32 v125, v125, v246
v_perm_b32 v1, v247, v247, s38
v_and_b32 v247, s39, v247
v_add_f32 v126, v126, v1
v_add_f32 v127, v127, v247
s_branch .LbackH2
.LslowH3:
buffer_load_dwordx2 v[28:29], off, s[48:51], s90 offset:768 sc1
s_waitcnt vmcnt(0)
v_cmp_eq_u64 vcc, s[98:99], v[28:29]
s_cmp_eq_u64 vcc, exec
s_cbranch_scc1 .LsgH3
s_sub_u32 s83, s83, 1
s_cbranch_scc0 .LslowH3
s_mov_b32 s83, 0
.LsgH3:
s_mov_b32 s40, 0x01000c0c
s_mov_b32 s41, 0xffff0000
s_add_u32 s74, s84, 32768
buffer_load_dwordx4 v[248:251], v31, s[48:51], s74 offen offset:0 sc1
buffer_load_dwordx4 v[252:255], v31, s[48:51], s74 offen offset:1024 sc1
s_waitcnt vmcnt(0)
v_perm_b32 v1, v248, v248, s40
v_and_b32 v248, s41, v248
v_add_f32 v112, v112, v1
v_add_f32 v113, v113, v248
v_perm_b32 v1, v249, v249, s40
v_and_b32 v249, s41, v249
v_add_f32 v114, v114, v1
v_add_f32 v115, v115, v249
v_perm_b32 v1, v250, v250, s40
v_and_b32 v250, s41, v250
v_add_f32 v116, v116, v1
v_add_f32 v117, v117, v250
v_perm_b32 v1, v251, v251, s40
v_and_b32 v251, s41, v251
v_add_f32 v118, v118, v1
v_add_f32 v119, v119, v251
v_perm_b32 v1, v252, v252, s40
v_and_b32 v252, s41, v252
v_add_f32 v120, v120, v1
v_add_f32 v121, v121, v252
v_perm_b32 v1, v253, v253, s40
v_and_b32 v253, s41, v253
v_add_f32 v122, v122, v1
v_add_f32 v123, v123, v253
v_perm_b32 v1, v254, v254, s40
v_and_b32 v254, s41, v254
v_add_f32 v124, v124, v1
v_add_f32 v125, v125, v254
v_perm_b32 v1, v255, v255, s40
v_and_b32 v255, s41, v255
v_add_f32 v126, v126, v1
v_add_f32 v127, v127, v255
s_branch .LbackH3
.LslowH1:
buffer_load_dwordx2 v[34:35], off, s[48:51], s88 offset:896 sc1
s_waitcnt vmcnt(0)
v_cmp_eq_u64 vcc, s[94:95], v[34:35]
s_cmp_eq_u64 vcc, exec
s_cbranch_scc1 .LsgH1
s_sub_u32 s83, s83, 1
s_cbranch_scc0 .LslowH1
s_mov_b32 s83, 0
.LsgH1:
s_mov_b32 s36, 0x01000c0c
s_mov_b32 s37, 0xffff0000
s_add_u32 s74, s84, 0
buffer_load_dwordx4 v[36:39], v31, s[48:51], s74 offen offset:0 sc1
buffer_load_dwordx4 v[40:43], v31, s[48:51], s74 offen offset:1024 sc1
s_waitcnt vmcnt(0)
v_perm_b32 v1, v36, v36, s36
v_and_b32 v36, s37, v36
v_add_f32 v112, v112, v1
v_add_f32 v113, v113, v36
v_perm_b32 v1, v37, v37, s36
v_and_b32 v37, s37, v37
v_add_f32 v114, v114, v1
v_add_f32 v115, v115, v37
v_perm_b32 v1, v38, v38, s36
v_and_b32 v38, s37, v38
v_add_f32 v116, v116, v1
v_add_f32 v117, v117, v38
v_perm_b32 v1, v39, v39, s36
v_and_b32 v39, s37, v39
v_add_f32 v118, v118, v1
v_add_f32 v119, v119, v39
v_perm_b32 v1, v40, v40, s36
v_and_b32 v40, s37, v40
v_add_f32 v120, v120, v1
v_add_f32 v121, v121, v40
v_perm_b32 v1, v41, v41, s36
v_and_b32 v41, s37, v41
v_add_f32 v122, v122, v1
v_add_f32 v123, v123, v41
v_perm_b32 v1, v42, v42, s36
v_and_b32 v42, s37, v42
v_add_f32 v124, v124, v1
v_add_f32 v125, v125, v42
v_perm_b32 v1, v43, v43, s36
v_and_b32 v43, s37, v43
v_add_f32 v126, v126, v1
v_add_f32 v127, v127, v43
s_branch .LbackH1
.Lcode_end:
)ASM"
  : "+v"(tid)
  : "s"(__builtin_amdgcn_kernarg_segment_ptr()), "s"(blockIdx.x), "s"((unsigned)(size_t)smem)
  : "v1", "v2", "v3", "v4", "v5", "v6", "v7", "v8", "v9", "v10", "v11", "v12", "v13", "v14", "v15", "v16", "v17", "v18", "v19", "v20", "v21", "v22", "v23", "v24", "v25", "v26", "v27", "v28", "v29", "v30", "v31", "v32", "v33", "v34", "v35", "v36", "v37", "v38", "v39", "v40", "v41", "v42", "v43", "v44", "v45", "v46", "v47", "v48", "v49", "v50", "v51", "v52", "v53", "v54", "v55", "v56", "v57", "v58", "v59", "v60", "v61", "v62", "v63", "v64", "v65", "v66", "v67", "v68", "v69", "v70", "v71", "v72", "v73", "v74", "v75", "v76", "v77", "v78", "v79", "v80", "v81", "v82", "v83", "v84", "v85", "v86", "v87", "v88", "v89", "v90", "v91", "v92", "v93", "v94", "v95", "v96", "v97", "v98", "v99", "v100", "v101", "v102", "v103", "v104", "v105", "v106", "v107", "v108", "v109", "v110", "v111", "v112", "v113", "v114", "v115", "v116", "v117", "v118", "v119", "v120", "v121", "v122", "v123", "v124", "v125", "v126", "v127", "v128", "v129", "v130", "v131", "v132", "v133", "v134", "v135", "v136", "v137", "v138", "v139", "v140", "v141", "v142", "v143", "v144", "v145", "v146", "v147", "v148", "v149", "v150", "v151", "v152", "v153", "v154", "v155", "v156", "v157", "v158", "v159", "v160", "v161", "v162", "v163", "v164", "v165", "v166", "v167", "v168", "v169", "v170", "v171", "v172", "v173", "v174", "v175", "v176", "v177", "v178", "v179", "v180", "v181", "v182", "v183", "v184", "v185", "v186", "v187", "v188", "v189", "v190", "v191", "v192", "v193", "v194", "v195", "v196", "v197", "v198", "v199", "v200", "v201", "v202", "v203", "v204", "v205", "v206", "v207", "v208", "v209", "v210", "v211", "v212", "v213", "v214", "v215", "v216", "v217", "v218", "v219", "v220", "v221", "v222", "v223", "v224", "v225", "v226", "v227", "v228", "v229", "v230", "v231", "v232", "v233", "v234", "v235", "v236", "v237", "v238", "v239", "v240", "v241", "v242", "v243", "v244", "v245", "v246", "v247", "v248", "v249", "v250", "v251", "v252", "v253", "v254", "v255", "s5", "s6", "s7", "s8", "s9", "s10", "s11", "s12", "s13", "s14", "s15", "s16", "s17", "s18", "s19", "s20", "s21", "s22", "s23", "s24", "s25", "s26", "s27", "s28", "s29", "s30", "s31", "s36", "s37", "s38", "s39", "s40", "s41", "s42", "s43", "s44", "s45", "s46", "s47", "s48", "s49", "s50", "s51", "s52", "s53", "s54", "s55", "s56", "s57", "s58", "s59", "s60", "s61", "s62", "s63", "s64", "s65", "s66", "s67", "s68", "s69", "s70", "s71", "s72", "s73", "s74", "s75", "s76", "s77", "s78", "s79", "s80", "s81", "s82", "s83", "s84", "s85", "s86", "s87", "s88", "s89", "s90", "s91", "s92", "s93", "s94", "s95", "s96", "s97", "s98", "s99", "vcc", "scc", "memory");
}

extern "C" void kernel_launch(void* const* d_in, const int* in_sizes, int n_in, void* d_out, int out_size, void* d_ws, size_t ws_size, hipStream_t stream) {
  fused_mlp<<<256, 512, 0, stream>>>(d_in[0], d_in[1], d_in[2], d_in[3], d_in[4], d_out, (char*)d_ws + ((size_t)0 << 20), d_ws);
}
```
